```python
import torch

def _softmax(x, axis=-1):
    return torch.softmax(x, dim=axis)
def _gelu(x):
    return torch.nn.functional.gelu(x, approximate="tanh")
def _silu(x):
    return torch.nn.functional.silu(x)
def _sigmoid(x):
    return torch.sigmoid(x)
def _layernorm(x, axis=-1, eps=1e-5):
    m = x.mean(dim=axis, keepdim=True)
    v = x.var(dim=axis, keepdim=True, unbiased=False)
    return (x - m) / torch.sqrt(v + eps)
def _scatter_add(tgt, idx, src, axis=0):
    return tgt.clone().index_add_(axis, idx, src)
def _segment_sum(src, seg, n):
    out = torch.zeros((n,) + src.shape[1:], dtype=src.dtype)
    return out.index_add_(0, seg, src)
def _pad(x, pw):
    flat = tuple(v for pair in reversed(pw) for v in pair)
    return torch.nn.functional.pad(x, flat)

kernel_name = 'concat_heads__mha_block__linear_kernel_hedgehog__prompt'

def setup_inputs():

    _w = (lambda a: (lambda t: t.to(torch.bfloat16) if t.is_floating_point() else t)(torch.as_tensor(a)))
    def _A_setup():
        B, S, D, H = 1, 2048, 448, 16
        return {
            "x": _w((lambda *s: torch.randn(*s, dtype=torch.bfloat16) * 0.4)(B, S, D)),
            "wqkv": _w((lambda *s: torch.randn(*s, dtype=torch.bfloat16) * 0.4)(D, 3 * D)),
            "wo": _w((lambda *s: torch.randn(*s, dtype=torch.bfloat16) * 0.05)(D, D)),
        }
    _A_in = _A_setup()

    def _B_setup():
        B, S, D, H = 1, 2048, 448, 16
        Dh = D // H
        return {
            "x": _w((lambda *s: torch.randn(*s, dtype=torch.bfloat16) * 0.8)(B, S, D)),
            "wqkv": _w((lambda *s: torch.randn(*s, dtype=torch.bfloat16) * 1.6)(D, 3 * D)),
            "w_phi": _w((lambda *s: torch.randn(*s, dtype=torch.bfloat16) * 1.6)(H, Dh, Dh) * (Dh ** -0.5)),
            "wo": _w((lambda *s: torch.randn(*s, dtype=torch.bfloat16) * 0.1)(D, D)),
        }
    _B_in = _B_setup()

    return {
        "A_x": _A_in["x"],
        "A_wqkv": _A_in["wqkv"],
        "A_wo": _A_in["wo"],
        "B_x": _B_in["x"],
        "B_wqkv": _B_in["wqkv"],
        "B_w_phi": _B_in["w_phi"],
        "B_wo": _B_in["wo"],
    }

def reference(A_x, A_wqkv, A_wo, B_x, B_wqkv, B_w_phi, B_wo):
    def _A_ref(x, wqkv, wo):
        B, S, D = x.shape
        H = 16
        Dh = D // H
        qkv = (lambda x, *d: x.permute(*d))((x @ wqkv).reshape(B, S, 3, H, Dh), 2, 0, 3, 1, 4)
        q, k, v = qkv[0], qkv[1], qkv[2]
        scores = torch.einsum("bhqd,bhkd->bhqk", q, k) * (Dh ** -0.5)
        p = _softmax(scores, axis=-1)
        o = torch.einsum("bhqk,bhkd->bhqd", p, v)
        return (lambda x, *d: x.permute(*d))(o, 0, 2, 1, 3).reshape(B, S, D) @ wo

    def _B_ref(x, wqkv, w_phi, wo):
        B, S, D = x.shape
        H = 16; Dh = D // H
        qkv = (lambda x, *d: x.permute(*d))((x @ wqkv).reshape(B, S, 3, H, Dh), 2, 0, 3, 1, 4)
        q, k, v = qkv[0], qkv[1], qkv[2]
        pq = torch.einsum("bhsd,hdf->bhsf", q, w_phi)
        pk = torch.einsum("bhsd,hdf->bhsf", k, w_phi)
        phi_q = torch.concatenate([_softmax(pq, axis=-1), _softmax(-pq, axis=-1)], axis=-1)
        phi_k = torch.concatenate([_softmax(pk, axis=-1), _softmax(-pk, axis=-1)], axis=-1)
        kv = torch.einsum("bhsf,bhsd->bhfd", phi_k, v)
        z  = torch.einsum("bhsf->bhf", phi_k)
        num = torch.einsum("bhqf,bhfd->bhqd", phi_q, kv)
        den = torch.einsum("bhqf,bhf->bhq", phi_q, z)[..., None] + 1e-6
        o = num / den
        return (lambda x, *d: x.permute(*d))(o, 0, 2, 1, 3).reshape(B, S, D) @ wo

    a = _A_ref(A_x, A_wqkv, A_wo)
    b = _B_ref(B_x, B_wqkv, B_w_phi, B_wo)
    return torch.concatenate([a, b], axis=-1)
```

```cpp
#include <hip/hip_runtime.h>
#include <hip/hip_bf16.h>
typedef __hip_bfloat16 bf16;
constexpr int S = 2048, D = 448, H = 16, DH = 28;
__device__ inline float b2f(bf16 v) { return __bfloat162float(v); }
__device__ inline float rb(float v) { return __bfloat162float(__float2bfloat16(v)); }

__global__ void gemm_naive(const bf16* x, const bf16* w, float* out, int N) {
    int n = blockIdx.x * 64 + threadIdx.x, m = blockIdx.y;
    if (n >= N) return;
    float acc = 0.f;
    for (int k = 0; k < D; ++k) acc += b2f(x[m * D + k]) * b2f(w[k * N + n]);
    out[m * N + n] = rb(acc);
}
__global__ void attn_naive(const float* qkv, float* o) {
    int q = blockIdx.x * 64 + threadIdx.x, h = blockIdx.y;
    float qv[DH], acc[DH];
    for (int d = 0; d < DH; ++d) { qv[d] = qkv[q * 3 * D + h * DH + d] * 0.18898223650461362f; acc[d] = 0.f; }
    float m = -1e30f, l = 0.f;
    for (int k = 0; k < S; ++k) {
        float s = 0.f;
        for (int d = 0; d < DH; ++d) s += qv[d] * qkv[k * 3 * D + D + h * DH + d];
        float mn = fmaxf(m, s), a = __expf(m - mn), p = __expf(s - mn);
        l = l * a + p;
        for (int d = 0; d < DH; ++d) acc[d] = acc[d] * a + p * qkv[k * 3 * D + 2 * D + h * DH + d];
        m = mn;
    }
    for (int d = 0; d < DH; ++d) o[q * D + h * DH + d] = rb(acc[d] / l);
}
__global__ void phi_naive(const float* qkv, const bf16* wphi, float* phiq, float* phik) {
    int s = blockIdx.x * 64 + threadIdx.x, h = blockIdx.y, which = blockIdx.z;
    float p[DH];
    for (int f = 0; f < DH; ++f) { float a = 0.f; for (int d = 0; d < DH; ++d) a += qkv[s * 3 * D + which * D + h * DH + d] * b2f(wphi[(h * DH + d) * DH + f]); p[f] = a; }
    float mx = -1e30f, mn = 1e30f;
    for (int f = 0; f < DH; ++f) { mx = fmaxf(mx, p[f]); mn = fminf(mn, p[f]); }
    float s1 = 0.f, s2 = 0.f;
    for (int f = 0; f < DH; ++f) { s1 += __expf(p[f] - mx); s2 += __expf(mn - p[f]); }
    float* dst = (which ? phik : phiq) + (size_t)(h * S + s) * 56;
    for (int f = 0; f < DH; ++f) { dst[f] = __expf(p[f] - mx) / s1; dst[28 + f] = __expf(mn - p[f]) / s2; }
}
__global__ void kv_naive(const float* qkv, const float* phik, float* kv) {
    int f = blockIdx.x, h = blockIdx.y, d = threadIdx.x;
    if (d > DH) return;
    float a = 0.f;
    for (int s = 0; s < S; ++s) a += phik[(size_t)(h * S + s) * 56 + f] * (d < DH ? qkv[s * 3 * D + 2 * D + h * DH + d] : 1.f);
    kv[(h * 56 + f) * 32 + d] = a;
}
__global__ void lin_naive(const float* phiq, const float* kv, float* o) {
    int s = blockIdx.x * 64 + threadIdx.x, h = blockIdx.y;
    float den = 0.f;
    for (int f = 0; f < 56; ++f) den += phiq[(size_t)(h * S + s) * 56 + f] * kv[(h * 56 + f) * 32 + 28];
    den += 1e-6f;
    for (int d = 0; d < DH; ++d) { float a = 0.f; for (int f = 0; f < 56; ++f) a += phiq[(size_t)(h * S + s) * 56 + f] * kv[(h * 56 + f) * 32 + d]; o[s * D + h * DH + d] = rb(a / den); }
}
__global__ void out_naive(const float* o, const bf16* wo, bf16* out, int off) {
    int n = blockIdx.x * 64 + threadIdx.x, m = blockIdx.y;
    float acc = 0.f;
    for (int k = 0; k < D; ++k) acc += o[m * D + k] * b2f(wo[k * D + n]);
    out[m * 2 * D + off + n] = __float2bfloat16(acc);
}
extern "C" void kernel_launch(void* const* d_in, const int* in_sizes, int n_in, void* d_out, int out_size, void* d_ws, size_t ws_size, hipStream_t stream) {
    const bf16* Ax = (const bf16*)d_in[0]; const bf16* Aw = (const bf16*)d_in[1]; const bf16* Awo = (const bf16*)d_in[2];
    const bf16* Bx = (const bf16*)d_in[3]; const bf16* Bw = (const bf16*)d_in[4]; const bf16* Bphi = (const bf16*)d_in[5]; const bf16* Bwo = (const bf16*)d_in[6];
    float* ws = (float*)d_ws;
    float* qkvA = ws; float* qkvB = qkvA + S * 3 * D; float* oA = qkvB + S * 3 * D; float* oB = oA + S * D;
    float* phiq = oB + S * D; float* phik = phiq + H * S * 56; float* kv = phik + H * S * 56;
    gemm_naive<<<dim3(21, S), 64, 0, stream>>>(Ax, Aw, qkvA, 3 * D);
    gemm_naive<<<dim3(21, S), 64, 0, stream>>>(Bx, Bw, qkvB, 3 * D);
    attn_naive<<<dim3(S / 64, H), 64, 0, stream>>>(qkvA, oA);
    phi_naive<<<dim3(S / 64, H, 2), 64, 0, stream>>>(qkvB, Bphi, phiq, phik);
    kv_naive<<<dim3(56, H), 64, 0, stream>>>(qkvB, phik, kv);
    lin_naive<<<dim3(S / 64, H), 64, 0, stream>>>(phiq, kv, oB);
    out_naive<<<dim3(7, S), 64, 0, stream>>>(oA, Awo, (bf16*)d_out, 0);
    out_naive<<<dim3(7, S), 64, 0, stream>>>(oB, Bwo, (bf16*)d_out, D);
}
```

```cpp
#include <hip/hip_runtime.h>
#include <stdint.h>

namespace {

typedef unsigned short u16;
typedef __attribute__((ext_vector_type(16))) float f32x16;
typedef __attribute__((ext_vector_type(4))) float f32x4;
typedef __attribute__((ext_vector_type(2))) float f32x2;
typedef __attribute__((ext_vector_type(8))) __bf16 bf16x8;
typedef __attribute__((ext_vector_type(2))) __bf16 bf16x2;
typedef __attribute__((ext_vector_type(4))) unsigned u32x4;
typedef __attribute__((ext_vector_type(2))) unsigned u32x2;
typedef __attribute__((ext_vector_type(4))) short s16x4;
typedef __amdgpu_buffer_rsrc_t rsrc_t;
#define LAS __attribute__((address_space(3)))
#define DI __device__ __forceinline__
#define SBAR() __builtin_amdgcn_sched_barrier(0)

constexpr int SEQ = 2048, DM = 448, NH = 16;
constexpr unsigned XROW = DM * 2;
constexpr unsigned WROW = 3 * DM * 2;
constexpr unsigned X_BYTES = SEQ * XROW, W_BYTES = DM * WROW, WO_BYTES = DM * XROW;
constexpr size_t WS_KVA = 65536;
constexpr size_t WS_OA = WS_KVA + (4u << 20);
constexpr size_t WS_OB = WS_OA + (2u << 20);
constexpr size_t WS_KVP = WS_OB + (2u << 20);
constexpr size_t WS_PHIQ = WS_KVP + (2u << 20);
constexpr int LDS_BYTES = 80 * 1024;
constexpr float LOG2E = 1.4426950408889634f;
constexpr float QSCALE = 0.18898223650461362f * LOG2E;
constexpr float REF_OFFSET = 32.f;
constexpr float L_LIMIT = 18446744073709551616.f;
constexpr int SC1 = 16;

DI f32x16 mfma(u32x4 a, u32x4 b, f32x16 c) {
    return __builtin_amdgcn_mfma_f32_32x32x16_bf16(__builtin_bit_cast(bf16x8, a), __builtin_bit_cast(bf16x8, b), c, 0, 0, 0);
}
DI unsigned pk(float lo, float hi) { f32x2 v = {lo, hi}; return __builtin_bit_cast(unsigned, __builtin_convertvector(v, bf16x2)); }
DI u32x4 pack_lo(const f32x16& x) { return u32x4{pk(x[0], x[1]), pk(x[2], x[3]), pk(x[4], x[5]), pk(x[6], x[7])}; }
DI u32x4 pack_hi(const f32x16& x) { return u32x4{pk(x[8], x[9]), pk(x[10], x[11]), pk(x[12], x[13]), pk(x[14], x[15])}; }
DI rsrc_t make_rsrc(const void* p, unsigned bytes) { return __builtin_amdgcn_make_buffer_rsrc((void*)p, 0, bytes, 0x00020000); }
DI void dma16(rsrc_t rs, unsigned lds_addr, unsigned voff, unsigned soff) {
    asm volatile("s_mov_b32 m0, %0\n\ts_nop 0\n\tbuffer_load_dwordx4 %1, %2, %3 offen lds" ::"s"(lds_addr), "v"(voff), "s"(rs), "s"(soff) : "memory");
}
template <int N> DI void wait_bar() { asm volatile("s_waitcnt vmcnt(%0) lgkmcnt(0)\n\ts_barrier" ::"n"(N) : "memory"); }
DI void lds_bar() { asm volatile("s_waitcnt lgkmcnt(0)\n\ts_barrier" ::: "memory"); }
DI u32x4 lds128(unsigned a) { return *(const LAS u32x4*)(size_t)a; }
DI void sts128(unsigned a, u32x4 v) { *(LAS u32x4*)(size_t)a = v; }
DI void sts64(unsigned a, u32x2 v) { *(LAS u32x2*)(size_t)a = v; }
DI u32x2 tr64(unsigned a) { return __builtin_bit_cast(u32x2, __builtin_amdgcn_ds_read_tr16_b64_v4i16((LAS s16x4*)(size_t)a)); }
DI u32x4 tr_frag(unsigned a0, unsigned a1) { u32x2 lo = tr64(a0), hi = tr64(a1); return u32x4{lo[0], lo[1], hi[0], hi[1]}; }
DI float other_half(float v) {
    auto r = __builtin_amdgcn_permlane32_swap(__float_as_uint(v), __float_as_uint(v), false, false);
    return __uint_as_float((threadIdx.x & 32) ? r[0] : r[1]);
}
DI float both_max(float v) { auto r = __builtin_amdgcn_permlane32_swap(__float_as_uint(v), __float_as_uint(v), false, false); return fmaxf(__uint_as_float(r[0]), __uint_as_float(r[1])); }
DI float both_min(float v) { auto r = __builtin_amdgcn_permlane32_swap(__float_as_uint(v), __float_as_uint(v), false, false); return fminf(__uint_as_float(r[0]), __uint_as_float(r[1])); }
DI float both_sum(float v) { auto r = __builtin_amdgcn_permlane32_swap(__float_as_uint(v), __float_as_uint(v), false, false); return __uint_as_float(r[0]) + __uint_as_float(r[1]); }
DI float upper_val(float v) { auto r = __builtin_amdgcn_permlane32_swap(__float_as_uint(v), __float_as_uint(v), false, false); return __uint_as_float(r[1]); }
DI float max16(const f32x16& s) {
    float a = fmaxf(fmaxf(s[0], s[1]), s[2]), b = fmaxf(fmaxf(s[3], s[4]), s[5]);
    a = fmaxf(fmaxf(a, s[6]), s[7]); b = fmaxf(fmaxf(b, s[8]), s[9]);
    a = fmaxf(fmaxf(a, s[10]), s[11]); b = fmaxf(fmaxf(b, s[12]), s[13]);
    a = fmaxf(fmaxf(a, s[14]), s[15]);
    return fmaxf(a, b);
}
DI float min16(const f32x16& s) {
    float a = fminf(fminf(s[0], s[1]), s[2]), b = fminf(fminf(s[3], s[4]), s[5]);
    a = fminf(fminf(a, s[6]), s[7]); b = fminf(fminf(b, s[8]), s[9]);
    a = fminf(fminf(a, s[10]), s[11]); b = fminf(fminf(b, s[12]), s[13]);
    a = fminf(fminf(a, s[14]), s[15]);
    return fminf(a, b);
}

struct Ctx {
    int lane, wid, hi, r32, h, rb;
    unsigned lds;
};

DI void x_frag_offsets(const Ctx& c, unsigned (&xo)[4]) {
#pragma unroll
    for (int s = 0; s < 4; ++s) xo[s] = c.r32 * 128 + (((2 * s + c.hi) ^ ((c.r32 >> 1) & 7)) << 4);
}
DI unsigned x_src(const Ctx& c, int odd) { return (c.lane >> 3) * XROW + (((c.lane & 7) ^ (4 * odd + (c.lane >> 4))) << 4); }

template <bool ISV> DI void a1_body(const Ctx& c, const u16* x, const u16* w, char* ws) {
    constexpr unsigned SLOT = 24576, WOFF = 8192;
    const int half = c.h & 1, hg = c.h >> 2;
    const int row0 = c.rb * 128 + half * 64;
    const rsrc_t rx = make_rsrc(x, X_BYTES), rw = make_rsrc(w, W_BYTES);
    const unsigned vx0 = (row0 + 16 * c.wid) * XROW + x_src(c, 0), vx1 = (row0 + 16 * c.wid + 8) * XROW + x_src(c, 1);
    const unsigned vw = (16 * c.wid + (c.lane >> 4)) * WROW + (ISV ? 2 : 1) * XROW + hg * 224 + (((c.lane & 15) ^ ((c.lane >> 4) << 2)) << 4);
    auto issue = [&](int ch) {
        const unsigned base = c.lds + (ch % 3) * SLOT;
        dma16(rx, base + c.wid * 2048, vx0, ch * 128);
        dma16(rx, base + c.wid * 2048 + 1024, vx1, ch * 128);
#pragma unroll
        for (int j = 0; j < 4; ++j) dma16(rw, base + WOFF + c.wid * 4096 + j * 1024, vw, (64 * ch + 4 * j) * WROW);
    };
    issue(0); issue(1); issue(2);
    unsigned xo[4]; x_frag_offsets(c, xo);
    const int q = (c.lane & 15) >> 2;
    const unsigned wo = (8 * c.hi + q) * 256 + ((56 * c.wid + 32 * ((c.lane >> 4) & 1) + 8 * (c.lane & 3)) ^ (q << 6));
    f32x16 acc0 = {}, acc1 = {};
#pragma unroll
    for (int ch = 0; ch < 7; ++ch) {
        if (ch == 0) wait_bar<12>(); else if (ch < 6) wait_bar<6>(); else wait_bar<0>();
        if (ch >= 1 && ch + 2 <= 6) issue(ch + 2);
        const unsigned base = c.lds + (ch % 3) * SLOT;
#pragma unroll
        for (int s = 0; s < 4; ++s) {
            const u32x4 wf = tr_frag(base + WOFF + wo + s * 4096, base + WOFF + wo + s * 4096 + 1024);
            const u32x4 x0 = lds128(base + xo[s]), x1 = lds128(base + xo[s] + 4096);
            if (ISV) { acc0 = mfma(x0, wf, acc0); acc1 = mfma(x1, wf, acc1); }
            else     { acc0 = mfma(wf, x0, acc0); acc1 = mfma(wf, x1, acc1); }
        }
    }
    u32x4 f[4] = {pack_lo(acc0), pack_hi(acc0), pack_lo(acc1), pack_hi(acc1)};
    if (ISV) {
        if (c.r32 >= 28) {
            const unsigned v = c.r32 == 28 ? 0x3F803F80u : 0u;
#pragma unroll
            for (int i = 0; i < 4; ++i) f[i] = u32x4{v, v, v, v};
        }
    } else if (c.hi) {
        f[1][2] = 0x3F803F80u; f[1][3] = 0x00003F80u; f[3][2] = 0x3F803F80u; f[3][3] = 0x00003F80u;
    }
    const int head = hg * 4 + c.wid, kt = c.rb * 4 + half * 2;
    const rsrc_t ro = make_rsrc(ws + WS_KVA + (size_t)(head * 64 + kt) * 4096, 8192);
    const unsigned vo = c.lane * 16 + (ISV ? 2048 : 0);
    __builtin_amdgcn_raw_buffer_store_b128(f[0], ro, vo, 0, SC1);
    __builtin_amdgcn_raw_buffer_store_b128(f[1], ro, vo + 1024, 0, SC1);
    __builtin_amdgcn_raw_buffer_store_b128(f[2], ro, vo, 4096, SC1);
    __builtin_amdgcn_raw_buffer_store_b128(f[3], ro, vo + 1024, 4096, SC1);
}
DI void phase_a1(const Ctx& c, const u16* x, const u16* w, char* ws) {
    if ((c.h >> 1) & 1) a1_body<true>(c, x, w, ws); else a1_body<false>(c, x, w, ws);
}

struct KV { u32x4 k0, k1, v0, v1; };

DI void set_reference(u32x4& q1, float nm, int hi) {
    const unsigned a = __float_as_uint(nm) & 0xFFFF0000u;
    const float r1 = nm - __uint_as_float(a);
    const unsigned b = __float_as_uint(r1) & 0xFFFF0000u;
    const float r2 = r1 - __uint_as_float(b);
    if (hi) { q1[2] = (a >> 16) | b; q1[3] = __float_as_uint(r2) >> 16; }
}

DI void phase_a2(const Ctx& c, const u16* x, const u16* w, char* ws) {
    constexpr unsigned SLOT = 20480, WOFF = 16384;
    u32x4 q[4][2];
    {
        const rsrc_t rx = make_rsrc(x, X_BYTES), rw = make_rsrc(w, W_BYTES);
        const unsigned xrow = (c.rb * 128 + 32 * c.wid) * XROW;
        const unsigned vx0 = xrow + x_src(c, 0), vx1 = xrow + 8 * XROW + x_src(c, 1);
        const unsigned vw = (16 * c.wid + (c.lane >> 2)) * WROW + c.h * 56 + (c.lane & 3) * 16;
        auto issue = [&](int ch) {
            const unsigned base = c.lds + (ch & 3) * SLOT;
            dma16(rx, base + c.wid * 4096, vx0, ch * 128);
            dma16(rx, base + c.wid * 4096 + 1024, vx1, ch * 128);
            dma16(rx, base + c.wid * 4096 + 2048, vx0, ch * 128 + 16 * XROW);
            dma16(rx, base + c.wid * 4096 + 3072, vx1, ch * 128 + 16 * XROW);
            dma16(rw, base + WOFF + c.wid * 1024, vw, 64 * ch * WROW);
        };
        issue(0); issue(1); issue(2); issue(3);
        unsigned xo[4]; x_frag_offsets(c, xo);
        const unsigned wo = (8 * c.hi + ((c.lane & 15) >> 2)) * 64 + 32 * ((c.lane >> 4) & 1) + 8 * (c.lane & 3);
        f32x16 acc = {};
#pragma unroll
        for (int ch = 0; ch < 7; ++ch) {
            if (ch == 0) wait_bar<15>(); else if (ch < 5) wait_bar<10>(); else if (ch == 5) wait_bar<5>(); else wait_bar<0>();
            if (ch >= 1 && ch + 3 <= 6) issue(ch + 3);
            const unsigned base = c.lds + (ch & 3) * SLOT;
#pragma unroll
            for (int s = 0; s < 4; ++s) {
                const u32x4 wf = tr_frag(base + WOFF + wo + s * 1024, base + WOFF + wo + s * 1024 + 256);
                acc = mfma(wf, lds128(base + c.wid * 4096 + xo[s]), acc);
            }
        }
        acc *= QSCALE;
        u32x4 q0 = pack_lo(acc), q1 = pack_hi(acc);
        if (c.hi) { q1[2] = 0; q1[3] = 0; }
        const unsigned xb = c.lds + 3 * SLOT;
        sts128(xb + c.wid * 2048 + c.lane * 16, q0); sts128(xb + c.wid * 2048 + 1024 + c.lane * 16, q1);
        lds_bar();
#pragma unroll
        for (int t = 0; t < 4; ++t) { q[t][0] = lds128(xb + t * 2048 + c.lane * 16); q[t][1] = lds128(xb + t * 2048 + 1024 + c.lane * 16); }
    }
    const rsrc_t rk = make_rsrc(ws + WS_KVA + (size_t)(c.h * 64 + c.wid * 16) * 4096, 16 * 4096);
    const unsigned voff = c.lane * 16;
#define LDKV(X, j) do { X.v1 = __builtin_amdgcn_raw_buffer_load_b128(rk, voff + 3072, (j) * 4096, SC1); X.v0 = __builtin_amdgcn_raw_buffer_load_b128(rk, voff + 2048, (j) * 4096, SC1); \
                        X.k1 = __builtin_amdgcn_raw_buffer_load_b128(rk, voff + 1024, (j) * 4096, SC1); X.k0 = __builtin_amdgcn_raw_buffer_load_b128(rk, voff, (j) * 4096, SC1); } while (0)
    const f32x16 zero = {};
    const unsigned comb = c.lds, stat = c.lds + 49152, flagw = c.lds + 49152 + 8192;
    if (threadIdx.x == 0) *(LAS unsigned*)(size_t)flagw = 0;
    for (int exact = 0;; ++exact) {
        KV a, b, cc, d;
        float ref[4];
        LDKV(a, 0); LDKV(b, 1); LDKV(cc, 2);
#pragma unroll
        for (int t = 0; t < 4; ++t) { f32x16 s = mfma(a.k0, q[t][0], zero); s = mfma(a.k1, q[t][1], s); ref[t] = max16(s); }
        if (exact) {
            for (int j = 1; j < 16; ++j) {
                const u32x4 k0 = __builtin_amdgcn_raw_buffer_load_b128(rk, voff, j * 4096, SC1), k1 = __builtin_amdgcn_raw_buffer_load_b128(rk, voff + 1024, j * 4096, SC1);
#pragma unroll
                for (int t = 0; t < 4; ++t) { f32x16 s = mfma(k0, q[t][0], zero); s = mfma(k1, q[t][1], s); ref[t] = fmaxf(ref[t], max16(s)); }
            }
        }
#pragma unroll
        for (int t = 0; t < 4; ++t) { ref[t] = both_max(ref[t]) + (exact ? 0.f : REF_OFFSET); set_reference(q[t][1], -ref[t], c.hi); }
        f32x16 o0 = {}, o1 = {}, o2 = {}, o3 = {};
        f32x16 sa, sb; u32x4 pa, pb = {};
        float e12 = 0.f, e13 = 0.f, e14 = 0.f, e15 = 0.f;
        d.v1 = u32x4{};
        sa = mfma(a.k0, q[0][0], zero); sa = mfma(a.k1, q[0][1], sa);
#define EX(S, i) S[i] = __builtin_amdgcn_exp2f(S[i])
#define STEP(SC, SN, KN0, KN1, QN0, QN1, OP, VP1, OC, VC0) do { \
    SN = mfma(KN0, QN0, zero); SBAR(); \
    pb[2] = pk(e12, e13); SBAR(); EX(SC, 0); SBAR(); pb[3] = pk(e14, e15); SBAR(); EX(SC, 1); EX(SC, 2); EX(SC, 3); SBAR(); \
    OP = mfma(VP1, pb, OP); SBAR(); \
    EX(SC, 4); EX(SC, 5); SBAR(); pa[0] = pk(SC[0], SC[1]); SBAR(); EX(SC, 6); SBAR(); pa[1] = pk(SC[2], SC[3]); SBAR(); EX(SC, 7); SBAR(); \
    SN = mfma(KN1, QN1, SN); SBAR(); \
    EX(SC, 8); SBAR(); pa[2] = pk(SC[4], SC[5]); SBAR(); EX(SC, 9); SBAR(); pa[3] = pk(SC[6], SC[7]); SBAR(); EX(SC, 10); EX(SC, 11); SBAR(); \
    OC = mfma(VC0, pa, OC); SBAR(); \
    e12 = __builtin_amdgcn_exp2f(SC[12]); SBAR(); pb[0] = pk(SC[8], SC[9]); SBAR(); e13 = __builtin_amdgcn_exp2f(SC[13]); SBAR(); pb[1] = pk(SC[10], SC[11]); SBAR(); \
    e14 = __builtin_amdgcn_exp2f(SC[14]); e15 = __builtin_amdgcn_exp2f(SC[15]); SBAR(); } while (0)
#define TILE(CUR, PREV, NXT, jload) do { \
    STEP(sa, sb, CUR.k0, CUR.k1, q[1][0], q[1][1], o3, PREV.v1, o0, CUR.v0); LDKV(PREV, jload); SBAR(); \
    STEP(sb, sa, CUR.k0, CUR.k1, q[2][0], q[2][1], o0, CUR.v1, o1, CUR.v0); \
    STEP(sa, sb, CUR.k0, CUR.k1, q[3][0], q[3][1], o1, CUR.v1, o2, CUR.v0); \
    STEP(sb, sa, NXT.k0, NXT.k1, q[0][0], q[0][1], o2, CUR.v1, o3, CUR.v0); } while (0)
        for (int j = 0; j < 16; j += 4) { TILE(a, d, b, j + 3); TILE(b, a, cc, j + 4); TILE(cc, b, d, j + 5); TILE(d, cc, a, j + 6); }
        pb[2] = pk(e12, e13); pb[3] = pk(e14, e15);
        o3 = mfma(d.v1, pb, o3);
#undef TILE
#undef STEP
#undef EX
        bool bad = false;
        {
            const f32x16* ot[4] = {&o0, &o1, &o2, &o3};
#pragma unroll
            for (int t = 0; t < 4; ++t) {
                const float l = (*ot[t])[12];
                if (c.hi) {
                    bad |= !(l < L_LIMIT);
                    sts64(stat + (t * 4 + c.wid) * 256 + c.r32 * 8, u32x2{__float_as_uint(ref[t]), __float_as_uint(ref[t] + __builtin_amdgcn_logf(l))});
                }
                if (t != c.wid) {
                    const unsigned dst = comb + (t * 3 + (c.wid > t ? c.wid - 1 : c.wid)) * 4096 + c.lane * 16;
#pragma unroll
                    for (int g = 0; g < 4; ++g)
                        sts128(dst + g * 1024, u32x4{__float_as_uint((*ot[t])[4 * g]), __float_as_uint((*ot[t])[4 * g + 1]), __float_as_uint((*ot[t])[4 * g + 2]), __float_as_uint((*ot[t])[4 * g + 3])});
                }
            }
        }
        f32x16 o = c.wid == 0 ? o0 : c.wid == 1 ? o1 : c.wid == 2 ? o2 : o3;
        lds_bar();
        {
            float m[4], e[4];
#pragma unroll
            for (int v = 0; v < 4; ++v) { const u32x2 s = *(const LAS u32x2*)(size_t)(stat + (c.wid * 4 + v) * 256 + c.r32 * 8); m[v] = __uint_as_float(s[0]); e[v] = __uint_as_float(s[1]); }
            const float top = fmaxf(fmaxf(e[0], e[1]), fmaxf(e[2], e[3]));
            const float mine = c.wid == 0 ? m[0] : c.wid == 1 ? m[1] : c.wid == 2 ? m[2] : m[3];
            o *= __builtin_amdgcn_exp2f(mine - top);
#pragma unroll
            for (int v = 0; v < 3; ++v) {
                const float mv = v == 0 ? (c.wid == 0 ? m[1] : m[0]) : v == 1 ? (c.wid <= 1 ? m[2] : m[1]) : (c.wid <= 2 ? m[3] : m[2]);
                const float cv = __builtin_amdgcn_exp2f(mv - top);
#pragma unroll
                for (int g = 0; g < 4; ++g) {
                    const u32x4 p = lds128(comb + (c.wid * 3 + v) * 4096 + g * 1024 + c.lane * 16);
#pragma unroll
                    for (int i = 0; i < 4; ++i) o[4 * g + i] = fmaf(__uint_as_float(p[i]), cv, o[4 * g + i]);
                }
            }
        }
        o *= __builtin_amdgcn_rcpf(upper_val(o[12]));
        float chk = 0.f;
#pragma unroll
        for (int i = 0; i < 16; ++i) chk = fmaf(o[i], 0.f, chk);
        bad |= !(chk == 0.f);
        u32x4 f0 = pack_lo(o), f1 = pack_hi(o);
        if (c.hi) { f1[2] = 0; f1[3] = 0; }
        const rsrc_t ro = make_rsrc(ws + WS_OA + (size_t)((c.rb * 4 + c.wid) * 16 + c.h) * 2048, 2048);
        __builtin_amdgcn_raw_buffer_store_b128(f0, ro, voff, 0, SC1);
        __builtin_amdgcn_raw_buffer_store_b128(f1, ro, voff + 1024, 0, SC1);
        if (exact) break;
        if (__any(bad) && c.lane == 0) *(LAS unsigned*)(size_t)flagw = 1;
        lds_bar();
        if (__builtin_amdgcn_readfirstlane(*(const LAS unsigned*)(size_t)flagw) == 0) break;
#pragma unroll
        for (int t = 0; t < 4; ++t) if (c.hi) { q[t][1][2] = 0; q[t][1][3] = 0; }
    }
#undef LDKV
}

DI void phase_out(const Ctx& c, const u16* wo, const char* ofr, u16* out) {
    if (c.h >= 14) return;
    const int cg = c.h % 7, rh = c.h / 7, rt = c.wid & 1, kh = c.wid >> 1;
    const rsrc_t rw = make_rsrc(wo, WO_BYTES);
    {
        const int row = c.lane >> 3;
        const unsigned vw = (112 * c.wid + row) * XROW + cg * 128 + (((c.lane & 7) ^ (((row >> 1) & 1) << 2)) << 4);
#pragma unroll
        for (int j = 0; j < 14; ++j) dma16(rw, c.lds + (14 * c.wid + j) * 1024, vw, 8 * j * XROW);
    }
    const int qt = c.rb * 4 + rh * 2 + rt;
    const rsrc_t ro = make_rsrc(ofr + (size_t)(qt * 16 + kh * 8) * 2048, 16384);
    u32x4 of[16];
#pragma unroll
    for (int i = 0; i < 16; ++i) of[i] = __builtin_amdgcn_raw_buffer_load_b128(ro, c.lane * 16 + (i & 3) * 1024, (i >> 2) * 4096, SC1);
    wait_bar<16>();
    const int q = (c.lane & 15) >> 2;
    const unsigned col = 32 * ((c.lane >> 4) & 1) + 8 * (c.lane & 3);
    const unsigned rbase = c.lds + (224 * kh + 4 * c.hi + q) * 128;
    const unsigned a0 = rbase + (col ^ ((q >> 1) << 6)), a1 = rbase + ((col + 64) ^ ((q >> 1) << 6));
    const unsigned back = c.hi ? 4 * 128 : 0;
    f32x16 acc0 = {}, acc1 = {};
#pragma unroll
    for (int i = 0; i < 16; ++i) {
        const unsigned ro0 = ((i >> 1) * 28 + (i & 1) * 16) * 128, ro1 = ro0 + 8 * 128;
        const unsigned adj = (i & 1) ? back : 0;
        acc0 = mfma(tr_frag(a0 + ro0, a0 + ro1 - adj), of[i], acc0);
        acc1 = mfma(tr_frag(a1 + ro0, a1 + ro1 - adj), of[i], acc1);
    }
    const unsigned xch = c.lds + 57344;
    {
        const f32x16& snd = kh ? acc0 : acc1;
#pragma unroll
        for (int g = 0; g < 4; ++g)
            sts128(xch + c.wid * 4096 + g * 1024 + c.lane * 16, u32x4{__float_as_uint(snd[4 * g]), __float_as_uint(snd[4 * g + 1]), __float_as_uint(snd[4 * g + 2]), __float_as_uint(snd[4 * g + 3])});
    }
    f32x16 r = kh ? acc1 : acc0;
    lds_bar();
#pragma unroll
    for (int g = 0; g < 4; ++g) {
        const u32x4 p = lds128(xch + (c.wid ^ 2) * 4096 + g * 1024 + c.lane * 16);
#pragma unroll
        for (int i = 0; i < 4; ++i) r[4 * g + i] += __uint_as_float(p[i]);
    }
    const u32x4 lo = pack_lo(r), hi = pack_hi(r);
    auto s0 = __builtin_amdgcn_permlane32_swap(lo[0], lo[2], false, false), s1 = __builtin_amdgcn_permlane32_swap(lo[1], lo[3], false, false);
    auto s2 = __builtin_amdgcn_permlane32_swap(hi[0], hi[2], false, false), s3 = __builtin_amdgcn_permlane32_swap(hi[1], hi[3], false, false);
    char* dst = (char*)out + (size_t)(qt * 32 + c.r32) * (2 * XROW) + cg * 128 + kh * 64 + c.hi * 16;
    *(u32x4*)dst = u32x4{s0[0], s1[0], s0[1], s1[1]};
    *(u32x4*)(dst + 32) = u32x4{s2[0], s3[0], s2[1], s3[1]};
}

DI void phase_b1(const Ctx& c, const u16* x, const u16* w, const u16* wphi, char* ws, u32x4 (&eq)[4]) {
    constexpr unsigned SLOT = 28672, WOFF = 16384;
    const rsrc_t rx = make_rsrc(x, X_BYTES), rw = make_rsrc(w, W_BYTES);
    const unsigned xrow = (c.rb * 128 + 32 * c.wid) * XROW;
    const unsigned vx0 = xrow + x_src(c, 0), vx1 = xrow + 8 * XROW + x_src(c, 1);
    unsigned vw[3];
#pragma unroll
    for (int j = 0; j < 3; ++j) { const int p = 64 * j + c.lane, kr = p / 12, rem = p % 12; vw[j] = (16 * c.wid + kr) * WROW + (rem >> 2) * XROW + c.h * 56 + (rem & 3) * 16; }
    auto issue = [&](int ch) {
        const unsigned base = c.lds + (ch & 1) * SLOT;
        dma16(rx, base + c.wid * 4096, vx0, ch * 128);
        dma16(rx, base + c.wid * 4096 + 1024, vx1, ch * 128);
        dma16(rx, base + c.wid * 4096 + 2048, vx0, ch * 128 + 16 * XROW);
        dma16(rx, base + c.wid * 4096 + 3072, vx1, ch * 128 + 16 * XROW);
#pragma unroll
        for (int j = 0; j < 3; ++j) dma16(rw, base + WOFF + c.wid * 3072 + j * 1024, vw[j], 64 * ch * WROW);
    };
    unsigned we[16];
    {
        const int f = c.r32 < 28 ? c.r32 : c.r32 - 4;
        const u16* p = wphi + c.h * 784 + f;
#pragma unroll
        for (int i = 0; i < 16; ++i) { const int d = 8 * (i >> 2) + 4 * c.hi + (i & 3); we[i] = p[(d < 28 ? d : d - 4) * 28]; }
    }
    issue(0); issue(1);
    unsigned xo[4]; x_frag_offsets(c, xo);
    const unsigned wo = (8 * c.hi + ((c.lane & 15) >> 2)) * 192 + 32 * ((c.lane >> 4) & 1) + 8 * (c.lane & 3);
    f32x16 aq = {}, ak = {}, av = {};
#pragma unroll
    for (int ch = 0; ch < 7; ++ch) {
        if (ch < 6) wait_bar<7>(); else wait_bar<0>();
        const unsigned base = c.lds + (ch & 1) * SLOT;
#pragma unroll
        for (int s = 0; s < 4; ++s) {
            const unsigned wa = base + WOFF + wo + s * 3072;
            const u32x4 xf = lds128(base + c.wid * 4096 + xo[s]);
            aq = mfma(tr_frag(wa, wa + 768), xf, aq);
            ak = mfma(tr_frag(wa + 64, wa + 64 + 768), xf, ak);
            av = mfma(xf, tr_frag(wa + 128, wa + 128 + 768), av);
        }
        if (ch + 2 <= 6) { lds_bar(); issue(ch + 2); }
    }
    const f32x16 zero = {};
    u32x4 wp[2];
#pragma unroll
    for (int i = 0; i < 16; ++i) asm volatile("" : "+v"(we[i]));
#pragma unroll
    for (int i = 0; i < 8; ++i) wp[i >> 2][i & 3] = we[2 * i] | (we[2 * i + 1] << 16);
    {
        u32x4 q0 = pack_lo(aq), q1 = pack_hi(aq);
        if (c.hi) { q1[2] = 0; q1[3] = 0; }
        f32x16 p = mfma(wp[0], q0, zero); p = mfma(wp[1], q1, p);
        const float mx = both_max(max16(p)) * LOG2E, mn = both_min(min16(p)) * LOG2E;
        f32x16 ep, en;
#pragma unroll
        for (int i = 0; i < 16; ++i) { ep[i] = __builtin_amdgcn_exp2f(fmaf(p[i], LOG2E, -mx)); en[i] = __builtin_amdgcn_exp2f(fmaf(p[i], -LOG2E, mn)); }
        eq[0] = pack_lo(ep); eq[1] = pack_hi(ep); eq[2] = pack_lo(en); eq[3] = pack_hi(en);
    }
    lds_bar();
    f32x16 kv0, kv1;
    {
        u32x4 k0 = pack_lo(ak), k1 = pack_hi(ak);
        if (c.hi) { k1[2] = 0; k1[3] = 0; }
        f32x16 p = mfma(wp[0], k0, zero); p = mfma(wp[1], k1, p);
        const float mx = both_max(max16(p)) * LOG2E, mn = both_min(min16(p)) * LOG2E;
        f32x16 ep, en;
#pragma unroll
        for (int i = 0; i < 16; ++i) { ep[i] = __builtin_amdgcn_exp2f(fmaf(p[i], LOG2E, -mx)); en[i] = __builtin_amdgcn_exp2f(fmaf(p[i], -LOG2E, mn)); }
        const float live = c.hi ? 0.f : 1.f;
        float sp = (ep[12] + ep[13] + ep[14] + ep[15]) * live, sn = (en[12] + en[13] + en[14] + en[15]) * live;
#pragma unroll
        for (int i = 0; i < 12; ++i) { sp += ep[i]; sn += en[i]; }
        const float rp = __builtin_amdgcn_rcpf(both_sum(sp)), rn = __builtin_amdgcn_rcpf(both_sum(sn));
        const float rp3 = rp * live, rn3 = rn * live;
        const unsigned img = c.lds + 32768 + c.wid * 4352, wr = img + c.r32 * 136 + c.hi * 8;
#pragma unroll
        for (int g = 0; g < 4; ++g) {
            const float a = g == 3 ? rp3 : rp, b = g == 3 ? rn3 : rn;
            sts64(wr + g * 16, u32x2{pk(ep[4 * g] * a, ep[4 * g + 1] * a), pk(ep[4 * g + 2] * a, ep[4 * g + 3] * a)});
            sts64(wr + 64 + g * 16, u32x2{pk(en[4 * g] * b, en[4 * g + 1] * b), pk(en[4 * g + 2] * b, en[4 * g + 3] * b)});
        }
        u32x4 v0 = pack_lo(av), v1 = pack_hi(av);
        if (c.r32 >= 28) { const unsigned v = c.r32 == 28 ? 0x3F803F80u : 0u; v0 = u32x4{v, v, v, v}; v1 = v0; }
        const unsigned rd = img + (4 * c.hi + ((c.lane & 15) >> 2)) * 136 + 32 * ((c.lane >> 4) & 1) + 8 * (c.lane & 3);
        kv0 = mfma(tr_frag(rd, rd + 8 * 136), v0, zero); kv0 = mfma(tr_frag(rd + 16 * 136, rd + 24 * 136), v1, kv0);
        kv1 = mfma(tr_frag(rd + 64, rd + 64 + 8 * 136), v0, zero); kv1 = mfma(tr_frag(rd + 64 + 16 * 136, rd + 64 + 24 * 136), v1, kv1);
    }
#pragma unroll
    for (int g = 0; g < 4; ++g) {
        sts128(c.lds + c.wid * 8192 + g * 1024 + c.lane * 16, u32x4{__float_as_uint(kv0[4 * g]), __float_as_uint(kv0[4 * g + 1]), __float_as_uint(kv0[4 * g + 2]), __float_as_uint(kv0[4 * g + 3])});
        sts128(c.lds + c.wid * 8192 + 4096 + g * 1024 + c.lane * 16, u32x4{__float_as_uint(kv1[4 * g]), __float_as_uint(kv1[4 * g + 1]), __float_as_uint(kv1[4 * g + 2]), __float_as_uint(kv1[4 * g + 3])});
    }
    lds_bar();
    const rsrc_t ro = make_rsrc(ws + WS_KVP + (size_t)(c.h * 16 + c.rb) * 8192, 8192);
#pragma unroll
    for (int i = 0; i < 2; ++i) {
        const unsigned off = (2 * c.wid + i) * 1024 + c.lane * 16;
        f32x4 s = __builtin_bit_cast(f32x4, lds128(c.lds + off));
#pragma unroll
        for (int v = 1; v < 4; ++v) s += __builtin_bit_cast(f32x4, lds128(c.lds + v * 8192 + off));
        __builtin_amdgcn_raw_buffer_store_b128(__builtin_bit_cast(u32x4, s), ro, off, 0, SC1);
    }
}

DI void phase_b2(const Ctx& c, char* ws, const u32x4 (&eq)[4]) {
    const rsrc_t rp = make_rsrc(ws + WS_KVP + (size_t)c.h * 16 * 8192, 16 * 8192);
    const unsigned off = 2 * c.wid * 1024 + c.lane * 16;
    u32x4 pa[16], pb[16];
#pragma unroll
    for (int i = 0; i < 16; ++i) { pa[i] = __builtin_amdgcn_raw_buffer_load_b128(rp, off, i * 8192, SC1); pb[i] = __builtin_amdgcn_raw_buffer_load_b128(rp, off + 1024, i * 8192, SC1); }
    f32x4 sa = __builtin_bit_cast(f32x4, pa[0]), sb = __builtin_bit_cast(f32x4, pb[0]);
#pragma unroll
    for (int i = 1; i < 16; ++i) { sa += __builtin_bit_cast(f32x4, pa[i]); sb += __builtin_bit_cast(f32x4, pb[i]); }
    u32x4 f = {pk(sa[0], sa[1]), pk(sa[2], sa[3]), pk(sb[0], sb[1]), pk(sb[2], sb[3])};
    if (c.r32 == 29) {
        const unsigned tail = ((c.wid & 1) && c.hi) ? 0u : 0x3F803F80u;
        f = u32x4{0x3F803F80u, 0x3F803F80u, tail, tail};
    }
    sts128(c.lds + c.wid * 1024 + c.lane * 16, f);
    lds_bar();
    const f32x16 zero = {};
    f32x16 np = mfma(lds128(c.lds + c.lane * 16), eq[0], zero); np = mfma(lds128(c.lds + 1024 + c.lane * 16), eq[1], np);
    f32x16 nn = mfma(lds128(c.lds + 2048 + c.lane * 16), eq[2], zero); nn = mfma(lds128(c.lds + 3072 + c.lane * 16), eq[3], nn);
    const float rp_ = __builtin_amdgcn_rcpf(np[13]), rn_ = __builtin_amdgcn_rcpf(nn[13]);
    const float inv = __builtin_amdgcn_rcpf(fmaf(np[12], rp_, fmaf(nn[12], rn_, 1e-6f)));
    const float ap = upper_val(rp_ * inv), an = upper_val(rn_ * inv);
    f32x16 o;
#pragma unroll
    for (int i = 0; i < 16; ++i) o[i] = fmaf(np[i], ap, nn[i] * an);
    u32x4 f0 = pack_lo(o), f1 = pack_hi(o);
    if (c.hi) { f1[2] = 0; f1[3] = 0; }
    const rsrc_t ro = make_rsrc(ws + WS_OB + (size_t)((c.rb * 4 + c.wid) * 16 + c.h) * 2048, 2048);
    __builtin_amdgcn_raw_buffer_store_b128(f0, ro, c.lane * 16, 0, SC1);
    __builtin_amdgcn_raw_buffer_store_b128(f1, ro, c.lane * 16 + 1024, 0, SC1);
}

DI Ctx make_ctx(char* lds) {
    Ctx c;
    c.lane = threadIdx.x & 63; c.wid = __builtin_amdgcn_readfirstlane(threadIdx.x >> 6);
    c.hi = c.lane >> 5; c.r32 = c.lane & 31;
    const int b = blockIdx.x & 255, xcd = b & 7, i = b >> 3;
    c.h = (xcd & 1) * 8 + (i & 7); c.rb = (xcd >> 1) * 4 + (i >> 3);
    c.lds = (unsigned)(size_t)(LAS char*)lds;
    return c;
}

template <int PHASE>
__global__ __launch_bounds__(256, 2) void stage(const u16* xa, const u16* wa, const u16* woa, const u16* xb, const u16* wb, const u16* wphi, const u16* wob, u16* out, char* ws) {
    extern __shared__ __attribute__((aligned(16))) char lds[];
    const Ctx c = make_ctx(lds);
    const bool is_b = blockIdx.x >= 256;
    u32x4* phiq = (u32x4*)(ws + WS_PHIQ) + (size_t)(c.h * 64 + c.rb * 4 + c.wid) * 256 + c.lane;
    if (PHASE == 0) {
        if (!is_b) phase_a1(c, xa, wa, ws);
        else { u32x4 eq[4]; phase_b1(c, xb, wb, wphi, ws, eq); for (int i = 0; i < 4; ++i) phiq[i * 64] = eq[i]; }
    } else if (PHASE == 1) {
        if (!is_b) phase_a2(c, xa, wa, ws);
        else { u32x4 eq[4]; for (int i = 0; i < 4; ++i) eq[i] = phiq[i * 64]; phase_b2(c, ws, eq); }
    } else {
        phase_out(c, is_b ? wob : woa, ws + (is_b ? WS_OB : WS_OA), out + (is_b ? DM : 0));
    }
}

}

extern "C" void kernel_launch(void* const* d_in, const int* in_sizes, int n_in, void* d_out, int out_size, void* d_ws, size_t ws_size, hipStream_t stream) {
    const u16* xa = (const u16*)d_in[0]; const u16* wa = (const u16*)d_in[1]; const u16* woa = (const u16*)d_in[2];
    const u16* xb = (const u16*)d_in[3]; const u16* wb = (const u16*)d_in[4]; const u16* wphi = (const u16*)d_in[5]; const u16* wob = (const u16*)d_in[6];
    (void)hipFuncSetAttribute((const void*)stage<0>, hipFuncAttributeMaxDynamicSharedMemorySize, LDS_BYTES);
    (void)hipFuncSetAttribute((const void*)stage<1>, hipFuncAttributeMaxDynamicSharedMemorySize, LDS_BYTES);
    (void)hipFuncSetAttribute((const void*)stage<2>, hipFuncAttributeMaxDynamicSharedMemorySize, LDS_BYTES);
    stage<0><<<512, 256, LDS_BYTES, stream>>>(xa, wa, woa, xb, wb, wphi, wob, (u16*)d_out, (char*)d_ws);
    stage<1><<<512, 256, LDS_BYTES, stream>>>(xa, wa, woa, xb, wb, wphi, wob, (u16*)d_out, (char*)d_ws);
    stage<2><<<512, 256, LDS_BYTES, stream>>>(xa, wa, woa, xb, wb, wphi, wob, (u16*)d_out, (char*)d_ws);
}
```

```cpp
#include <hip/hip_runtime.h>
#include <stdint.h>

namespace {

typedef unsigned short u16;
typedef unsigned long long u64;
typedef __attribute__((ext_vector_type(16))) float f32x16;
typedef __attribute__((ext_vector_type(4))) float f32x4;
typedef __attribute__((ext_vector_type(2))) float f32x2;
typedef __attribute__((ext_vector_type(8))) __bf16 bf16x8;
typedef __attribute__((ext_vector_type(2))) __bf16 bf16x2;
typedef __attribute__((ext_vector_type(4))) unsigned u32x4;
typedef __attribute__((ext_vector_type(2))) unsigned u32x2;
typedef __attribute__((ext_vector_type(4))) short s16x4;
typedef __amdgpu_buffer_rsrc_t rsrc_t;
#define LAS __attribute__((address_space(3)))
#define GAS __attribute__((address_space(1)))
#define DI __device__ __forceinline__
#define SBAR() __builtin_amdgcn_sched_barrier(0)

constexpr int SEQ = 2048, DM = 448;
constexpr unsigned XROW = DM * 2;
constexpr unsigned WROW = 3 * DM * 2;
constexpr unsigned X_BYTES = SEQ * XROW, W_BYTES = DM * WROW, WO_BYTES = DM * XROW;
constexpr size_t WS_EPOCH_A = 0, WS_EPOCH_B = 256;
constexpr size_t WS_FK = 4096;
constexpr size_t WS_FG = WS_FK + 8192;
constexpr size_t WS_FP = WS_FG + 8192;
constexpr size_t WS_FOA = WS_FP + 8192;
constexpr size_t WS_FOB = WS_FOA + 8192;
constexpr size_t WS_KVA = 65536;
constexpr size_t WS_OA = WS_KVA + (4u << 20);
constexpr size_t WS_OB = WS_OA + (2u << 20);
constexpr size_t WS_KVP = WS_OB + (2u << 20);
constexpr int LDS_BYTES = 80 * 1024;
constexpr float LOG2E = 1.4426950408889634f;
constexpr float QSCALE = 0.18898223650461362f * LOG2E;
#define REF_OFF 32.f
constexpr float REF_OFFSET = REF_OFF;
constexpr float L_LIMIT = 18446744073709551616.f;
constexpr int SC1 = 16;
constexpr unsigned SPIN_CAP = 1u << 18;
#define ROT 0
#define BAHEAD 2
#define QEARLY 3
#define QSPLIT 16
#define BDELAY 0

DI f32x16 mfma(u32x4 a, u32x4 b, f32x16 c) {
    return __builtin_amdgcn_mfma_f32_32x32x16_bf16(__builtin_bit_cast(bf16x8, a), __builtin_bit_cast(bf16x8, b), c, 0, 0, 0);
}
DI unsigned pk(float lo, float hi) { f32x2 v = {lo, hi}; return __builtin_bit_cast(unsigned, __builtin_convertvector(v, bf16x2)); }
DI u32x4 pack_lo(const f32x16& x) { return u32x4{pk(x[0], x[1]), pk(x[2], x[3]), pk(x[4], x[5]), pk(x[6], x[7])}; }
DI u32x4 pack_hi(const f32x16& x) { return u32x4{pk(x[8], x[9]), pk(x[10], x[11]), pk(x[12], x[13]), pk(x[14], x[15])}; }
DI u32x4 quad(const f32x16& x, int g) { return u32x4{__float_as_uint(x[4 * g]), __float_as_uint(x[4 * g + 1]), __float_as_uint(x[4 * g + 2]), __float_as_uint(x[4 * g + 3])}; }
DI rsrc_t make_rsrc(const void* p, unsigned bytes) { return __builtin_amdgcn_make_buffer_rsrc((void*)p, 0, bytes, 0x00020000); }
DI u32x4 ld128(rsrc_t rs, unsigned voff, unsigned soff) { return __builtin_amdgcn_raw_buffer_load_b128(rs, voff, soff, 0); }
DI u32x4 ld128_dev(rsrc_t rs, unsigned voff, unsigned soff) { return __builtin_amdgcn_raw_buffer_load_b128(rs, voff, soff, SC1); }
DI void st128_dev(u32x4 v, rsrc_t rs, unsigned voff, unsigned soff) { __builtin_amdgcn_raw_buffer_store_b128(v, rs, voff, soff, SC1); }
DI void dma16(rsrc_t rs, unsigned lds_addr, unsigned voff, unsigned soff) {
    asm volatile("s_mov_b32 m0, %0\n\ts_nop 0\n\tbuffer_load_dwordx4 %1, %2, %3 offen lds" ::"s"(lds_addr), "v"(voff), "s"(rs), "s"(soff) : "memory");
}
DI void ld128_async(u32x4& d, rsrc_t rs, unsigned voff, unsigned soff) {
    asm volatile("buffer_load_dwordx4 %0, %1, %2, %3 offen" : "=v"(d) : "v"(voff), "s"(rs), "s"(soff) : "memory");
}
template <int N> DI void wait_vm() { asm volatile("s_waitcnt vmcnt(%0)" ::"n"(N) : "memory"); }
template <int N> DI void wait_bar() { asm volatile("s_waitcnt vmcnt(%0) lgkmcnt(0)\n\ts_barrier" ::"n"(N) : "memory"); }
DI void lds_bar() { asm volatile("s_waitcnt lgkmcnt(0)\n\ts_barrier" ::: "memory"); }
DI u32x4 lds128(unsigned a) { return *(const LAS u32x4*)(size_t)a; }
DI void sts128(unsigned a, u32x4 v) { *(LAS u32x4*)(size_t)a = v; }
DI void sts64(unsigned a, u32x2 v) { *(LAS u32x2*)(size_t)a = v; }
DI u32x2 tr64(unsigned a) { return __builtin_bit_cast(u32x2, __builtin_amdgcn_ds_read_tr16_b64_v4i16((LAS s16x4*)(size_t)a)); }
DI u32x4 tr_frag(unsigned a0, unsigned a1) { u32x2 lo = tr64(a0), hi = tr64(a1); return u32x4{lo[0], lo[1], hi[0], hi[1]}; }
DI float both_max(float v) { auto r = __builtin_amdgcn_permlane32_swap(__float_as_uint(v), __float_as_uint(v), false, false); return fmaxf(__uint_as_float(r[0]), __uint_as_float(r[1])); }
DI float both_min(float v) { auto r = __builtin_amdgcn_permlane32_swap(__float_as_uint(v), __float_as_uint(v), false, false); return fminf(__uint_as_float(r[0]), __uint_as_float(r[1])); }
DI float both_sum(float v) { auto r = __builtin_amdgcn_permlane32_swap(__float_as_uint(v), __float_as_uint(v), false, false); return __uint_as_float(r[0]) + __uint_as_float(r[1]); }
DI float upper_val(float v) { auto r = __builtin_amdgcn_permlane32_swap(__float_as_uint(v), __float_as_uint(v), false, false); return __uint_as_float(r[1]); }
DI float max16(const f32x16& s) {
    float a = fmaxf(fmaxf(s[0], s[1]), s[2]), b = fmaxf(fmaxf(s[3], s[4]), s[5]);
    a = fmaxf(fmaxf(a, s[6]), s[7]); b = fmaxf(fmaxf(b, s[8]), s[9]);
    a = fmaxf(fmaxf(a, s[10]), s[11]); b = fmaxf(fmaxf(b, s[12]), s[13]);
    a = fmaxf(fmaxf(a, s[14]), s[15]);
    return fmaxf(a, b);
}
DI float min16(const f32x16& s) {
    float a = fminf(fminf(s[0], s[1]), s[2]), b = fminf(fminf(s[3], s[4]), s[5]);
    a = fminf(fminf(a, s[6]), s[7]); b = fminf(fminf(b, s[8]), s[9]);
    a = fminf(fminf(a, s[10]), s[11]); b = fminf(fminf(b, s[12]), s[13]);
    a = fminf(fminf(a, s[14]), s[15]);
    return fminf(a, b);
}

#define STAMP(c, id) do {} while (0)

struct Ctx {
    int lane, wid, hi, r32, h, rb, rot;
    static constexpr unsigned lds = 0;
};

DI Ctx fresh(const Ctx& c) {
    Ctx n = c;
    asm volatile("v_mbcnt_lo_u32_b32 %0, -1, 0\n\tv_mbcnt_hi_u32_b32 %0, -1, %0" : "=v"(n.lane));
    n.hi = n.lane >> 5; n.r32 = n.lane & 31;
    return n;
}
DI int chunk_at(const Ctx& c, int p) { const int v = c.rot + p; return v >= 7 ? v - 7 : v; }
DI u64 load_word(const char* p) { return __hip_atomic_load((GAS u64*)(size_t)p, __ATOMIC_RELAXED, __HIP_MEMORY_SCOPE_AGENT); }
DI void store_word(char* p, u64 v) { __hip_atomic_store((GAS u64*)(size_t)p, v, __ATOMIC_RELAXED, __HIP_MEMORY_SCOPE_AGENT); }
DI void epoch_request(u64& v, const char* p) { asm volatile("s_load_dwordx2 %0, %1, 0x0" : "=s"(v) : "s"(p) : "memory"); }
DI void publish(const Ctx& c, char* word, u64 nonce) { if (c.lane == 0) store_word(word, nonce); }
DI void await(const Ctx& c, const char* words, int mask, u64 nonce, unsigned cap) {
    const char* p = words + (c.lane & mask) * 8;
#pragma unroll 1
    for (unsigned spins = 0; spins < cap; ++spins) {
        if (__all(load_word(p) == nonce)) break;
        __builtin_amdgcn_s_sleep(2);
    }
    __builtin_amdgcn_fence(__ATOMIC_ACQUIRE, "wavefront");
    asm volatile("" ::: "memory");
}

DI void x_frag_offsets(const Ctx& c, unsigned (&xo)[4]) {
#pragma unroll
    for (int s = 0; s < 4; ++s) xo[s] = c.r32 * 128 + (((2 * s + c.hi) ^ ((c.r32 >> 1) & 7)) << 4);
}
DI unsigned x_swz(const Ctx& c, int odd) { return ((c.lane & 7) ^ (4 * odd + (c.lane >> 4))) << 4; }
DI void x_bounce(const Ctx& c, unsigned buf, const u32x4 (&xr)[28], int ch) {
    const unsigned w0 = buf + (c.lane >> 3) * 128 + x_swz(c, 0), w1 = buf + (c.lane >> 3) * 128 + x_swz(c, 1);
    sts128(w0, xr[4 * ch]); sts128(w1 + 1024, xr[4 * ch + 1]); sts128(w0 + 2048, xr[4 * ch + 2]); sts128(w1 + 3072, xr[4 * ch + 3]);
}

DI void out_prefetch(const Ctx& c, const u16* wo) {
    const int cg = c.h % 7, row = c.lane >> 3;
    const rsrc_t rw = make_rsrc(wo, WO_BYTES);
    const unsigned vw = (112 * c.wid + row) * XROW + cg * 128 + (((c.lane & 7) ^ (((row >> 1) & 1) << 2)) << 4);
#pragma unroll
    for (int j = 0; j < 14; ++j) dma16(rw, c.lds + (14 * c.wid + j) * 1024, vw, 8 * j * XROW);
}
DI void phase_out(const Ctx& c, const char* ofr, const char* flags, u64 nonce, u16* out, char* epoch) {
    const int cg = c.h % 7, rh = c.h / 7, rt = c.wid & 1, kh = c.wid >> 1;
    const int qt = c.rb * 4 + rh * 2 + rt;
    await(c, flags + (qt * 16 + kh * 8) * 8, 7, nonce, SPIN_CAP);
    STAMP(c, 11);
    const rsrc_t ro = make_rsrc(ofr + (size_t)(qt * 16 + kh * 8) * 2048, 16384);
    u32x4 of[16];
#pragma unroll
    for (int i = 0; i < 16; ++i) of[i] = ld128_dev(ro, c.lane * 16 + (i & 3) * 1024, (i >> 2) * 4096);
    wait_bar<16>();
    const int q = (c.lane & 15) >> 2;
    const unsigned col = 32 * ((c.lane >> 4) & 1) + 8 * (c.lane & 3);
    const unsigned rbase = c.lds + (224 * kh + 4 * c.hi + q) * 128;
    const unsigned a0 = rbase + (col ^ ((q >> 1) << 6)), a1 = rbase + ((col + 64) ^ ((q >> 1) << 6));
    const unsigned back = c.hi ? 4 * 128 : 0;
    f32x16 acc0 = {}, acc1 = {};
#pragma unroll
    for (int i = 0; i < 16; ++i) {
        const unsigned ro0 = ((i >> 1) * 28 + (i & 1) * 16) * 128, ro1 = ro0 + 8 * 128;
        const unsigned adj = (i & 1) ? back : 0;
        acc0 = mfma(tr_frag(a0 + ro0, a0 + ro1 - adj), of[i], acc0);
        acc1 = mfma(tr_frag(a1 + ro0, a1 + ro1 - adj), of[i], acc1);
    }
    STAMP(c, 12);
    const unsigned xch = c.lds + 57344;
#pragma unroll
    for (int g = 0; g < 4; ++g) sts128(xch + c.wid * 4096 + g * 1024 + c.lane * 16, kh ? quad(acc0, g) : quad(acc1, g));
    f32x16 r = kh ? acc1 : acc0;
    lds_bar();
    if (c.h == 0 && c.rb == 0 && c.wid == 0 && c.lane == 0) store_word(epoch, nonce);
#pragma unroll
    for (int g = 0; g < 4; ++g) {
        const u32x4 p = lds128(xch + (c.wid ^ 2) * 4096 + g * 1024 + c.lane * 16);
#pragma unroll
        for (int i = 0; i < 4; ++i) r[4 * g + i] += __uint_as_float(p[i]);
    }
    const u32x4 lo = pack_lo(r), hi = pack_hi(r);
    auto s0 = __builtin_amdgcn_permlane32_swap(lo[0], lo[2], false, false), s1 = __builtin_amdgcn_permlane32_swap(lo[1], lo[3], false, false);
    auto s2 = __builtin_amdgcn_permlane32_swap(hi[0], hi[2], false, false), s3 = __builtin_amdgcn_permlane32_swap(hi[1], hi[3], false, false);
    char* dst = (char*)out + (size_t)(qt * 32 + c.r32) * (2 * XROW) + cg * 128 + kh * 64 + c.hi * 16;
    *(u32x4*)dst = u32x4{s0[0], s1[0], s0[1], s1[1]};
    *(u32x4*)(dst + 32) = u32x4{s2[0], s3[0], s2[1], s3[1]};
}

DI void mfma_pair(int isv, u32x4 xf, u32x4 w0, u32x4 w1, f32x16& acc0, f32x16& acc1) {
    asm("s_cmp_lg_u32 %5, 0\n\ts_cbranch_scc1 1f\n\t"
        "v_mfma_f32_32x32x16_bf16 %0, %3, %2, %0\n\tv_mfma_f32_32x32x16_bf16 %1, %4, %2, %1\n\ts_branch 2f\n"
        "1:\n\tv_mfma_f32_32x32x16_bf16 %0, %2, %3, %0\n\tv_mfma_f32_32x32x16_bf16 %1, %2, %4, %1\n2:"
        : "+v"(acc0), "+v"(acc1) : "v"(xf), "v"(w0), "v"(w1), "s"(isv) : "scc");
}
template <int N> DI void landed4(u32x4 (&r)[28], int ch) {
    asm volatile("s_waitcnt vmcnt(%4)" : "+v"(r[4 * ch]), "+v"(r[4 * ch + 1]), "+v"(r[4 * ch + 2]), "+v"(r[4 * ch + 3]) : "n"(N) : "memory");
}
DI void kv_gemm(const Ctx& c, int isv, rsrc_t rx, rsrc_t rw, f32x16& acc0, f32x16& acc1, u32x4 (&xf)[28]) {
    const unsigned bounce = c.lds + 57344 + c.wid * 4096;
    const int row = c.lane >> 3;
    const unsigned piece = (c.lane & 7) ^ (((row >> 1) & 1) << 2);
    const unsigned vw = piece < 7 ? (16 * c.wid + row) * WROW + (1 + isv) * XROW + (c.h >> 1) * 112 + (piece << 4) : 0x80000000u;
    const unsigned vx = (c.rb * 128 + 32 * c.wid + row) * XROW + (c.lane & 7) * 16;
    u32x4 xr[28];
    auto issue = [&](int ch) {
        const unsigned k = chunk_at(c, ch);
        dma16(rw, c.lds + ch * 8192 + c.wid * 2048, vw, k * 64 * WROW);
        dma16(rw, c.lds + ch * 8192 + c.wid * 2048 + 1024, vw, (k * 64 + 8) * WROW);
#pragma unroll
        for (int i = 0; i < 4; ++i) ld128_async(xr[4 * ch + i], rx, vx, k * 128 + i * 8 * XROW);
    };
    issue(0); issue(1); issue(2); issue(3);
    asm volatile("s_waitcnt lgkmcnt(0)" ::: "memory");
    STAMP(c, 1);
    unsigned xo[4]; x_frag_offsets(c, xo);
    const int q = (c.lane & 15) >> 2;
    const unsigned col = 32 * ((c.lane >> 4) & 1) + 8 * (c.lane & 3);
    const unsigned w0 = c.lds + (8 * c.hi + q) * 128 + (col ^ ((q >> 1) << 6)), w1 = c.lds + (8 * c.hi + q) * 128 + ((col + 56) ^ ((q >> 1) << 6));
    acc0 = f32x16{}; acc1 = f32x16{};
    asm volatile("s_nop 1" : "+v"(acc0), "+v"(acc1));
    u32x4 wf[2][8];
#pragma unroll
    for (int p = 0; p <= 7; ++p) {
        if (p < 7) {
            if (p <= 3) landed4<18>(xr, p); else if (p == 4) landed4<12>(xr, p); else if (p == 5) landed4<6>(xr, p); else landed4<0>(xr, p);
            x_bounce(c, bounce, xr, p);
        }
        SBAR();
        if (p > 0) {
            mfma_pair(isv, xf[4 * p - 4], wf[(p - 1) & 1][0], wf[(p - 1) & 1][1], acc0, acc1);
            mfma_pair(isv, xf[4 * p - 3], wf[(p - 1) & 1][2], wf[(p - 1) & 1][3], acc0, acc1);
        }
        SBAR();
        if (p < 7) {
            asm volatile("s_barrier" ::: "memory");
            if (p == 0) STAMP(c, 2);
            if (p == 6) STAMP(c, 3);
#pragma unroll
            for (int s = 0; s < 4; ++s) {
                const unsigned o = p * 8192 + s * 2048;
                xf[4 * p + s] = lds128(bounce + xo[s]);
                wf[p & 1][2 * s] = tr_frag(w0 + o, w0 + o + 512); wf[p & 1][2 * s + 1] = tr_frag(w1 + o, w1 + o + 512);
            }
        }
        SBAR();
        if (p > 0) {
            mfma_pair(isv, xf[4 * p - 2], wf[(p - 1) & 1][4], wf[(p - 1) & 1][5], acc0, acc1);
            mfma_pair(isv, xf[4 * p - 1], wf[(p - 1) & 1][6], wf[(p - 1) & 1][7], acc0, acc1);
        }
        SBAR();
        if (p + 4 <= 6) issue(p + 4);
    }
    asm volatile("s_nop 11" : "+v"(acc0), "+v"(acc1));
}

struct KV { u32x4 k0, k1, v0, v1; };

DI void set_reference(u32x4& q1, float nm, int hi) {
    const unsigned a = __float_as_uint(nm) & 0xFFFF0000u;
    const float r1 = nm - __uint_as_float(a);
    const unsigned b = __float_as_uint(r1) & 0xFFFF0000u;
    const float r2 = r1 - __uint_as_float(b);
    if (hi) { q1[2] = (a >> 16) | b; q1[3] = __float_as_uint(r2) >> 16; }
}

DI void branch_a(const Ctx& c, const u16* x, const u16* w, char* ws, u64& nonce) {
    u64 epoch; epoch_request(epoch, ws + WS_EPOCH_A);
    const rsrc_t rx = make_rsrc(x, X_BYTES), rw = make_rsrc(w, W_BYTES);
    const unsigned voff = c.lane * 16;
    u32x4 q[4][2];
    const rsrc_t rk = make_rsrc(ws + WS_KVA + (size_t)(c.h * 64 + c.wid * 16) * 4096, 16 * 4096);
#define LDKV(X, j) do { X.v1 = ld128_dev(rk, voff + 3072, (j) * 4096); X.v0 = ld128_dev(rk, voff + 2048, (j) * 4096); \
                        X.k1 = ld128_dev(rk, voff + 1024, (j) * 4096); X.k0 = ld128_dev(rk, voff, (j) * 4096); } while (0)
    KV a, b, cc, d;
    {
        u32x4 xf[28];
        f32x16 acc0, acc1;
        const int isv = c.h & 1;
        kv_gemm(c, isv, rx, rw, acc0, acc1, xf);
        asm volatile("" : "+s"(epoch));
        u32x4 f[4] = {pack_lo(acc0), pack_hi(acc0), pack_lo(acc1), pack_hi(acc1)};
        {
            const bool all = isv && c.r32 >= 28, tail = isv ? all : (bool)c.hi;
            const unsigned va = c.r32 == 28 ? 0x3F803F80u : 0u, v2 = isv ? va : 0x3F803F80u, v3 = isv ? va : 0x00003F80u;
#pragma unroll
            for (int i = 0; i < 4; ++i) {
                f[i][0] = all ? va : f[i][0]; f[i][1] = all ? va : f[i][1];
                if (i & 1) { f[i][2] = tail ? v2 : f[i][2]; f[i][3] = tail ? v3 : f[i][3]; }
                else       { f[i][2] = all ? va : f[i][2]; f[i][3] = all ? va : f[i][3]; }
            }
        }
        const int kt = c.rb * 4 + c.wid;
        const rsrc_t ro = make_rsrc(ws + WS_KVA + (size_t)((c.h & 14) * 64 + kt) * 4096 + isv * 2048, 64 * 4096 + 2048);
        st128_dev(f[0], ro, voff, 0); st128_dev(f[1], ro, voff + 1024, 0); st128_dev(f[2], ro, voff, 64 * 4096); st128_dev(f[3], ro, voff + 1024, 64 * 4096);
        STAMP(c, 4);
        const unsigned wq = c.lds, xb = c.lds + 32768;
        const unsigned vwq = (16 * c.wid + (c.lane >> 2)) * WROW + c.h * 56 + (c.lane & 3) * 16;
        nonce = epoch + 1;
        publish(c, ws + WS_FG + (size_t)(blockIdx.x * 4 + c.wid) * 8, nonce);
        lds_bar();
#pragma unroll
        for (int j = 0; j < QEARLY; ++j) dma16(rw, wq + j * 4096 + c.wid * 1024, vwq, chunk_at(c, j) * 64 * WROW);
        wait_vm<QEARLY + 1>();
        publish(c, ws + WS_FK + (size_t)(((c.h >> 1) * 64 + kt) * 2 + isv) * 8, nonce);
        STAMP(c, 5);
#pragma unroll
        for (int j = QEARLY; j < 7; ++j) dma16(rw, wq + j * 4096 + c.wid * 1024, vwq, chunk_at(c, j) * 64 * WROW);
        wait_bar<QEARLY == 7 ? 1 : 0>();
        const unsigned wo = wq + (8 * c.hi + ((c.lane & 15) >> 2)) * 64 + 32 * ((c.lane >> 4) & 1) + 8 * (c.lane & 3);
        f32x16 acc = {};
#pragma unroll
        for (int i = 0; i < 28; ++i) {
            if (i == QSPLIT) {
                STAMP(c, 6);
                await(c, ws + WS_FK + (size_t)((c.h >> 1) * 64 + c.wid * 16) * 16, 31, nonce, SPIN_CAP);
                STAMP(c, 7);
                LDKV(a, 0); LDKV(b, 1); LDKV(cc, 2);
            }
            acc = mfma(tr_frag(wo + i * 1024, wo + i * 1024 + 256), xf[i], acc);
        }
        acc *= QSCALE;
        u32x4 q0 = pack_lo(acc), q1 = pack_hi(acc);
        if (c.hi) { q1[2] = 0; q1[3] = 0; }
        sts128(xb + c.wid * 2048 + voff, q0); sts128(xb + c.wid * 2048 + 1024 + voff, q1);
        lds_bar();
#pragma unroll
        for (int t = 0; t < 4; ++t) { q[t][0] = lds128(xb + t * 2048 + voff); q[t][1] = lds128(xb + t * 2048 + 1024 + voff); }
    }
    const Ctx c0 = c;
    const unsigned comb = c.lds, stat = c.lds + 49152, flagw = c.lds + 57344;
    if (c.wid == 0 && c.lane == 0) *(LAS unsigned*)(size_t)flagw = 0;
    const f32x16 zero = {};
    for (int exact = 0;; ++exact) {
        float ref[4];
        if (exact) { LDKV(a, 0); LDKV(b, 1); LDKV(cc, 2); }
#pragma unroll
        for (int t = 0; t < 4; ++t) { f32x16 s = mfma(a.k0, q[t][0], zero); s = mfma(a.k1, q[t][1], s); ref[t] = max16(s); }
        if (exact) {
            for (int j = 1; j < 16; ++j) {
                const u32x4 k0 = ld128_dev(rk, voff, j * 4096), k1 = ld128_dev(rk, voff + 1024, j * 4096);
#pragma unroll
                for (int t = 0; t < 4; ++t) { f32x16 s = mfma(k0, q[t][0], zero); s = mfma(k1, q[t][1], s); ref[t] = fmaxf(ref[t], max16(s)); }
            }
        }
#pragma unroll
        for (int t = 0; t < 4; ++t) { ref[t] = both_max(ref[t]) + (exact ? 0.f : REF_OFFSET); set_reference(q[t][1], -ref[t], c.hi); }
        STAMP(c, 8);
        f32x16 o0 = {}, o1 = {}, o2 = {}, o3 = {};
        f32x16 sa, sb; u32x4 pa, pb = {};
        float e12 = 0.f, e13 = 0.f, e14 = 0.f, e15 = 0.f;
        d.v1 = u32x4{};
        sa = mfma(a.k0, q[0][0], zero); sa = mfma(a.k1, q[0][1], sa);
#define EX(S, i) S[i] = __builtin_amdgcn_exp2f(S[i])
#define STEP(SC, SN, KN0, KN1, QN0, QN1, OP, VP1, OC, VC0) do { \
    SN = mfma(KN0, QN0, zero); SBAR(); \
    pb[2] = pk(e12, e13); SBAR(); EX(SC, 0); SBAR(); pb[3] = pk(e14, e15); SBAR(); EX(SC, 1); EX(SC, 2); EX(SC, 3); SBAR(); \
    OP = mfma(VP1, pb, OP); SBAR(); \
    EX(SC, 4); EX(SC, 5); SBAR(); pa[0] = pk(SC[0], SC[1]); SBAR(); EX(SC, 6); SBAR(); pa[1] = pk(SC[2], SC[3]); SBAR(); EX(SC, 7); SBAR(); \
    SN = mfma(KN1, QN1, SN); SBAR(); \
    EX(SC, 8); SBAR(); pa[2] = pk(SC[4], SC[5]); SBAR(); EX(SC, 9); SBAR(); pa[3] = pk(SC[6], SC[7]); SBAR(); EX(SC, 10); EX(SC, 11); SBAR(); \
    OC = mfma(VC0, pa, OC); SBAR(); \
    e12 = __builtin_amdgcn_exp2f(SC[12]); SBAR(); pb[0] = pk(SC[8], SC[9]); SBAR(); e13 = __builtin_amdgcn_exp2f(SC[13]); SBAR(); pb[1] = pk(SC[10], SC[11]); SBAR(); \
    e14 = __builtin_amdgcn_exp2f(SC[14]); e15 = __builtin_amdgcn_exp2f(SC[15]); SBAR(); } while (0)
#define TILE(CUR, PREV, NXT, jload) do { \
    STEP(sa, sb, CUR.k0, CUR.k1, q[1][0], q[1][1], o3, PREV.v1, o0, CUR.v0); LDKV(PREV, jload); SBAR(); \
    STEP(sb, sa, CUR.k0, CUR.k1, q[2][0], q[2][1], o0, CUR.v1, o1, CUR.v0); \
    STEP(sa, sb, CUR.k0, CUR.k1, q[3][0], q[3][1], o1, CUR.v1, o2, CUR.v0); \
    STEP(sb, sa, NXT.k0, NXT.k1, q[0][0], q[0][1], o2, CUR.v1, o3, CUR.v0); } while (0)
        for (int j = 0; j < 16; j += 4) { TILE(a, d, b, j + 3); TILE(b, a, cc, j + 4); TILE(cc, b, d, j + 5); TILE(d, cc, a, j + 6); }
        pb[2] = pk(e12, e13); pb[3] = pk(e14, e15);
        o3 = mfma(d.v1, pb, o3);
#undef TILE
#undef STEP
#undef EX
        __builtin_amdgcn_s_setprio(3);
        STAMP(c, 9);
        const Ctx c = fresh(c0);
        const unsigned voff = c.lane * 16;
        bool bad = false;
        {
            const f32x16* ot[4] = {&o0, &o1, &o2, &o3};
#pragma unroll
            for (int t = 0; t < 4; ++t) {
                const float l = (*ot[t])[12];
                if (c.hi) {
                    bad |= !(l < L_LIMIT);
                    sts64(stat + (t * 4 + c.wid) * 256 + c.r32 * 8, u32x2{__float_as_uint(ref[t]), __float_as_uint(ref[t] + __builtin_amdgcn_logf(l))});
                }
                if (t != c.wid) {
                    const unsigned dst = comb + (t * 3 + (c.wid > t ? c.wid - 1 : c.wid)) * 4096 + voff;
#pragma unroll
                    for (int g = 0; g < 4; ++g) sts128(dst + g * 1024, quad(*ot[t], g));
                }
            }
        }
        f32x16 o = c.wid == 0 ? o0 : c.wid == 1 ? o1 : c.wid == 2 ? o2 : o3;
        lds_bar();
        {
            float m[4], e[4];
#pragma unroll
            for (int v = 0; v < 4; ++v) { const u32x2 s = *(const LAS u32x2*)(size_t)(stat + (c.wid * 4 + v) * 256 + c.r32 * 8); m[v] = __uint_as_float(s[0]); e[v] = __uint_as_float(s[1]); }
            const float top = fmaxf(fmaxf(e[0], e[1]), fmaxf(e[2], e[3]));
            const float mine = c.wid == 0 ? m[0] : c.wid == 1 ? m[1] : c.wid == 2 ? m[2] : m[3];
            o *= __builtin_amdgcn_exp2f(mine - top);
#pragma unroll
            for (int v = 0; v < 3; ++v) {
                const float mv = v == 0 ? (c.wid == 0 ? m[1] : m[0]) : v == 1 ? (c.wid <= 1 ? m[2] : m[1]) : (c.wid <= 2 ? m[3] : m[2]);
                const float cv = __builtin_amdgcn_exp2f(mv - top);
#pragma unroll
                for (int g = 0; g < 4; ++g) {
                    const u32x4 p = lds128(comb + (c.wid * 3 + v) * 4096 + g * 1024 + voff);
#pragma unroll
                    for (int i = 0; i < 4; ++i) o[4 * g + i] = fmaf(__uint_as_float(p[i]), cv, o[4 * g + i]);
                }
            }
        }
        o *= __builtin_amdgcn_rcpf(upper_val(o[12]));
        float chk = 0.f;
#pragma unroll
        for (int i = 0; i < 16; ++i) chk = fmaf(o[i], 0.f, chk);
        bad |= !(chk == 0.f);
        u32x4 f0 = pack_lo(o), f1 = pack_hi(o);
        if (c.hi) { f1[2] = 0; f1[3] = 0; }
        const rsrc_t ro = make_rsrc(ws + WS_OA + (size_t)((c.rb * 4 + c.wid) * 16 + c.h) * 2048, 2048);
        st128_dev(f0, ro, voff, 0); st128_dev(f1, ro, voff + 1024, 0);
        if (!exact && __any(bad) && c.lane == 0) *(LAS unsigned*)(size_t)flagw = 1;
        lds_bar();
        if (exact || __builtin_amdgcn_readfirstlane(*(const LAS unsigned*)(size_t)flagw) == 0) break;
#pragma unroll
        for (int t = 0; t < 4; ++t) if (c.hi) { q[t][1][2] = 0; q[t][1][3] = 0; }
    }
#undef LDKV
}

DI void branch_b(const Ctx& c, const u16* x, const u16* w, const u16* wphi, char* ws, u64& nonce) {
    u64 epoch, epoch_a; epoch_request(epoch, ws + WS_EPOCH_B); epoch_request(epoch_a, ws + WS_EPOCH_A);
    const unsigned voff = c.lane * 16;
    unsigned we[16];
    {
        const int f = c.r32 < 28 ? c.r32 : c.r32 - 4;
        const u16* p = wphi + c.h * 784 + f;
#pragma unroll
        for (int i = 0; i < 16; ++i) { const int d = 8 * (i >> 2) + 4 * c.hi + (i & 3); we[i] = p[(d < 28 ? d : d - 4) * 28]; }
    }
    asm volatile("s_waitcnt lgkmcnt(0)" : "+s"(epoch), "+s"(epoch_a) :: "memory");
    nonce = epoch + 1;
    await(c, ws + WS_FG + (size_t)(blockIdx.x - 256) * 32, 3, epoch_a + 1, 24);
    u32x4 wp[2];
#pragma unroll
    for (int i = 0; i < 8; ++i) wp[i >> 2][i & 3] = we[2 * i] | (we[2 * i + 1] << 16);
    asm volatile("" : "+v"(wp[0]), "+v"(wp[1]));
    for (int i = 0; i < BDELAY; ++i) __builtin_amdgcn_s_sleep(2);
    __builtin_amdgcn_s_setprio(2);
    STAMP(c, 1);
    u32x4 eq[4];
    const f32x16 zero = {};
    {
        static_assert(BAHEAD >= 2 && BAHEAD <= 3, "slot reuse distance");
        constexpr unsigned SLOT = 12288;
        const rsrc_t rx = make_rsrc(x, X_BYTES), rw = make_rsrc(w, W_BYTES);
        unsigned vw[3];
#pragma unroll
        for (int j = 0; j < 3; ++j) { const int p = 64 * j + c.lane, kr = p / 12, rem = p % 12; vw[j] = (16 * c.wid + kr) * WROW + (rem >> 2) * XROW + c.h * 56 + (rem & 3) * 16; }
        const unsigned vx = (c.rb * 128 + 32 * c.wid + (c.lane >> 3)) * XROW + (c.lane & 7) * 16;
        u32x4 xr[28];
        auto issue = [&](int ch) {
            const unsigned k = chunk_at(c, ch);
#pragma unroll
            for (int j = 0; j < 3; ++j) dma16(rw, c.lds + (ch % 5) * SLOT + c.wid * 3072 + j * 1024, vw[j], k * 64 * WROW);
#pragma unroll
            for (int i = 0; i < 4; ++i) ld128_async(xr[4 * ch + i], rx, vx, k * 128 + i * 8 * XROW);
        };
#pragma unroll
        for (int ch = 0; ch < BAHEAD; ++ch) issue(ch);
        STAMP(c, 2);
        const unsigned bounce = c.lds + 61440 + c.wid * 4096;
        unsigned xo[4]; x_frag_offsets(c, xo);
        const unsigned wo = c.lds + (8 * c.hi + ((c.lane & 15) >> 2)) * 192 + 32 * ((c.lane >> 4) & 1) + 8 * (c.lane & 3);
        f32x16 aq = {}, ak = {}, av = {};
        u32x4 xf[2][4], wf[2][12];
        auto step = [&](int set, int s) {
            aq = mfma(wf[set][3 * s], xf[set][s], aq); ak = mfma(wf[set][3 * s + 1], xf[set][s], ak); av = mfma(xf[set][s], wf[set][3 * s + 2], av);
        };
#pragma unroll
        for (int p = 0; p <= 7; ++p) {
            if (p < 7) {
                constexpr int A = BAHEAD - 1;
                if (p + A <= 6) landed4<7 * A>(xr, p); else if (p == 3) landed4<21>(xr, p); else if (p == 4) landed4<14>(xr, p); else if (p == 5) landed4<7>(xr, p); else landed4<0>(xr, p);
                x_bounce(c, bounce, xr, p);
            }
            SBAR();
            if (p > 0) { step((p - 1) & 1, 0); step((p - 1) & 1, 1); }
            SBAR();
            if (p < 7) {
                asm volatile("s_barrier" ::: "memory");
#pragma unroll
                for (int s = 0; s < 4; ++s) {
                    const unsigned wa = wo + (p % 5) * SLOT + s * 3072;
                    xf[p & 1][s] = lds128(bounce + xo[s]);
                    wf[p & 1][3 * s] = tr_frag(wa, wa + 768); wf[p & 1][3 * s + 1] = tr_frag(wa + 64, wa + 64 + 768); wf[p & 1][3 * s + 2] = tr_frag(wa + 128, wa + 128 + 768);
                }
            }
            SBAR();
            if (p > 0) { step((p - 1) & 1, 2); step((p - 1) & 1, 3); }
            SBAR();
            if (p + BAHEAD <= 6) issue(p + BAHEAD);
        }
        STAMP(c, 3);
        f32x16 kv0, kv1;
        {
            u32x4 k0 = pack_lo(ak), k1 = pack_hi(ak);
            if (c.hi) { k1[2] = 0; k1[3] = 0; }
            f32x16 p = mfma(wp[0], k0, zero); p = mfma(wp[1], k1, p);
            const float mx = both_max(max16(p)) * LOG2E, mn = both_min(min16(p)) * LOG2E;
            f32x16 ep, en;
#pragma unroll
            for (int i = 0; i < 16; ++i) { ep[i] = __builtin_amdgcn_exp2f(fmaf(p[i], LOG2E, -mx)); en[i] = __builtin_amdgcn_exp2f(fmaf(p[i], -LOG2E, mn)); }
            const float live = c.hi ? 0.f : 1.f;
            float sp = (ep[12] + ep[13] + ep[14] + ep[15]) * live, sn = (en[12] + en[13] + en[14] + en[15]) * live;
#pragma unroll
            for (int i = 0; i < 12; ++i) { sp += ep[i]; sn += en[i]; }
            const float rp = __builtin_amdgcn_rcpf(both_sum(sp)), rn = __builtin_amdgcn_rcpf(both_sum(sn));
            const float rp3 = rp * live, rn3 = rn * live;
            const unsigned img = c.lds + 36864 + c.wid * 4352, wr = img + c.r32 * 136 + c.hi * 8;
#pragma unroll
            for (int g = 0; g < 4; ++g) {
                const float a = g == 3 ? rp3 : rp, b = g == 3 ? rn3 : rn;
                sts64(wr + g * 16, u32x2{pk(ep[4 * g] * a, ep[4 * g + 1] * a), pk(ep[4 * g + 2] * a, ep[4 * g + 3] * a)});
                sts64(wr + 64 + g * 16, u32x2{pk(en[4 * g] * b, en[4 * g + 1] * b), pk(en[4 * g + 2] * b, en[4 * g + 3] * b)});
            }
            u32x4 v0 = pack_lo(av), v1 = pack_hi(av);
            if (c.r32 >= 28) { const unsigned v = c.r32 == 28 ? 0x3F803F80u : 0u; v0 = u32x4{v, v, v, v}; v1 = v0; }
            const unsigned rd = img + (4 * c.hi + ((c.lane & 15) >> 2)) * 136 + 32 * ((c.lane >> 4) & 1) + 8 * (c.lane & 3);
            kv0 = mfma(tr_frag(rd, rd + 8 * 136), v0, zero); kv0 = mfma(tr_frag(rd + 16 * 136, rd + 24 * 136), v1, kv0);
            kv1 = mfma(tr_frag(rd + 64, rd + 64 + 8 * 136), v0, zero); kv1 = mfma(tr_frag(rd + 64 + 16 * 136, rd + 64 + 24 * 136), v1, kv1);
        }
        lds_bar();
#pragma unroll
        for (int g = 0; g < 4; ++g) { sts128(c.lds + c.wid * 8192 + g * 1024 + voff, quad(kv0, g)); sts128(c.lds + c.wid * 8192 + 4096 + g * 1024 + voff, quad(kv1, g)); }
        lds_bar();
        const rsrc_t ro = make_rsrc(ws + WS_KVP + (size_t)(c.h * 16 + c.rb) * 8192, 8192);
#pragma unroll
        for (int i = 0; i < 2; ++i) {
            const unsigned off = (2 * c.wid + i) * 1024 + voff;
            f32x4 s = __builtin_bit_cast(f32x4, lds128(c.lds + off));
#pragma unroll
            for (int v = 1; v < 4; ++v) s += __builtin_bit_cast(f32x4, lds128(c.lds + v * 8192 + off));
            st128_dev(__builtin_bit_cast(u32x4, s), ro, off, 0);
        }
        STAMP(c, 4);
        {
            u32x4 q0 = pack_lo(aq), q1 = pack_hi(aq);
            if (c.hi) { q1[2] = 0; q1[3] = 0; }
            f32x16 p = mfma(wp[0], q0, zero); p = mfma(wp[1], q1, p);
            const float mx = both_max(max16(p)) * LOG2E, mn = both_min(min16(p)) * LOG2E;
            f32x16 ep, en;
#pragma unroll
            for (int i = 0; i < 16; ++i) { ep[i] = __builtin_amdgcn_exp2f(fmaf(p[i], LOG2E, -mx)); en[i] = __builtin_amdgcn_exp2f(fmaf(p[i], -LOG2E, mn)); }
            eq[0] = pack_lo(ep); eq[1] = pack_hi(ep); eq[2] = pack_lo(en); eq[3] = pack_hi(en);
            asm volatile("" : "+v"(eq[0]), "+v"(eq[1]), "+v"(eq[2]), "+v"(eq[3]));
        }
        wait_vm<0>();
        publish(c, ws + WS_FP + (size_t)((c.h * 4 + c.wid) * 16 + c.rb) * 8, nonce);
        STAMP(c, 5);
    }
    await(c, ws + WS_FP + (size_t)(c.h * 4 + c.wid) * 128, 15, nonce, SPIN_CAP);
    STAMP(c, 6);
    {
        const rsrc_t rp = make_rsrc(ws + WS_KVP + (size_t)c.h * 16 * 8192, 16 * 8192);
        const unsigned off = 2 * c.wid * 1024 + voff, st = c.lds + 65536;
        u32x4 pa[16], pb[16];
#pragma unroll
        for (int i = 0; i < 16; ++i) { pa[i] = ld128_dev(rp, off, i * 8192); pb[i] = ld128_dev(rp, off + 1024, i * 8192); }
        f32x4 sa = __builtin_bit_cast(f32x4, pa[0]), sb = __builtin_bit_cast(f32x4, pb[0]);
#pragma unroll
        for (int i = 1; i < 16; ++i) { sa += __builtin_bit_cast(f32x4, pa[i]); sb += __builtin_bit_cast(f32x4, pb[i]); }
        u32x4 f = {pk(sa[0], sa[1]), pk(sa[2], sa[3]), pk(sb[0], sb[1]), pk(sb[2], sb[3])};
        if (c.r32 == 29) {
            const unsigned tail = ((c.wid & 1) && c.hi) ? 0u : 0x3F803F80u;
            f = u32x4{0x3F803F80u, 0x3F803F80u, tail, tail};
        }
        sts128(st + c.wid * 1024 + voff, f);
        lds_bar();
        STAMP(c, 7);
        f32x16 np = mfma(lds128(st + voff), eq[0], zero); np = mfma(lds128(st + 1024 + voff), eq[1], np);
        f32x16 nn = mfma(lds128(st + 2048 + voff), eq[2], zero); nn = mfma(lds128(st + 3072 + voff), eq[3], nn);
        const float rp_ = __builtin_amdgcn_rcpf(np[13]), rn_ = __builtin_amdgcn_rcpf(nn[13]);
        const float inv = __builtin_amdgcn_rcpf(fmaf(np[12], rp_, fmaf(nn[12], rn_, 1e-6f)));
        const float ap = upper_val(rp_ * inv), an = upper_val(rn_ * inv);
        f32x16 o;
#pragma unroll
        for (int i = 0; i < 16; ++i) o[i] = fmaf(np[i], ap, nn[i] * an);
        u32x4 f0 = pack_lo(o), f1 = pack_hi(o);
        if (c.hi) { f1[2] = 0; f1[3] = 0; }
        const rsrc_t ro = make_rsrc(ws + WS_OB + (size_t)((c.rb * 4 + c.wid) * 16 + c.h) * 2048, 2048);
        st128_dev(f0, ro, voff, 0); st128_dev(f1, ro, voff + 1024, 0);
    }
}

__global__ __launch_bounds__(256, 2) void fused(const u16* xa, const u16* wa, const u16* woa, const u16* xb, const u16* wb, const u16* wphi, const u16* wob, u16* out, char* ws) {
    Ctx c;
    c.lane = threadIdx.x & 63; c.wid = __builtin_amdgcn_readfirstlane(threadIdx.x >> 6);
    c.hi = c.lane >> 5; c.r32 = c.lane & 31;
    const int b = blockIdx.x & 255, xcd = b & 7, i = b >> 3;
    c.h = (xcd & 1) * 8 + (i & 7); c.rb = (xcd >> 1) * 4 + (i >> 3);
    c.rot = ROT ? (2 * (i >> 3) + (i & 7)) % 7 : 0;
    STAMP(c, 0);
    const bool is_a = blockIdx.x < 256;
    u64 nonce;
    if (is_a) branch_a(fresh(c), xa, wa, ws, nonce);
    else branch_b(fresh(c), xb, wb, wphi, ws, nonce);
    c = fresh(c);
    char* flags = ws + (is_a ? WS_FOA : WS_FOB);
    char* oflag = flags + (size_t)((c.rb * 4 + c.wid) * 16 + c.h) * 8;
    wait_vm<0>();
    publish(c, oflag, nonce);
    STAMP(c, 10);
    if (c.h < 14) {
        out_prefetch(c, is_a ? woa : wob);
        phase_out(c, ws + (is_a ? WS_OA : WS_OB), flags, nonce, out + (is_a ? 0 : DM), ws + (is_a ? WS_EPOCH_A : WS_EPOCH_B));
    }
}

}

extern "C" void kernel_launch(void* const* d_in, const int* in_sizes, int n_in, void* d_out, int out_size, void* d_ws, size_t ws_size, hipStream_t stream) {
    (void)hipFuncSetAttribute((const void*)fused, hipFuncAttributeMaxDynamicSharedMemorySize, LDS_BYTES);
    fused<<<512, 256, LDS_BYTES, stream>>>((const u16*)d_in[0], (const u16*)d_in[1], (const u16*)d_in[2], (const u16*)d_in[3], (const u16*)d_in[4], (const u16*)d_in[5],
                                           (const u16*)d_in[6], (u16*)d_out, (char*)d_ws);
}
```

```cpp
#define DIAG -1
#define DIAGBLK 0
#include <hip/hip_runtime.h>
#include <stdint.h>

namespace {

typedef unsigned short u16;
typedef unsigned long long u64;
typedef __attribute__((ext_vector_type(16))) float f32x16;
typedef __attribute__((ext_vector_type(4))) float f32x4;
typedef __attribute__((ext_vector_type(2))) float f32x2;
typedef __attribute__((ext_vector_type(8))) __bf16 bf16x8;
typedef __attribute__((ext_vector_type(2))) __bf16 bf16x2;
typedef __attribute__((ext_vector_type(4))) unsigned u32x4;
typedef __attribute__((ext_vector_type(2))) unsigned u32x2;
typedef __attribute__((ext_vector_type(4))) short s16x4;
typedef __amdgpu_buffer_rsrc_t rsrc_t;
#define LAS __attribute__((address_space(3)))
#define GAS __attribute__((address_space(1)))
#define DI __device__ __forceinline__
#define SBAR() __builtin_amdgcn_sched_barrier(0)

constexpr int SEQ = 2048, DM = 448;
constexpr unsigned XROW = DM * 2;
constexpr unsigned WROW = 3 * DM * 2;
constexpr unsigned X_BYTES = SEQ * XROW, W_BYTES = DM * WROW, WO_BYTES = DM * XROW;
constexpr size_t WS_EPOCH_A = 0, WS_EPOCH_B = 256;
constexpr size_t WS_FK = 4096;
constexpr size_t WS_FG = WS_FK + 8192;
constexpr size_t WS_FP = WS_FG + 8192;
constexpr size_t WS_FOA = WS_FP + 8192;
constexpr size_t WS_FOB = WS_FOA + 8192;
constexpr size_t WS_KVA = 65536;
constexpr size_t WS_OA = WS_KVA + (4u << 20);
constexpr size_t WS_OB = WS_OA + (2u << 20);
constexpr size_t WS_KVP = WS_OB + (2u << 20);
constexpr int LDS_BYTES = 80 * 1024;
constexpr float LOG2E = 1.4426950408889634f;
constexpr float QSCALE = 0.18898223650461362f * LOG2E;
#define REF_OFF 32.f
constexpr float REF_OFFSET = REF_OFF;
constexpr float L_LIMIT = 18446744073709551616.f;
constexpr int SC1 = 16;
constexpr unsigned SPIN_CAP = 1u << 18;
#define ROT 0
#define BAHEAD 2
#define QEARLY 3
#define QSPLIT 16
#define BDELAY 0

DI f32x16 mfma(u32x4 a, u32x4 b, f32x16 c) {
    return __builtin_amdgcn_mfma_f32_32x32x16_bf16(__builtin_bit_cast(bf16x8, a), __builtin_bit_cast(bf16x8, b), c, 0, 0, 0);
}
DI unsigned pk(float lo, float hi) { f32x2 v = {lo, hi}; return __builtin_bit_cast(unsigned, __builtin_convertvector(v, bf16x2)); }
DI u32x4 pack_lo(const f32x16& x) { return u32x4{pk(x[0], x[1]), pk(x[2], x[3]), pk(x[4], x[5]), pk(x[6], x[7])}; }
DI u32x4 pack_hi(const f32x16& x) { return u32x4{pk(x[8], x[9]), pk(x[10], x[11]), pk(x[12], x[13]), pk(x[14], x[15])}; }
DI u32x4 quad(const f32x16& x, int g) { return u32x4{__float_as_uint(x[4 * g]), __float_as_uint(x[4 * g + 1]), __float_as_uint(x[4 * g + 2]), __float_as_uint(x[4 * g + 3])}; }
DI rsrc_t make_rsrc(const void* p, unsigned bytes) { return __builtin_amdgcn_make_buffer_rsrc((void*)p, 0, bytes, 0x00020000); }
DI u32x4 ld128(rsrc_t rs, unsigned voff, unsigned soff) { return __builtin_amdgcn_raw_buffer_load_b128(rs, voff, soff, 0); }
DI u32x4 ld128_dev(rsrc_t rs, unsigned voff, unsigned soff) { return __builtin_amdgcn_raw_buffer_load_b128(rs, voff, soff, SC1); }
DI void st128_dev(u32x4 v, rsrc_t rs, unsigned voff, unsigned soff) { __builtin_amdgcn_raw_buffer_store_b128(v, rs, voff, soff, SC1); }
DI void dma16(rsrc_t rs, unsigned lds_addr, unsigned voff, unsigned soff) {
    asm volatile("s_mov_b32 m0, %0\n\ts_nop 0\n\tbuffer_load_dwordx4 %1, %2, %3 offen lds" ::"s"(lds_addr), "v"(voff), "s"(rs), "s"(soff) : "memory");
}
DI void ld128_async(u32x4& d, rsrc_t rs, unsigned voff, unsigned soff) {
    asm volatile("buffer_load_dwordx4 %0, %1, %2, %3 offen" : "=v"(d) : "v"(voff), "s"(rs), "s"(soff) : "memory");
}
template <int N> DI void wait_vm() { asm volatile("s_waitcnt vmcnt(%0)" ::"n"(N) : "memory"); }
template <int N> DI void wait_bar() { asm volatile("s_waitcnt vmcnt(%0) lgkmcnt(0)\n\ts_barrier" ::"n"(N) : "memory"); }
DI void lds_bar() { asm volatile("s_waitcnt lgkmcnt(0)\n\ts_barrier" ::: "memory"); }
DI u32x4 lds128(unsigned a) { return *(const LAS u32x4*)(size_t)a; }
DI void sts128(unsigned a, u32x4 v) { *(LAS u32x4*)(size_t)a = v; }
DI void sts64(unsigned a, u32x2 v) { *(LAS u32x2*)(size_t)a = v; }
DI u32x2 tr64(unsigned a) { return __builtin_bit_cast(u32x2, __builtin_amdgcn_ds_read_tr16_b64_v4i16((LAS s16x4*)(size_t)a)); }
DI u32x4 tr_frag(unsigned a0, unsigned a1) { u32x2 lo = tr64(a0), hi = tr64(a1); return u32x4{lo[0], lo[1], hi[0], hi[1]}; }
DI float both_max(float v) { auto r = __builtin_amdgcn_permlane32_swap(__float_as_uint(v), __float_as_uint(v), false, false); return fmaxf(__uint_as_float(r[0]), __uint_as_float(r[1])); }
DI float both_min(float v) { auto r = __builtin_amdgcn_permlane32_swap(__float_as_uint(v), __float_as_uint(v), false, false); return fminf(__uint_as_float(r[0]), __uint_as_float(r[1])); }
DI float both_sum(float v) { auto r = __builtin_amdgcn_permlane32_swap(__float_as_uint(v), __float_as_uint(v), false, false); return __uint_as_float(r[0]) + __uint_as_float(r[1]); }
DI float upper_val(float v) { auto r = __builtin_amdgcn_permlane32_swap(__float_as_uint(v), __float_as_uint(v), false, false); return __uint_as_float(r[1]); }
DI float max16(const f32x16& s) {
    float a = fmaxf(fmaxf(s[0], s[1]), s[2]), b = fmaxf(fmaxf(s[3], s[4]), s[5]);
    a = fmaxf(fmaxf(a, s[6]), s[7]); b = fmaxf(fmaxf(b, s[8]), s[9]);
    a = fmaxf(fmaxf(a, s[10]), s[11]); b = fmaxf(fmaxf(b, s[12]), s[13]);
    a = fmaxf(fmaxf(a, s[14]), s[15]);
    return fmaxf(a, b);
}
DI float min16(const f32x16& s) {
    float a = fminf(fminf(s[0], s[1]), s[2]), b = fminf(fminf(s[3], s[4]), s[5]);
    a = fminf(fminf(a, s[6]), s[7]); b = fminf(fminf(b, s[8]), s[9]);
    a = fminf(fminf(a, s[10]), s[11]); b = fminf(fminf(b, s[12]), s[13]);
    a = fminf(fminf(a, s[14]), s[15]);
    return fminf(a, b);
}

#define STAMP(c, id) do { if (id == DIAG || id == 0) { u64 t_; asm volatile("s_memrealtime %0\n\ts_waitcnt lgkmcnt(0)" : "=s"(t_) :: "memory"); *(LAS u64*)(size_t)((c).lds + LDS_BYTES - 512 + (c).wid * 128 + (id == 0 ? 0 : 8)) = t_; } } while (0)

struct Ctx {
    int lane, wid, hi, r32, h, rb, rot;
    static constexpr unsigned lds = 0;
};

DI Ctx fresh(const Ctx& c) {
    Ctx n = c;
    asm volatile("v_mbcnt_lo_u32_b32 %0, -1, 0\n\tv_mbcnt_hi_u32_b32 %0, -1, %0" : "=v"(n.lane));
    n.hi = n.lane >> 5; n.r32 = n.lane & 31;
    return n;
}
DI int chunk_at(const Ctx& c, int p) { const int v = c.rot + p; return v >= 7 ? v - 7 : v; }
DI u64 load_word(const char* p) { return __hip_atomic_load((GAS u64*)(size_t)p, __ATOMIC_RELAXED, __HIP_MEMORY_SCOPE_AGENT); }
DI void store_word(char* p, u64 v) { __hip_atomic_store((GAS u64*)(size_t)p, v, __ATOMIC_RELAXED, __HIP_MEMORY_SCOPE_AGENT); }
DI void epoch_request(u64& v, const char* p) { asm volatile("s_load_dwordx2 %0, %1, 0x0" : "=s"(v) : "s"(p) : "memory"); }
DI void publish(const Ctx& c, char* word, u64 nonce) { if (c.lane == 0) store_word(word, nonce); }
DI void await(const Ctx& c, const char* words, int mask, u64 nonce, unsigned cap) {
    const char* p = words + (c.lane & mask) * 8;
#pragma unroll 1
    for (unsigned spins = 0; spins < cap; ++spins) {
        if (__all(load_word(p) == nonce)) break;
        __builtin_amdgcn_s_sleep(2);
    }
    __builtin_amdgcn_fence(__ATOMIC_ACQUIRE, "wavefront");
    asm volatile("" ::: "memory");
}

DI void x_frag_offsets(const Ctx& c, unsigned (&xo)[4]) {
#pragma unroll
    for (int s = 0; s < 4; ++s) xo[s] = c.r32 * 128 + (((2 * s + c.hi) ^ ((c.r32 >> 1) & 7)) << 4);
}
DI unsigned x_swz(const Ctx& c, int odd) { return ((c.lane & 7) ^ (4 * odd + (c.lane >> 4))) << 4; }
DI void x_bounce(const Ctx& c, unsigned buf, const u32x4 (&xr)[28], int ch) {
    const unsigned w0 = buf + (c.lane >> 3) * 128 + x_swz(c, 0), w1 = buf + (c.lane >> 3) * 128 + x_swz(c, 1);
    sts128(w0, xr[4 * ch]); sts128(w1 + 1024, xr[4 * ch + 1]); sts128(w0 + 2048, xr[4 * ch + 2]); sts128(w1 + 3072, xr[4 * ch + 3]);
}

DI void out_prefetch(const Ctx& c, const u16* wo) {
    const int cg = c.h % 7, row = c.lane >> 3;
    const rsrc_t rw = make_rsrc(wo, WO_BYTES);
    const unsigned vw = (112 * c.wid + row) * XROW + cg * 128 + (((c.lane & 7) ^ (((row >> 1) & 1) << 2)) << 4);
#pragma unroll
    for (int j = 0; j < 14; ++j) dma16(rw, c.lds + (14 * c.wid + j) * 1024, vw, 8 * j * XROW);
}
DI void phase_out(const Ctx& c, const char* ofr, const char* flags, u64 nonce, u16* out, char* epoch) {
    const int cg = c.h % 7, rh = c.h / 7, rt = c.wid & 1, kh = c.wid >> 1;
    const int qt = c.rb * 4 + rh * 2 + rt;
    await(c, flags + (qt * 16 + kh * 8) * 8, 7, nonce, SPIN_CAP);
    STAMP(c, 11);
    const rsrc_t ro = make_rsrc(ofr + (size_t)(qt * 16 + kh * 8) * 2048, 16384);
    u32x4 of[16];
#pragma unroll
    for (int i = 0; i < 16; ++i) of[i] = ld128_dev(ro, c.lane * 16 + (i & 3) * 1024, (i >> 2) * 4096);
    wait_bar<16>();
    const int q = (c.lane & 15) >> 2;
    const unsigned col = 32 * ((c.lane >> 4) & 1) + 8 * (c.lane & 3);
    const unsigned rbase = c.lds + (224 * kh + 4 * c.hi + q) * 128;
    const unsigned a0 = rbase + (col ^ ((q >> 1) << 6)), a1 = rbase + ((col + 64) ^ ((q >> 1) << 6));
    const unsigned back = c.hi ? 4 * 128 : 0;
    f32x16 acc0 = {}, acc1 = {};
#pragma unroll
    for (int i = 0; i < 16; ++i) {
        const unsigned ro0 = ((i >> 1) * 28 + (i & 1) * 16) * 128, ro1 = ro0 + 8 * 128;
        const unsigned adj = (i & 1) ? back : 0;
        acc0 = mfma(tr_frag(a0 + ro0, a0 + ro1 - adj), of[i], acc0);
        acc1 = mfma(tr_frag(a1 + ro0, a1 + ro1 - adj), of[i], acc1);
    }
    STAMP(c, 12);
    const unsigned xch = c.lds + 57344;
#pragma unroll
    for (int g = 0; g < 4; ++g) sts128(xch + c.wid * 4096 + g * 1024 + c.lane * 16, kh ? quad(acc0, g) : quad(acc1, g));
    f32x16 r = kh ? acc1 : acc0;
    lds_bar();
    if (c.h == 0 && c.rb == 0 && c.wid == 0 && c.lane == 0) store_word(epoch, nonce);
#pragma unroll
    for (int g = 0; g < 4; ++g) {
        const u32x4 p = lds128(xch + (c.wid ^ 2) * 4096 + g * 1024 + c.lane * 16);
#pragma unroll
        for (int i = 0; i < 4; ++i) r[4 * g + i] += __uint_as_float(p[i]);
    }
    const u32x4 lo = pack_lo(r), hi = pack_hi(r);
    auto s0 = __builtin_amdgcn_permlane32_swap(lo[0], lo[2], false, false), s1 = __builtin_amdgcn_permlane32_swap(lo[1], lo[3], false, false);
    auto s2 = __builtin_amdgcn_permlane32_swap(hi[0], hi[2], false, false), s3 = __builtin_amdgcn_permlane32_swap(hi[1], hi[3], false, false);
    char* dst = (char*)out + (size_t)(qt * 32 + c.r32) * (2 * XROW) + cg * 128 + kh * 64 + c.hi * 16;
    *(u32x4*)dst = u32x4{s0[0], s1[0], s0[1], s1[1]};
    *(u32x4*)(dst + 32) = u32x4{s2[0], s3[0], s2[1], s3[1]};
}

DI void mfma_pair(int isv, u32x4 xf, u32x4 w0, u32x4 w1, f32x16& acc0, f32x16& acc1) {
    asm("s_cmp_lg_u32 %5, 0\n\ts_cbranch_scc1 1f\n\t"
        "v_mfma_f32_32x32x16_bf16 %0, %3, %2, %0\n\tv_mfma_f32_32x32x16_bf16 %1, %4, %2, %1\n\ts_branch 2f\n"
        "1:\n\tv_mfma_f32_32x32x16_bf16 %0, %2, %3, %0\n\tv_mfma_f32_32x32x16_bf16 %1, %2, %4, %1\n2:"
        : "+v"(acc0), "+v"(acc1) : "v"(xf), "v"(w0), "v"(w1), "s"(isv) : "scc");
}
template <int N> DI void landed4(u32x4 (&r)[28], int ch) {
    asm volatile("s_waitcnt vmcnt(%4)" : "+v"(r[4 * ch]), "+v"(r[4 * ch + 1]), "+v"(r[4 * ch + 2]), "+v"(r[4 * ch + 3]) : "n"(N) : "memory");
}
DI void kv_gemm(const Ctx& c, int isv, rsrc_t rx, rsrc_t rw, f32x16& acc0, f32x16& acc1, u32x4 (&xf)[28]) {
    const unsigned bounce = c.lds + 57344 + c.wid * 4096;
    const int row = c.lane >> 3;
    const unsigned piece = (c.lane & 7) ^ (((row >> 1) & 1) << 2);
    const unsigned vw = piece < 7 ? (16 * c.wid + row) * WROW + (1 + isv) * XROW + (c.h >> 1) * 112 + (piece << 4) : 0x80000000u;
    const unsigned vx = (c.rb * 128 + 32 * c.wid + row) * XROW + (c.lane & 7) * 16;
    u32x4 xr[28];
    auto issue = [&](int ch) {
        const unsigned k = chunk_at(c, ch);
        dma16(rw, c.lds + ch * 8192 + c.wid * 2048, vw, k * 64 * WROW);
        dma16(rw, c.lds + ch * 8192 + c.wid * 2048 + 1024, vw, (k * 64 + 8) * WROW);
#pragma unroll
        for (int i = 0; i < 4; ++i) ld128_async(xr[4 * ch + i], rx, vx, k * 128 + i * 8 * XROW);
    };
    issue(0); issue(1); issue(2); issue(3);
    asm volatile("s_waitcnt lgkmcnt(0)" ::: "memory");
    STAMP(c, 1);
    unsigned xo[4]; x_frag_offsets(c, xo);
    const int q = (c.lane & 15) >> 2;
    const unsigned col = 32 * ((c.lane >> 4) & 1) + 8 * (c.lane & 3);
    const unsigned w0 = c.lds + (8 * c.hi + q) * 128 + (col ^ ((q >> 1) << 6)), w1 = c.lds + (8 * c.hi + q) * 128 + ((col + 56) ^ ((q >> 1) << 6));
    acc0 = f32x16{}; acc1 = f32x16{};
    asm volatile("s_nop 1" : "+v"(acc0), "+v"(acc1));
    u32x4 wf[2][8];
#pragma unroll
    for (int p = 0; p <= 7; ++p) {
        if (p < 7) {
            if (p <= 3) landed4<18>(xr, p); else if (p == 4) landed4<12>(xr, p); else if (p == 5) landed4<6>(xr, p); else landed4<0>(xr, p);
            x_bounce(c, bounce, xr, p);
        }
        SBAR();
        if (p > 0) {
            mfma_pair(isv, xf[4 * p - 4], wf[(p - 1) & 1][0], wf[(p - 1) & 1][1], acc0, acc1);
            mfma_pair(isv, xf[4 * p - 3], wf[(p - 1) & 1][2], wf[(p - 1) & 1][3], acc0, acc1);
        }
        SBAR();
        if (p < 7) {
            asm volatile("s_barrier" ::: "memory");
            if (p == 0) STAMP(c, 2);
            if (p == 6) STAMP(c, 3);
#pragma unroll
            for (int s = 0; s < 4; ++s) {
                const unsigned o = p * 8192 + s * 2048;
                xf[4 * p + s] = lds128(bounce + xo[s]);
                wf[p & 1][2 * s] = tr_frag(w0 + o, w0 + o + 512); wf[p & 1][2 * s + 1] = tr_frag(w1 + o, w1 + o + 512);
            }
        }
        SBAR();
        if (p > 0) {
            mfma_pair(isv, xf[4 * p - 2], wf[(p - 1) & 1][4], wf[(p - 1) & 1][5], acc0, acc1);
            mfma_pair(isv, xf[4 * p - 1], wf[(p - 1) & 1][6], wf[(p - 1) & 1][7], acc0, acc1);
        }
        SBAR();
        if (p + 4 <= 6) issue(p + 4);
    }
    asm volatile("s_nop 11" : "+v"(acc0), "+v"(acc1));
}

struct KV { u32x4 k0, k1, v0, v1; };

DI void set_reference(u32x4& q1, float nm, int hi) {
    const unsigned a = __float_as_uint(nm) & 0xFFFF0000u;
    const float r1 = nm - __uint_as_float(a);
    const unsigned b = __float_as_uint(r1) & 0xFFFF0000u;
    const float r2 = r1 - __uint_as_float(b);
    if (hi) { q1[2] = (a >> 16) | b; q1[3] = __float_as_uint(r2) >> 16; }
}

DI void branch_a(const Ctx& c, const u16* x, const u16* w, char* ws, u64& nonce) {
    u64 epoch; epoch_request(epoch, ws + WS_EPOCH_A);
    const rsrc_t rx = make_rsrc(x, X_BYTES), rw = make_rsrc(w, W_BYTES);
    const unsigned voff = c.lane * 16;
    u32x4 q[4][2];
    const rsrc_t rk = make_rsrc(ws + WS_KVA + (size_t)(c.h * 64 + c.wid * 16) * 4096, 16 * 4096);
#define LDKV(X, j) do { X.v1 = ld128_dev(rk, voff + 3072, (j) * 4096); X.v0 = ld128_dev(rk, voff + 2048, (j) * 4096); \
                        X.k1 = ld128_dev(rk, voff + 1024, (j) * 4096); X.k0 = ld128_dev(rk, voff, (j) * 4096); } while (0)
    KV a, b, cc, d;
    {
        u32x4 xf[28];
        f32x16 acc0, acc1;
        const int isv = c.h & 1;
        kv_gemm(c, isv, rx, rw, acc0, acc1, xf);
        asm volatile("" : "+s"(epoch));
        u32x4 f[4] = {pack_lo(acc0), pack_hi(acc0), pack_lo(acc1), pack_hi(acc1)};
        {
            const bool all = isv && c.r32 >= 28, tail = isv ? all : (bool)c.hi;
            const unsigned va = c.r32 == 28 ? 0x3F803F80u : 0u, v2 = isv ? va : 0x3F803F80u, v3 = isv ? va : 0x00003F80u;
#pragma unroll
            for (int i = 0; i < 4; ++i) {
                f[i][0] = all ? va : f[i][0]; f[i][1] = all ? va : f[i][1];
                if (i & 1) { f[i][2] = tail ? v2 : f[i][2]; f[i][3] = tail ? v3 : f[i][3]; }
                else       { f[i][2] = all ? va : f[i][2]; f[i][3] = all ? va : f[i][3]; }
            }
        }
        const int kt = c.rb * 4 + c.wid;
        const rsrc_t ro = make_rsrc(ws + WS_KVA + (size_t)((c.h & 14) * 64 + kt) * 4096 + isv * 2048, 64 * 4096 + 2048);
        st128_dev(f[0], ro, voff, 0); st128_dev(f[1], ro, voff + 1024, 0); st128_dev(f[2], ro, voff, 64 * 4096); st128_dev(f[3], ro, voff + 1024, 64 * 4096);
        STAMP(c, 4);
        const unsigned wq = c.lds, xb = c.lds + 32768;
        const unsigned vwq = (16 * c.wid + (c.lane >> 2)) * WROW + c.h * 56 + (c.lane & 3) * 16;
        nonce = epoch + 1;
        publish(c, ws + WS_FG + (size_t)(blockIdx.x * 4 + c.wid) * 8, nonce);
        lds_bar();
#pragma unroll
        for (int j = 0; j < QEARLY; ++j) dma16(rw, wq + j * 4096 + c.wid * 1024, vwq, chunk_at(c, j) * 64 * WROW);
        wait_vm<QEARLY + 1>();
        publish(c, ws + WS_FK + (size_t)(((c.h >> 1) * 64 + kt) * 2 + isv) * 8, nonce);
        STAMP(c, 5);
#pragma unroll
        for (int j = QEARLY; j < 7; ++j) dma16(rw, wq + j * 4096 + c.wid * 1024, vwq, chunk_at(c, j) * 64 * WROW);
        wait_bar<QEARLY == 7 ? 1 : 0>();
        const unsigned wo = wq + (8 * c.hi + ((c.lane & 15) >> 2)) * 64 + 32 * ((c.lane >> 4) & 1) + 8 * (c.lane & 3);
        f32x16 acc = {};
#pragma unroll
        for (int i = 0; i < 28; ++i) {
            if (i == QSPLIT) {
                STAMP(c, 6);
                await(c, ws + WS_FK + (size_t)((c.h >> 1) * 64 + c.wid * 16) * 16, 31, nonce, SPIN_CAP);
                STAMP(c, 7);
                LDKV(a, 0); LDKV(b, 1); LDKV(cc, 2);
            }
            acc = mfma(tr_frag(wo + i * 1024, wo + i * 1024 + 256), xf[i], acc);
        }
        acc *= QSCALE;
        u32x4 q0 = pack_lo(acc), q1 = pack_hi(acc);
        if (c.hi) { q1[2] = 0; q1[3] = 0; }
        sts128(xb + c.wid * 2048 + voff, q0); sts128(xb + c.wid * 2048 + 1024 + voff, q1);
        lds_bar();
#pragma unroll
        for (int t = 0; t < 4; ++t) { q[t][0] = lds128(xb + t * 2048 + voff); q[t][1] = lds128(xb + t * 2048 + 1024 + voff); }
    }
    const Ctx c0 = c;
    const unsigned comb = c.lds, stat = c.lds + 49152, flagw = c.lds + 57344;
    if (c.wid == 0 && c.lane == 0) *(LAS unsigned*)(size_t)flagw = 0;
    const f32x16 zero = {};
    for (int exact = 0;; ++exact) {
        float ref[4];
        if (exact) { LDKV(a, 0); LDKV(b, 1); LDKV(cc, 2); }
#pragma unroll
        for (int t = 0; t < 4; ++t) { f32x16 s = mfma(a.k0, q[t][0], zero); s = mfma(a.k1, q[t][1], s); ref[t] = max16(s); }
        if (exact) {
            for (int j = 1; j < 16; ++j) {
                const u32x4 k0 = ld128_dev(rk, voff, j * 4096), k1 = ld128_dev(rk, voff + 1024, j * 4096);
#pragma unroll
                for (int t = 0; t < 4; ++t) { f32x16 s = mfma(k0, q[t][0], zero); s = mfma(k1, q[t][1], s); ref[t] = fmaxf(ref[t], max16(s)); }
            }
        }
#pragma unroll
        for (int t = 0; t < 4; ++t) { ref[t] = both_max(ref[t]) + (exact ? 0.f : REF_OFFSET); set_reference(q[t][1], -ref[t], c.hi); }
        STAMP(c, 8);
        f32x16 o0 = {}, o1 = {}, o2 = {}, o3 = {};
        f32x16 sa, sb; u32x4 pa, pb = {};
        float e12 = 0.f, e13 = 0.f, e14 = 0.f, e15 = 0.f;
        d.v1 = u32x4{};
        sa = mfma(a.k0, q[0][0], zero); sa = mfma(a.k1, q[0][1], sa);
#define EX(S, i) S[i] = __builtin_amdgcn_exp2f(S[i])
#define STEP(SC, SN, KN0, KN1, QN0, QN1, OP, VP1, OC, VC0) do { \
    SN = mfma(KN0, QN0, zero); SBAR(); \
    pb[2] = pk(e12, e13); SBAR(); EX(SC, 0); SBAR(); pb[3] = pk(e14, e15); SBAR(); EX(SC, 1); EX(SC, 2); EX(SC, 3); SBAR(); \
    OP = mfma(VP1, pb, OP); SBAR(); \
    EX(SC, 4); EX(SC, 5); SBAR(); pa[0] = pk(SC[0], SC[1]); SBAR(); EX(SC, 6); SBAR(); pa[1] = pk(SC[2], SC[3]); SBAR(); EX(SC, 7); SBAR(); \
    SN = mfma(KN1, QN1, SN); SBAR(); \
    EX(SC, 8); SBAR(); pa[2] = pk(SC[4], SC[5]); SBAR(); EX(SC, 9); SBAR(); pa[3] = pk(SC[6], SC[7]); SBAR(); EX(SC, 10); EX(SC, 11); SBAR(); \
    OC = mfma(VC0, pa, OC); SBAR(); \
    e12 = __builtin_amdgcn_exp2f(SC[12]); SBAR(); pb[0] = pk(SC[8], SC[9]); SBAR(); e13 = __builtin_amdgcn_exp2f(SC[13]); SBAR(); pb[1] = pk(SC[10], SC[11]); SBAR(); \
    e14 = __builtin_amdgcn_exp2f(SC[14]); e15 = __builtin_amdgcn_exp2f(SC[15]); SBAR(); } while (0)
#define TILE(CUR, PREV, NXT, jload) do { \
    STEP(sa, sb, CUR.k0, CUR.k1, q[1][0], q[1][1], o3, PREV.v1, o0, CUR.v0); LDKV(PREV, jload); SBAR(); \
    STEP(sb, sa, CUR.k0, CUR.k1, q[2][0], q[2][1], o0, CUR.v1, o1, CUR.v0); \
    STEP(sa, sb, CUR.k0, CUR.k1, q[3][0], q[3][1], o1, CUR.v1, o2, CUR.v0); \
    STEP(sb, sa, NXT.k0, NXT.k1, q[0][0], q[0][1], o2, CUR.v1, o3, CUR.v0); } while (0)
        for (int j = 0; j < 16; j += 4) { TILE(a, d, b, j + 3); TILE(b, a, cc, j + 4); TILE(cc, b, d, j + 5); TILE(d, cc, a, j + 6); }
        pb[2] = pk(e12, e13); pb[3] = pk(e14, e15);
        o3 = mfma(d.v1, pb, o3);
#undef TILE
#undef STEP
#undef EX
        __builtin_amdgcn_s_setprio(3);
        STAMP(c, 9);
        const Ctx c = fresh(c0);
        const unsigned voff = c.lane * 16;
        bool bad = false;
        {
            const f32x16* ot[4] = {&o0, &o1, &o2, &o3};
#pragma unroll
            for (int t = 0; t < 4; ++t) {
                const float l = (*ot[t])[12];
                if (c.hi) {
                    bad |= !(l < L_LIMIT);
                    sts64(stat + (t * 4 + c.wid) * 256 + c.r32 * 8, u32x2{__float_as_uint(ref[t]), __float_as_uint(ref[t] + __builtin_amdgcn_logf(l))});
                }
                if (t != c.wid) {
                    const unsigned dst = comb + (t * 3 + (c.wid > t ? c.wid - 1 : c.wid)) * 4096 + voff;
#pragma unroll
                    for (int g = 0; g < 4; ++g) sts128(dst + g * 1024, quad(*ot[t], g));
                }
            }
        }
        f32x16 o = c.wid == 0 ? o0 : c.wid == 1 ? o1 : c.wid == 2 ? o2 : o3;
        lds_bar();
        {
            float m[4], e[4];
#pragma unroll
            for (int v = 0; v < 4; ++v) { const u32x2 s = *(const LAS u32x2*)(size_t)(stat + (c.wid * 4 + v) * 256 + c.r32 * 8); m[v] = __uint_as_float(s[0]); e[v] = __uint_as_float(s[1]); }
            const float top = fmaxf(fmaxf(e[0], e[1]), fmaxf(e[2], e[3]));
            const float mine = c.wid == 0 ? m[0] : c.wid == 1 ? m[1] : c.wid == 2 ? m[2] : m[3];
            o *= __builtin_amdgcn_exp2f(mine - top);
#pragma unroll
            for (int v = 0; v < 3; ++v) {
                const float mv = v == 0 ? (c.wid == 0 ? m[1] : m[0]) : v == 1 ? (c.wid <= 1 ? m[2] : m[1]) : (c.wid <= 2 ? m[3] : m[2]);
                const float cv = __builtin_amdgcn_exp2f(mv - top);
#pragma unroll
                for (int g = 0; g < 4; ++g) {
                    const u32x4 p = lds128(comb + (c.wid * 3 + v) * 4096 + g * 1024 + voff);
#pragma unroll
                    for (int i = 0; i < 4; ++i) o[4 * g + i] = fmaf(__uint_as_float(p[i]), cv, o[4 * g + i]);
                }
            }
        }
        o *= __builtin_amdgcn_rcpf(upper_val(o[12]));
        float chk = 0.f;
#pragma unroll
        for (int i = 0; i < 16; ++i) chk = fmaf(o[i], 0.f, chk);
        bad |= !(chk == 0.f);
        u32x4 f0 = pack_lo(o), f1 = pack_hi(o);
        if (c.hi) { f1[2] = 0; f1[3] = 0; }
        const rsrc_t ro = make_rsrc(ws + WS_OA + (size_t)((c.rb * 4 + c.wid) * 16 + c.h) * 2048, 2048);
        st128_dev(f0, ro, voff, 0); st128_dev(f1, ro, voff + 1024, 0);
        if (!exact && __any(bad) && c.lane == 0) *(LAS unsigned*)(size_t)flagw = 1;
        lds_bar();
        if (exact || __builtin_amdgcn_readfirstlane(*(const LAS unsigned*)(size_t)flagw) == 0) break;
#pragma unroll
        for (int t = 0; t < 4; ++t) if (c.hi) { q[t][1][2] = 0; q[t][1][3] = 0; }
    }
#undef LDKV
}

DI void branch_b(const Ctx& c, const u16* x, const u16* w, const u16* wphi, char* ws, u64& nonce) {
    u64 epoch, epoch_a; epoch_request(epoch, ws + WS_EPOCH_B); epoch_request(epoch_a, ws + WS_EPOCH_A);
    const unsigned voff = c.lane * 16;
    unsigned we[16];
    {
        const int f = c.r32 < 28 ? c.r32 : c.r32 - 4;
        const u16* p = wphi + c.h * 784 + f;
#pragma unroll
        for (int i = 0; i < 16; ++i) { const int d = 8 * (i >> 2) + 4 * c.hi + (i & 3); we[i] = p[(d < 28 ? d : d - 4) * 28]; }
    }
    asm volatile("s_waitcnt lgkmcnt(0)" : "+s"(epoch), "+s"(epoch_a) :: "memory");
    nonce = epoch + 1;
    await(c, ws + WS_FG + (size_t)(blockIdx.x - 256) * 32, 3, epoch_a + 1, 24);
    u32x4 wp[2];
#pragma unroll
    for (int i = 0; i < 8; ++i) wp[i >> 2][i & 3] = we[2 * i] | (we[2 * i + 1] << 16);
    asm volatile("" : "+v"(wp[0]), "+v"(wp[1]));
    for (int i = 0; i < BDELAY; ++i) __builtin_amdgcn_s_sleep(2);
    __builtin_amdgcn_s_setprio(2);
    STAMP(c, 1);
    u32x4 eq[4];
    const f32x16 zero = {};
    {
        static_assert(BAHEAD >= 2 && BAHEAD <= 3, "slot reuse distance");
        constexpr unsigned SLOT = 12288;
        const rsrc_t rx = make_rsrc(x, X_BYTES), rw = make_rsrc(w, W_BYTES);
        unsigned vw[3];
#pragma unroll
        for (int j = 0; j < 3; ++j) { const int p = 64 * j + c.lane, kr = p / 12, rem = p % 12; vw[j] = (16 * c.wid + kr) * WROW + (rem >> 2) * XROW + c.h * 56 + (rem & 3) * 16; }
        const unsigned vx = (c.rb * 128 + 32 * c.wid + (c.lane >> 3)) * XROW + (c.lane & 7) * 16;
        u32x4 xr[28];
        auto issue = [&](int ch) {
            const unsigned k = chunk_at(c, ch);
#pragma unroll
            for (int j = 0; j < 3; ++j) dma16(rw, c.lds + (ch % 5) * SLOT + c.wid * 3072 + j * 1024, vw[j], k * 64 * WROW);
#pragma unroll
            for (int i = 0; i < 4; ++i) ld128_async(xr[4 * ch + i], rx, vx, k * 128 + i * 8 * XROW);
        };
#pragma unroll
        for (int ch = 0; ch < BAHEAD; ++ch) issue(ch);
        STAMP(c, 2);
        const unsigned bounce = c.lds + 61440 + c.wid * 4096;
        unsigned xo[4]; x_frag_offsets(c, xo);
        const unsigned wo = c.lds + (8 * c.hi + ((c.lane & 15) >> 2)) * 192 + 32 * ((c.lane >> 4) & 1) + 8 * (c.lane & 3);
        f32x16 aq = {}, ak = {}, av = {};
        u32x4 xf[2][4], wf[2][12];
        auto step = [&](int set, int s) {
            aq = mfma(wf[set][3 * s], xf[set][s], aq); ak = mfma(wf[set][3 * s + 1], xf[set][s], ak); av = mfma(xf[set][s], wf[set][3 * s + 2], av);
        };
#pragma unroll
        for (int p = 0; p <= 7; ++p) {
            if (p < 7) {
                constexpr int A = BAHEAD - 1;
                if (p + A <= 6) landed4<7 * A>(xr, p); else if (p == 3) landed4<21>(xr, p); else if (p == 4) landed4<14>(xr, p); else if (p == 5) landed4<7>(xr, p); else landed4<0>(xr, p);
                x_bounce(c, bounce, xr, p);
            }
            SBAR();
            if (p > 0) { step((p - 1) & 1, 0); step((p - 1) & 1, 1); }
            SBAR();
            if (p < 7) {
                asm volatile("s_barrier" ::: "memory");
#pragma unroll
                for (int s = 0; s < 4; ++s) {
                    const unsigned wa = wo + (p % 5) * SLOT + s * 3072;
                    xf[p & 1][s] = lds128(bounce + xo[s]);
                    wf[p & 1][3 * s] = tr_frag(wa, wa + 768); wf[p & 1][3 * s + 1] = tr_frag(wa + 64, wa + 64 + 768); wf[p & 1][3 * s + 2] = tr_frag(wa + 128, wa + 128 + 768);
                }
            }
            SBAR();
            if (p > 0) { step((p - 1) & 1, 2); step((p - 1) & 1, 3); }
            SBAR();
            if (p + BAHEAD <= 6) issue(p + BAHEAD);
        }
        STAMP(c, 3);
        f32x16 kv0, kv1;
        {
            u32x4 k0 = pack_lo(ak), k1 = pack_hi(ak);
            if (c.hi) { k1[2] = 0; k1[3] = 0; }
            f32x16 p = mfma(wp[0], k0, zero); p = mfma(wp[1], k1, p);
            const float mx = both_max(max16(p)) * LOG2E, mn = both_min(min16(p)) * LOG2E;
            f32x16 ep, en;
#pragma unroll
            for (int i = 0; i < 16; ++i) { ep[i] = __builtin_amdgcn_exp2f(fmaf(p[i], LOG2E, -mx)); en[i] = __builtin_amdgcn_exp2f(fmaf(p[i], -LOG2E, mn)); }
            const float live = c.hi ? 0.f : 1.f;
            float sp = (ep[12] + ep[13] + ep[14] + ep[15]) * live, sn = (en[12] + en[13] + en[14] + en[15]) * live;
#pragma unroll
            for (int i = 0; i < 12; ++i) { sp += ep[i]; sn += en[i]; }
            const float rp = __builtin_amdgcn_rcpf(both_sum(sp)), rn = __builtin_amdgcn_rcpf(both_sum(sn));
            const float rp3 = rp * live, rn3 = rn * live;
            const unsigned img = c.lds + 36864 + c.wid * 4352, wr = img + c.r32 * 136 + c.hi * 8;
#pragma unroll
            for (int g = 0; g < 4; ++g) {
                const float a = g == 3 ? rp3 : rp, b = g == 3 ? rn3 : rn;
                sts64(wr + g * 16, u32x2{pk(ep[4 * g] * a, ep[4 * g + 1] * a), pk(ep[4 * g + 2] * a, ep[4 * g + 3] * a)});
                sts64(wr + 64 + g * 16, u32x2{pk(en[4 * g] * b, en[4 * g + 1] * b), pk(en[4 * g + 2] * b, en[4 * g + 3] * b)});
            }
            u32x4 v0 = pack_lo(av), v1 = pack_hi(av);
            if (c.r32 >= 28) { const unsigned v = c.r32 == 28 ? 0x3F803F80u : 0u; v0 = u32x4{v, v, v, v}; v1 = v0; }
            const unsigned rd = img + (4 * c.hi + ((c.lane & 15) >> 2)) * 136 + 32 * ((c.lane >> 4) & 1) + 8 * (c.lane & 3);
            kv0 = mfma(tr_frag(rd, rd + 8 * 136), v0, zero); kv0 = mfma(tr_frag(rd + 16 * 136, rd + 24 * 136), v1, kv0);
            kv1 = mfma(tr_frag(rd + 64, rd + 64 + 8 * 136), v0, zero); kv1 = mfma(tr_frag(rd + 64 + 16 * 136, rd + 64 + 24 * 136), v1, kv1);
        }
        lds_bar();
#pragma unroll
        for (int g = 0; g < 4; ++g) { sts128(c.lds + c.wid * 8192 + g * 1024 + voff, quad(kv0, g)); sts128(c.lds + c.wid * 8192 + 4096 + g * 1024 + voff, quad(kv1, g)); }
        lds_bar();
        const rsrc_t ro = make_rsrc(ws + WS_KVP + (size_t)(c.h * 16 + c.rb) * 8192, 8192);
#pragma unroll
        for (int i = 0; i < 2; ++i) {
            const unsigned off = (2 * c.wid + i) * 1024 + voff;
            f32x4 s = __builtin_bit_cast(f32x4, lds128(c.lds + off));
#pragma unroll
            for (int v = 1; v < 4; ++v) s += __builtin_bit_cast(f32x4, lds128(c.lds + v * 8192 + off));
            st128_dev(__builtin_bit_cast(u32x4, s), ro, off, 0);
        }
        STAMP(c, 4);
        {
            u32x4 q0 = pack_lo(aq), q1 = pack_hi(aq);
            if (c.hi) { q1[2] = 0; q1[3] = 0; }
            f32x16 p = mfma(wp[0], q0, zero); p = mfma(wp[1], q1, p);
            const float mx = both_max(max16(p)) * LOG2E, mn = both_min(min16(p)) * LOG2E;
            f32x16 ep, en;
#pragma unroll
            for (int i = 0; i < 16; ++i) { ep[i] = __builtin_amdgcn_exp2f(fmaf(p[i], LOG2E, -mx)); en[i] = __builtin_amdgcn_exp2f(fmaf(p[i], -LOG2E, mn)); }
            eq[0] = pack_lo(ep); eq[1] = pack_hi(ep); eq[2] = pack_lo(en); eq[3] = pack_hi(en);
            asm volatile("" : "+v"(eq[0]), "+v"(eq[1]), "+v"(eq[2]), "+v"(eq[3]));
        }
        wait_vm<0>();
        publish(c, ws + WS_FP + (size_t)((c.h * 4 + c.wid) * 16 + c.rb) * 8, nonce);
        STAMP(c, 5);
    }
    await(c, ws + WS_FP + (size_t)(c.h * 4 + c.wid) * 128, 15, nonce, SPIN_CAP);
    STAMP(c, 6);
    {
        const rsrc_t rp = make_rsrc(ws + WS_KVP + (size_t)c.h * 16 * 8192, 16 * 8192);
        const unsigned off = 2 * c.wid * 1024 + voff, st = c.lds + 65536;
        u32x4 pa[16], pb[16];
#pragma unroll
        for (int i = 0; i < 16; ++i) { pa[i] = ld128_dev(rp, off, i * 8192); pb[i] = ld128_dev(rp, off + 1024, i * 8192); }
        f32x4 sa = __builtin_bit_cast(f32x4, pa[0]), sb = __builtin_bit_cast(f32x4, pb[0]);
#pragma unroll
        for (int i = 1; i < 16; ++i) { sa += __builtin_bit_cast(f32x4, pa[i]); sb += __builtin_bit_cast(f32x4, pb[i]); }
        u32x4 f = {pk(sa[0], sa[1]), pk(sa[2], sa[3]), pk(sb[0], sb[1]), pk(sb[2], sb[3])};
        if (c.r32 == 29) {
            const unsigned tail = ((c.wid & 1) && c.hi) ? 0u : 0x3F803F80u;
            f = u32x4{0x3F803F80u, 0x3F803F80u, tail, tail};
        }
        sts128(st + c.wid * 1024 + voff, f);
        lds_bar();
        STAMP(c, 7);
        f32x16 np = mfma(lds128(st + voff), eq[0], zero); np = mfma(lds128(st + 1024 + voff), eq[1], np);
        f32x16 nn = mfma(lds128(st + 2048 + voff), eq[2], zero); nn = mfma(lds128(st + 3072 + voff), eq[3], nn);
        const float rp_ = __builtin_amdgcn_rcpf(np[13]), rn_ = __builtin_amdgcn_rcpf(nn[13]);
        const float inv = __builtin_amdgcn_rcpf(fmaf(np[12], rp_, fmaf(nn[12], rn_, 1e-6f)));
        const float ap = upper_val(rp_ * inv), an = upper_val(rn_ * inv);
        f32x16 o;
#pragma unroll
        for (int i = 0; i < 16; ++i) o[i] = fmaf(np[i], ap, nn[i] * an);
        u32x4 f0 = pack_lo(o), f1 = pack_hi(o);
        if (c.hi) { f1[2] = 0; f1[3] = 0; }
        const rsrc_t ro = make_rsrc(ws + WS_OB + (size_t)((c.rb * 4 + c.wid) * 16 + c.h) * 2048, 2048);
        st128_dev(f0, ro, voff, 0); st128_dev(f1, ro, voff + 1024, 0);
    }
}

__global__ __launch_bounds__(256, 2) void fused(const u16* xa, const u16* wa, const u16* woa, const u16* xb, const u16* wb, const u16* wphi, const u16* wob, u16* out, char* ws) {
    Ctx c;
    c.lane = threadIdx.x & 63; c.wid = __builtin_amdgcn_readfirstlane(threadIdx.x >> 6);
    c.hi = c.lane >> 5; c.r32 = c.lane & 31;
    const int b = blockIdx.x & 255, xcd = b & 7, i = b >> 3;
    c.h = (xcd & 1) * 8 + (i & 7); c.rb = (xcd >> 1) * 4 + (i >> 3);
    c.rot = ROT ? (2 * (i >> 3) + (i & 7)) % 7 : 0;
    STAMP(c, 0);
    const bool is_a = blockIdx.x < 256;
    u64 nonce;
    if (is_a) branch_a(fresh(c), xa, wa, ws, nonce);
    else branch_b(fresh(c), xb, wb, wphi, ws, nonce);
    c = fresh(c);
    char* flags = ws + (is_a ? WS_FOA : WS_FOB);
    char* oflag = flags + (size_t)((c.rb * 4 + c.wid) * 16 + c.h) * 8;
    wait_vm<0>();
    publish(c, oflag, nonce);
    STAMP(c, 10);
    if (c.h < 14) {
        out_prefetch(c, is_a ? woa : wob);
        phase_out(c, ws + (is_a ? WS_OA : WS_OB), flags, nonce, out + (is_a ? 0 : DM), ws + (is_a ? WS_EPOCH_A : WS_EPOCH_B));
    }
    STAMP(c, 13);
    if (blockIdx.x == DIAGBLK && c.wid == 0) {
        const u64 t0 = *(LAS u64*)(size_t)(c.lds + LDS_BYTES - 512), t1 = *(LAS u64*)(size_t)(c.lds + LDS_BYTES - 512 + 8);
        const u64 target = t0 + 10000 + (DIAG < 0 ? 0 : 4 * (t1 - t0));
        while (__builtin_amdgcn_s_memrealtime() < target) __builtin_amdgcn_s_sleep(1);
    }
}

}

extern "C" void kernel_launch(void* const* d_in, const int* in_sizes, int n_in, void* d_out, int out_size, void* d_ws, size_t ws_size, hipStream_t stream) {
    (void)hipFuncSetAttribute((const void*)fused, hipFuncAttributeMaxDynamicSharedMemorySize, LDS_BYTES);
    fused<<<512, 256, LDS_BYTES, stream>>>((const u16*)d_in[0], (const u16*)d_in[1], (const u16*)d_in[2], (const u16*)d_in[3], (const u16*)d_in[4], (const u16*)d_in[5],
                                           (const u16*)d_in[6], (u16*)d_out, (char*)d_ws);
}
```

```cpp
#define DIAG -1
#define DIAGBLK 0
#include <hip/hip_runtime.h>
#include <stdint.h>

namespace {

typedef unsigned short u16;
typedef unsigned long long u64;
typedef __attribute__((ext_vector_type(16))) float f32x16;
typedef __attribute__((ext_vector_type(4))) float f32x4;
typedef __attribute__((ext_vector_type(2))) float f32x2;
typedef __attribute__((ext_vector_type(8))) __bf16 bf16x8;
typedef __attribute__((ext_vector_type(2))) __bf16 bf16x2;
typedef __attribute__((ext_vector_type(4))) unsigned u32x4;
typedef __attribute__((ext_vector_type(2))) unsigned u32x2;
typedef __attribute__((ext_vector_type(4))) short s16x4;
typedef __amdgpu_buffer_rsrc_t rsrc_t;
#define LAS __attribute__((address_space(3)))
#define GAS __attribute__((address_space(1)))
#define DI __device__ __forceinline__
#define SBAR() __builtin_amdgcn_sched_barrier(0)

constexpr int SEQ = 2048, DM = 448;
constexpr unsigned XROW = DM * 2;
constexpr unsigned WROW = 3 * DM * 2;
constexpr unsigned X_BYTES = SEQ * XROW, W_BYTES = DM * WROW, WO_BYTES = DM * XROW;
constexpr size_t WS_EPOCH_A = 0, WS_EPOCH_B = 256;
constexpr size_t WS_FK = 4096;
constexpr size_t WS_FG = WS_FK + 8192;
constexpr size_t WS_FP = WS_FG + 8192;
constexpr size_t WS_FOA = WS_FP + 8192;
constexpr size_t WS_FOB = WS_FOA + 8192;
constexpr size_t WS_KVA = 65536;
constexpr size_t WS_OA = WS_KVA + (4u << 20);
constexpr size_t WS_OB = WS_OA + (2u << 20);
constexpr size_t WS_KVP = WS_OB + (2u << 20);
constexpr int LDS_BYTES = 80 * 1024;
constexpr float LOG2E = 1.4426950408889634f;
constexpr float QSCALE = 0.18898223650461362f * LOG2E;
#define REF_OFF 32.f
constexpr float REF_OFFSET = REF_OFF;
constexpr float L_LIMIT = 18446744073709551616.f;
constexpr int SC1 = 16;
constexpr unsigned SPIN_CAP = 1u << 18;
#define ROT 0
#define BAHEAD 2
#define QEARLY 3
#define QSPLIT 16
#define BDELAY 0

DI f32x16 mfma(u32x4 a, u32x4 b, f32x16 c) {
    return __builtin_amdgcn_mfma_f32_32x32x16_bf16(__builtin_bit_cast(bf16x8, a), __builtin_bit_cast(bf16x8, b), c, 0, 0, 0);
}
DI unsigned pk(float lo, float hi) { f32x2 v = {lo, hi}; return __builtin_bit_cast(unsigned, __builtin_convertvector(v, bf16x2)); }
DI u32x4 pack_lo(const f32x16& x) { return u32x4{pk(x[0], x[1]), pk(x[2], x[3]), pk(x[4], x[5]), pk(x[6], x[7])}; }
DI u32x4 pack_hi(const f32x16& x) { return u32x4{pk(x[8], x[9]), pk(x[10], x[11]), pk(x[12], x[13]), pk(x[14], x[15])}; }
DI u32x4 quad(const f32x16& x, int g) { return u32x4{__float_as_uint(x[4 * g]), __float_as_uint(x[4 * g + 1]), __float_as_uint(x[4 * g + 2]), __float_as_uint(x[4 * g + 3])}; }
DI rsrc_t make_rsrc(const void* p, unsigned bytes) { return __builtin_amdgcn_make_buffer_rsrc((void*)p, 0, bytes, 0x00020000); }
DI u32x4 ld128(rsrc_t rs, unsigned voff, unsigned soff) { return __builtin_amdgcn_raw_buffer_load_b128(rs, voff, soff, 0); }
DI u32x4 ld128_dev(rsrc_t rs, unsigned voff, unsigned soff) { return __builtin_amdgcn_raw_buffer_load_b128(rs, voff, soff, SC1); }
DI void st128_dev(u32x4 v, rsrc_t rs, unsigned voff, unsigned soff) { __builtin_amdgcn_raw_buffer_store_b128(v, rs, voff, soff, SC1); }
DI void dma16(rsrc_t rs, unsigned lds_addr, unsigned voff, unsigned soff) {
    asm volatile("s_mov_b32 m0, %0\n\ts_nop 0\n\tbuffer_load_dwordx4 %1, %2, %3 offen lds" ::"s"(lds_addr), "v"(voff), "s"(rs), "s"(soff) : "memory");
}
DI void ld128_async(u32x4& d, rsrc_t rs, unsigned voff, unsigned soff) {
    asm volatile("buffer_load_dwordx4 %0, %1, %2, %3 offen" : "=v"(d) : "v"(voff), "s"(rs), "s"(soff) : "memory");
}
template <int N> DI void wait_vm() { asm volatile("s_waitcnt vmcnt(%0)" ::"n"(N) : "memory"); }
template <int N> DI void wait_bar() { asm volatile("s_waitcnt vmcnt(%0) lgkmcnt(0)\n\ts_barrier" ::"n"(N) : "memory"); }
DI void lds_bar() { asm volatile("s_waitcnt lgkmcnt(0)\n\ts_barrier" ::: "memory"); }
DI u32x4 lds128(unsigned a) { return *(const LAS u32x4*)(size_t)a; }
DI void sts128(unsigned a, u32x4 v) { *(LAS u32x4*)(size_t)a = v; }
DI void sts64(unsigned a, u32x2 v) { *(LAS u32x2*)(size_t)a = v; }
DI u32x2 tr64(unsigned a) { return __builtin_bit_cast(u32x2, __builtin_amdgcn_ds_read_tr16_b64_v4i16((LAS s16x4*)(size_t)a)); }
DI u32x4 tr_frag(unsigned a0, unsigned a1) { u32x2 lo = tr64(a0), hi = tr64(a1); return u32x4{lo[0], lo[1], hi[0], hi[1]}; }
DI float both_max(float v) { auto r = __builtin_amdgcn_permlane32_swap(__float_as_uint(v), __float_as_uint(v), false, false); return fmaxf(__uint_as_float(r[0]), __uint_as_float(r[1])); }
DI float both_min(float v) { auto r = __builtin_amdgcn_permlane32_swap(__float_as_uint(v), __float_as_uint(v), false, false); return fminf(__uint_as_float(r[0]), __uint_as_float(r[1])); }
DI float both_sum(float v) { auto r = __builtin_amdgcn_permlane32_swap(__float_as_uint(v), __float_as_uint(v), false, false); return __uint_as_float(r[0]) + __uint_as_float(r[1]); }
DI float upper_val(float v) { auto r = __builtin_amdgcn_permlane32_swap(__float_as_uint(v), __float_as_uint(v), false, false); return __uint_as_float(r[1]); }
DI float max16(const f32x16& s) {
    float a = fmaxf(fmaxf(s[0], s[1]), s[2]), b = fmaxf(fmaxf(s[3], s[4]), s[5]);
    a = fmaxf(fmaxf(a, s[6]), s[7]); b = fmaxf(fmaxf(b, s[8]), s[9]);
    a = fmaxf(fmaxf(a, s[10]), s[11]); b = fmaxf(fmaxf(b, s[12]), s[13]);
    a = fmaxf(fmaxf(a, s[14]), s[15]);
    return fmaxf(a, b);
}
DI float min16(const f32x16& s) {
    float a = fminf(fminf(s[0], s[1]), s[2]), b = fminf(fminf(s[3], s[4]), s[5]);
    a = fminf(fminf(a, s[6]), s[7]); b = fminf(fminf(b, s[8]), s[9]);
    a = fminf(fminf(a, s[10]), s[11]); b = fminf(fminf(b, s[12]), s[13]);
    a = fminf(fminf(a, s[14]), s[15]);
    return fminf(a, b);
}

#define STAMP(c, id) do { if (id == DIAG || id == 0) { u64 t_; asm volatile("s_memrealtime %0\n\ts_waitcnt lgkmcnt(0)" : "=s"(t_) :: "memory"); *(LAS u64*)(size_t)((c).lds + LDS_BYTES - 512 + (c).wid * 128 + (id == 0 ? 0 : 8)) = t_; } } while (0)

struct Ctx {
    int lane, wid, hi, r32, h, rb, rot;
    static constexpr unsigned lds = 0;
};

DI Ctx fresh(const Ctx& c) {
    Ctx n = c;
    asm volatile("v_mbcnt_lo_u32_b32 %0, -1, 0\n\tv_mbcnt_hi_u32_b32 %0, -1, %0" : "=v"(n.lane));
    n.hi = n.lane >> 5; n.r32 = n.lane & 31;
    return n;
}
DI int chunk_at(const Ctx& c, int p) { const int v = c.rot + p; return v >= 7 ? v - 7 : v; }
DI u64 load_word(const char* p) { return __hip_atomic_load((GAS u64*)(size_t)p, __ATOMIC_RELAXED, __HIP_MEMORY_SCOPE_AGENT); }
DI void store_word(char* p, u64 v) { __hip_atomic_store((GAS u64*)(size_t)p, v, __ATOMIC_RELAXED, __HIP_MEMORY_SCOPE_AGENT); }
DI void epoch_request(u64& v, const char* p) { asm volatile("s_load_dwordx2 %0, %1, 0x0" : "=s"(v) : "s"(p) : "memory"); }
DI void publish(const Ctx& c, char* word, u64 nonce) { if (c.lane == 0) store_word(word, nonce); }
DI void await(const Ctx& c, const char* words, int mask, u64 nonce, unsigned cap) {
    const char* p = words + (c.lane & mask) * 8;
#pragma unroll 1
    for (unsigned spins = 0; spins < cap; ++spins) {
        if (__all(load_word(p) == nonce)) break;
        __builtin_amdgcn_s_sleep(2);
    }
    __builtin_amdgcn_fence(__ATOMIC_ACQUIRE, "wavefront");
    asm volatile("" ::: "memory");
}

DI void x_frag_offsets(const Ctx& c, unsigned (&xo)[4]) {
#pragma unroll
    for (int s = 0; s < 4; ++s) xo[s] = c.r32 * 128 + (((2 * s + c.hi) ^ ((c.r32 >> 1) & 7)) << 4);
}
DI unsigned x_swz(const Ctx& c, int odd) { return ((c.lane & 7) ^ (4 * odd + (c.lane >> 4))) << 4; }
DI void x_bounce(const Ctx& c, unsigned buf, const u32x4 (&xr)[28], int ch) {
    const unsigned w0 = buf + (c.lane >> 3) * 128 + x_swz(c, 0), w1 = buf + (c.lane >> 3) * 128 + x_swz(c, 1);
    sts128(w0, xr[4 * ch]); sts128(w1 + 1024, xr[4 * ch + 1]); sts128(w0 + 2048, xr[4 * ch + 2]); sts128(w1 + 3072, xr[4 * ch + 3]);
}

DI void out_prefetch(const Ctx& c, const u16* wo) {
    const int cg = c.h % 7, row = c.lane >> 3;
    const rsrc_t rw = make_rsrc(wo, WO_BYTES);
    const unsigned vw = (112 * c.wid + row) * XROW + cg * 128 + (((c.lane & 7) ^ (((row >> 1) & 1) << 2)) << 4);
#pragma unroll
    for (int j = 0; j < 14; ++j) dma16(rw, c.lds + (14 * c.wid + j) * 1024, vw, 8 * j * XROW);
}
DI void phase_out(const Ctx& c, const char* ofr, const char* flags, u64 nonce, u16* out, char* epoch) {
    const int cg = c.h % 7, rh = c.h / 7, rt = c.wid & 1, kh = c.wid >> 1;
    const int qt = c.rb * 4 + rh * 2 + rt;
    await(c, flags + (qt * 16 + kh * 8) * 8, 7, nonce, SPIN_CAP);
    STAMP(c, 11);
    const rsrc_t ro = make_rsrc(ofr + (size_t)(qt * 16 + kh * 8) * 2048, 16384);
    u32x4 of[16];
#pragma unroll
    for (int i = 0; i < 16; ++i) of[i] = ld128_dev(ro, c.lane * 16 + (i & 3) * 1024, (i >> 2) * 4096);
    wait_bar<16>();
    const int q = (c.lane & 15) >> 2;
    const unsigned col = 32 * ((c.lane >> 4) & 1) + 8 * (c.lane & 3);
    const unsigned rbase = c.lds + (224 * kh + 4 * c.hi + q) * 128;
    const unsigned a0 = rbase + (col ^ ((q >> 1) << 6)), a1 = rbase + ((col + 64) ^ ((q >> 1) << 6));
    const unsigned back = c.hi ? 4 * 128 : 0;
    f32x16 acc0 = {}, acc1 = {};
#pragma unroll
    for (int i = 0; i < 16; ++i) {
        const unsigned ro0 = ((i >> 1) * 28 + (i & 1) * 16) * 128, ro1 = ro0 + 8 * 128;
        const unsigned adj = (i & 1) ? back : 0;
        acc0 = mfma(tr_frag(a0 + ro0, a0 + ro1 - adj), of[i], acc0);
        acc1 = mfma(tr_frag(a1 + ro0, a1 + ro1 - adj), of[i], acc1);
    }
    STAMP(c, 12);
    const unsigned xch = c.lds + 57344;
#pragma unroll
    for (int g = 0; g < 4; ++g) sts128(xch + c.wid * 4096 + g * 1024 + c.lane * 16, kh ? quad(acc0, g) : quad(acc1, g));
    f32x16 r = kh ? acc1 : acc0;
    lds_bar();
    if (c.h == 0 && c.rb == 0 && c.wid == 0 && c.lane == 0) store_word(epoch, nonce);
#pragma unroll
    for (int g = 0; g < 4; ++g) {
        const u32x4 p = lds128(xch + (c.wid ^ 2) * 4096 + g * 1024 + c.lane * 16);
#pragma unroll
        for (int i = 0; i < 4; ++i) r[4 * g + i] += __uint_as_float(p[i]);
    }
    const u32x4 lo = pack_lo(r), hi = pack_hi(r);
    auto s0 = __builtin_amdgcn_permlane32_swap(lo[0], lo[2], false, false), s1 = __builtin_amdgcn_permlane32_swap(lo[1], lo[3], false, false);
    auto s2 = __builtin_amdgcn_permlane32_swap(hi[0], hi[2], false, false), s3 = __builtin_amdgcn_permlane32_swap(hi[1], hi[3], false, false);
    char* dst = (char*)out + (size_t)(qt * 32 + c.r32) * (2 * XROW) + cg * 128 + kh * 64 + c.hi * 16;
    {
        const rsrc_t rd = make_rsrc(out, 2 * X_BYTES);
        const unsigned o = (unsigned)(dst - (char*)out);
        __builtin_amdgcn_raw_buffer_store_b128(u32x4{s0[0], s1[0], s0[1], s1[1]}, rd, o, 0, 16);
        __builtin_amdgcn_raw_buffer_store_b128(u32x4{s2[0], s3[0], s2[1], s3[1]}, rd, o + 32, 0, 16);
    }
}

DI void mfma_pair(int isv, u32x4 xf, u32x4 w0, u32x4 w1, f32x16& acc0, f32x16& acc1) {
    asm("s_cmp_lg_u32 %5, 0\n\ts_cbranch_scc1 1f\n\t"
        "v_mfma_f32_32x32x16_bf16 %0, %3, %2, %0\n\tv_mfma_f32_32x32x16_bf16 %1, %4, %2, %1\n\ts_branch 2f\n"
        "1:\n\tv_mfma_f32_32x32x16_bf16 %0, %2, %3, %0\n\tv_mfma_f32_32x32x16_bf16 %1, %2, %4, %1\n2:"
        : "+v"(acc0), "+v"(acc1) : "v"(xf), "v"(w0), "v"(w1), "s"(isv) : "scc");
}
template <int N> DI void landed4(u32x4 (&r)[28], int ch) {
    asm volatile("s_waitcnt vmcnt(%4)" : "+v"(r[4 * ch]), "+v"(r[4 * ch + 1]), "+v"(r[4 * ch + 2]), "+v"(r[4 * ch + 3]) : "n"(N) : "memory");
}
DI void kv_gemm(const Ctx& c, int isv, rsrc_t rx, rsrc_t rw, f32x16& acc0, f32x16& acc1, u32x4 (&xf)[28]) {
    const unsigned bounce = c.lds + 57344 + c.wid * 4096;
    const int row = c.lane >> 3;
    const unsigned piece = (c.lane & 7) ^ (((row >> 1) & 1) << 2);
    const unsigned vw = piece < 7 ? (16 * c.wid + row) * WROW + (1 + isv) * XROW + (c.h >> 1) * 112 + (piece << 4) : 0x80000000u;
    const unsigned vx = (c.rb * 128 + 32 * c.wid + row) * XROW + (c.lane & 7) * 16;
    u32x4 xr[28];
    auto issue = [&](int ch) {
        const unsigned k = chunk_at(c, ch);
        dma16(rw, c.lds + ch * 8192 + c.wid * 2048, vw, k * 64 * WROW);
        dma16(rw, c.lds + ch * 8192 + c.wid * 2048 + 1024, vw, (k * 64 + 8) * WROW);
#pragma unroll
        for (int i = 0; i < 4; ++i) ld128_async(xr[4 * ch + i], rx, vx, k * 128 + i * 8 * XROW);
    };
    issue(0); issue(1); issue(2); issue(3);
    asm volatile("s_waitcnt lgkmcnt(0)" ::: "memory");
    STAMP(c, 1);
    unsigned xo[4]; x_frag_offsets(c, xo);
    const int q = (c.lane & 15) >> 2;
    const unsigned col = 32 * ((c.lane >> 4) & 1) + 8 * (c.lane & 3);
    const unsigned w0 = c.lds + (8 * c.hi + q) * 128 + (col ^ ((q >> 1) << 6)), w1 = c.lds + (8 * c.hi + q) * 128 + ((col + 56) ^ ((q >> 1) << 6));
    acc0 = f32x16{}; acc1 = f32x16{};
    asm volatile("s_nop 1" : "+v"(acc0), "+v"(acc1));
    u32x4 wf[2][8];
#pragma unroll
    for (int p = 0; p <= 7; ++p) {
        if (p < 7) {
            if (p <= 3) landed4<18>(xr, p); else if (p == 4) landed4<12>(xr, p); else if (p == 5) landed4<6>(xr, p); else landed4<0>(xr, p);
            x_bounce(c, bounce, xr, p);
        }
        SBAR();
        if (p > 0) {
            mfma_pair(isv, xf[4 * p - 4], wf[(p - 1) & 1][0], wf[(p - 1) & 1][1], acc0, acc1);
            mfma_pair(isv, xf[4 * p - 3], wf[(p - 1) & 1][2], wf[(p - 1) & 1][3], acc0, acc1);
        }
        SBAR();
        if (p < 7) {
            asm volatile("s_barrier" ::: "memory");
            if (p == 0) STAMP(c, 2);
            if (p == 6) STAMP(c, 3);
#pragma unroll
            for (int s = 0; s < 4; ++s) {
                const unsigned o = p * 8192 + s * 2048;
                xf[4 * p + s] = lds128(bounce + xo[s]);
                wf[p & 1][2 * s] = tr_frag(w0 + o, w0 + o + 512); wf[p & 1][2 * s + 1] = tr_frag(w1 + o, w1 + o + 512);
            }
        }
        SBAR();
        if (p > 0) {
            mfma_pair(isv, xf[4 * p - 2], wf[(p - 1) & 1][4], wf[(p - 1) & 1][5], acc0, acc1);
            mfma_pair(isv, xf[4 * p - 1], wf[(p - 1) & 1][6], wf[(p - 1) & 1][7], acc0, acc1);
        }
        SBAR();
        if (p + 4 <= 6) issue(p + 4);
    }
    asm volatile("s_nop 11" : "+v"(acc0), "+v"(acc1));
}

struct KV { u32x4 k0, k1, v0, v1; };

DI void set_reference(u32x4& q1, float nm, int hi) {
    const unsigned a = __float_as_uint(nm) & 0xFFFF0000u;
    const float r1 = nm - __uint_as_float(a);
    const unsigned b = __float_as_uint(r1) & 0xFFFF0000u;
    const float r2 = r1 - __uint_as_float(b);
    if (hi) { q1[2] = (a >> 16) | b; q1[3] = __float_as_uint(r2) >> 16; }
}

DI void branch_a(const Ctx& c, const u16* x, const u16* w, char* ws, u64& nonce) {
    u64 epoch; epoch_request(epoch, ws + WS_EPOCH_A);
    const rsrc_t rx = make_rsrc(x, X_BYTES), rw = make_rsrc(w, W_BYTES);
    const unsigned voff = c.lane * 16;
    u32x4 q[4][2];
    const rsrc_t rk = make_rsrc(ws + WS_KVA + (size_t)(c.h * 64 + c.wid * 16) * 4096, 16 * 4096);
#define LDKV(X, j) do { X.v1 = ld128_dev(rk, voff + 3072, (j) * 4096); X.v0 = ld128_dev(rk, voff + 2048, (j) * 4096); \
                        X.k1 = ld128_dev(rk, voff + 1024, (j) * 4096); X.k0 = ld128_dev(rk, voff, (j) * 4096); } while (0)
    KV a, b, cc, d;
    {
        u32x4 xf[28];
        f32x16 acc0, acc1;
        const int isv = c.h & 1;
        kv_gemm(c, isv, rx, rw, acc0, acc1, xf);
        asm volatile("" : "+s"(epoch));
        u32x4 f[4] = {pack_lo(acc0), pack_hi(acc0), pack_lo(acc1), pack_hi(acc1)};
        {
            const bool all = isv && c.r32 >= 28, tail = isv ? all : (bool)c.hi;
            const unsigned va = c.r32 == 28 ? 0x3F803F80u : 0u, v2 = isv ? va : 0x3F803F80u, v3 = isv ? va : 0x00003F80u;
#pragma unroll
            for (int i = 0; i < 4; ++i) {
                f[i][0] = all ? va : f[i][0]; f[i][1] = all ? va : f[i][1];
                if (i & 1) { f[i][2] = tail ? v2 : f[i][2]; f[i][3] = tail ? v3 : f[i][3]; }
                else       { f[i][2] = all ? va : f[i][2]; f[i][3] = all ? va : f[i][3]; }
            }
        }
        const int kt = c.rb * 4 + c.wid;
        const rsrc_t ro = make_rsrc(ws + WS_KVA + (size_t)((c.h & 14) * 64 + kt) * 4096 + isv * 2048, 64 * 4096 + 2048);
        st128_dev(f[0], ro, voff, 0); st128_dev(f[1], ro, voff + 1024, 0); st128_dev(f[2], ro, voff, 64 * 4096); st128_dev(f[3], ro, voff + 1024, 64 * 4096);
        STAMP(c, 4);
        const unsigned wq = c.lds, xb = c.lds + 32768;
        const unsigned vwq = (16 * c.wid + (c.lane >> 2)) * WROW + c.h * 56 + (c.lane & 3) * 16;
        nonce = epoch + 1;
        publish(c, ws + WS_FG + (size_t)(blockIdx.x * 4 + c.wid) * 8, nonce);
        lds_bar();
#pragma unroll
        for (int j = 0; j < QEARLY; ++j) dma16(rw, wq + j * 4096 + c.wid * 1024, vwq, chunk_at(c, j) * 64 * WROW);
        wait_vm<QEARLY + 1>();
        publish(c, ws + WS_FK + (size_t)(((c.h >> 1) * 64 + kt) * 2 + isv) * 8, nonce);
        STAMP(c, 5);
#pragma unroll
        for (int j = QEARLY; j < 7; ++j) dma16(rw, wq + j * 4096 + c.wid * 1024, vwq, chunk_at(c, j) * 64 * WROW);
        wait_bar<QEARLY == 7 ? 1 : 0>();
        const unsigned wo = wq + (8 * c.hi + ((c.lane & 15) >> 2)) * 64 + 32 * ((c.lane >> 4) & 1) + 8 * (c.lane & 3);
        f32x16 acc = {};
#pragma unroll
        for (int i = 0; i < 28; ++i) {
            if (i == QSPLIT) {
                STAMP(c, 6);
                await(c, ws + WS_FK + (size_t)((c.h >> 1) * 64 + c.wid * 16) * 16, 31, nonce, SPIN_CAP);
                STAMP(c, 7);
                LDKV(a, 0); LDKV(b, 1); LDKV(cc, 2);
            }
            acc = mfma(tr_frag(wo + i * 1024, wo + i * 1024 + 256), xf[i], acc);
        }
        acc *= QSCALE;
        u32x4 q0 = pack_lo(acc), q1 = pack_hi(acc);
        if (c.hi) { q1[2] = 0; q1[3] = 0; }
        sts128(xb + c.wid * 2048 + voff, q0); sts128(xb + c.wid * 2048 + 1024 + voff, q1);
        lds_bar();
#pragma unroll
        for (int t = 0; t < 4; ++t) { q[t][0] = lds128(xb + t * 2048 + voff); q[t][1] = lds128(xb + t * 2048 + 1024 + voff); }
    }
    const Ctx c0 = c;
    const unsigned comb = c.lds, stat = c.lds + 49152, flagw = c.lds + 57344;
    if (c.wid == 0 && c.lane == 0) *(LAS unsigned*)(size_t)flagw = 0;
    const f32x16 zero = {};
    for (int exact = 0;; ++exact) {
        float ref[4];
        if (exact) { LDKV(a, 0); LDKV(b, 1); LDKV(cc, 2); }
#pragma unroll
        for (int t = 0; t < 4; ++t) { f32x16 s = mfma(a.k0, q[t][0], zero); s = mfma(a.k1, q[t][1], s); ref[t] = max16(s); }
        if (exact) {
            for (int j = 1; j < 16; ++j) {
                const u32x4 k0 = ld128_dev(rk, voff, j * 4096), k1 = ld128_dev(rk, voff + 1024, j * 4096);
#pragma unroll
                for (int t = 0; t < 4; ++t) { f32x16 s = mfma(k0, q[t][0], zero); s = mfma(k1, q[t][1], s); ref[t] = fmaxf(ref[t], max16(s)); }
            }
        }
#pragma unroll
        for (int t = 0; t < 4; ++t) { ref[t] = both_max(ref[t]) + (exact ? 0.f : REF_OFFSET); set_reference(q[t][1], -ref[t], c.hi); }
        STAMP(c, 8);
        f32x16 o0 = {}, o1 = {}, o2 = {}, o3 = {};
        f32x16 sa, sb; u32x4 pa, pb = {};
        float e12 = 0.f, e13 = 0.f, e14 = 0.f, e15 = 0.f;
        d.v1 = u32x4{};
        sa = mfma(a.k0, q[0][0], zero); sa = mfma(a.k1, q[0][1], sa);
#define EX(S, i) S[i] = __builtin_amdgcn_exp2f(S[i])
#define STEP(SC, SN, KN0, KN1, QN0, QN1, OP, VP1, OC, VC0) do { \
    SN = mfma(KN0, QN0, zero); SBAR(); \
    pb[2] = pk(e12, e13); SBAR(); EX(SC, 0); SBAR(); pb[3] = pk(e14, e15); SBAR(); EX(SC, 1); EX(SC, 2); EX(SC, 3); SBAR(); \
    OP = mfma(VP1, pb, OP); SBAR(); \
    EX(SC, 4); EX(SC, 5); SBAR(); pa[0] = pk(SC[0], SC[1]); SBAR(); EX(SC, 6); SBAR(); pa[1] = pk(SC[2], SC[3]); SBAR(); EX(SC, 7); SBAR(); \
    SN = mfma(KN1, QN1, SN); SBAR(); \
    EX(SC, 8); SBAR(); pa[2] = pk(SC[4], SC[5]); SBAR(); EX(SC, 9); SBAR(); pa[3] = pk(SC[6], SC[7]); SBAR(); EX(SC, 10); EX(SC, 11); SBAR(); \
    OC = mfma(VC0, pa, OC); SBAR(); \
    e12 = __builtin_amdgcn_exp2f(SC[12]); SBAR(); pb[0] = pk(SC[8], SC[9]); SBAR(); e13 = __builtin_amdgcn_exp2f(SC[13]); SBAR(); pb[1] = pk(SC[10], SC[11]); SBAR(); \
    e14 = __builtin_amdgcn_exp2f(SC[14]); e15 = __builtin_amdgcn_exp2f(SC[15]); SBAR(); } while (0)
#define TILE(CUR, PREV, NXT, jload) do { \
    STEP(sa, sb, CUR.k0, CUR.k1, q[1][0], q[1][1], o3, PREV.v1, o0, CUR.v0); LDKV(PREV, jload); SBAR(); \
    STEP(sb, sa, CUR.k0, CUR.k1, q[2][0], q[2][1], o0, CUR.v1, o1, CUR.v0); \
    STEP(sa, sb, CUR.k0, CUR.k1, q[3][0], q[3][1], o1, CUR.v1, o2, CUR.v0); \
    STEP(sb, sa, NXT.k0, NXT.k1, q[0][0], q[0][1], o2, CUR.v1, o3, CUR.v0); } while (0)
        for (int j = 0; j < 16; j += 4) { TILE(a, d, b, j + 3); TILE(b, a, cc, j + 4); TILE(cc, b, d, j + 5); TILE(d, cc, a, j + 6); }
        pb[2] = pk(e12, e13); pb[3] = pk(e14, e15);
        o3 = mfma(d.v1, pb, o3);
#undef TILE
#undef STEP
#undef EX
        __builtin_amdgcn_s_setprio(3);
        STAMP(c, 9);
        const Ctx c = fresh(c0);
        const unsigned voff = c.lane * 16;
        bool bad = false;
        {
            const f32x16* ot[4] = {&o0, &o1, &o2, &o3};
#pragma unroll
            for (int t = 0; t < 4; ++t) {
                const float l = (*ot[t])[12];
                if (c.hi) {
                    bad |= !(l < L_LIMIT);
                    sts64(stat + (t * 4 + c.wid) * 256 + c.r32 * 8, u32x2{__float_as_uint(ref[t]), __float_as_uint(ref[t] + __builtin_amdgcn_logf(l))});
                }
                if (t != c.wid) {
                    const unsigned dst = comb + (t * 3 + (c.wid > t ? c.wid - 1 : c.wid)) * 4096 + voff;
#pragma unroll
                    for (int g = 0; g < 4; ++g) sts128(dst + g * 1024, quad(*ot[t], g));
                }
            }
        }
        f32x16 o = c.wid == 0 ? o0 : c.wid == 1 ? o1 : c.wid == 2 ? o2 : o3;
        lds_bar();
        {
            float m[4], e[4];
#pragma unroll
            for (int v = 0; v < 4; ++v) { const u32x2 s = *(const LAS u32x2*)(size_t)(stat + (c.wid * 4 + v) * 256 + c.r32 * 8); m[v] = __uint_as_float(s[0]); e[v] = __uint_as_float(s[1]); }
            const float top = fmaxf(fmaxf(e[0], e[1]), fmaxf(e[2], e[3]));
            const float mine = c.wid == 0 ? m[0] : c.wid == 1 ? m[1] : c.wid == 2 ? m[2] : m[3];
            o *= __builtin_amdgcn_exp2f(mine - top);
#pragma unroll
            for (int v = 0; v < 3; ++v) {
                const float mv = v == 0 ? (c.wid == 0 ? m[1] : m[0]) : v == 1 ? (c.wid <= 1 ? m[2] : m[1]) : (c.wid <= 2 ? m[3] : m[2]);
                const float cv = __builtin_amdgcn_exp2f(mv - top);
#pragma unroll
                for (int g = 0; g < 4; ++g) {
                    const u32x4 p = lds128(comb + (c.wid * 3 + v) * 4096 + g * 1024 + voff);
#pragma unroll
                    for (int i = 0; i < 4; ++i) o[4 * g + i] = fmaf(__uint_as_float(p[i]), cv, o[4 * g + i]);
                }
            }
        }
        o *= __builtin_amdgcn_rcpf(upper_val(o[12]));
        float chk = 0.f;
#pragma unroll
        for (int i = 0; i < 16; ++i) chk = fmaf(o[i], 0.f, chk);
        bad |= !(chk == 0.f);
        u32x4 f0 = pack_lo(o), f1 = pack_hi(o);
        if (c.hi) { f1[2] = 0; f1[3] = 0; }
        const rsrc_t ro = make_rsrc(ws + WS_OA + (size_t)((c.rb * 4 + c.wid) * 16 + c.h) * 2048, 2048);
        st128_dev(f0, ro, voff, 0); st128_dev(f1, ro, voff + 1024, 0);
        if (!exact && __any(bad) && c.lane == 0) *(LAS unsigned*)(size_t)flagw = 1;
        lds_bar();
        if (exact || __builtin_amdgcn_readfirstlane(*(const LAS unsigned*)(size_t)flagw) == 0) break;
#pragma unroll
        for (int t = 0; t < 4; ++t) if (c.hi) { q[t][1][2] = 0; q[t][1][3] = 0; }
    }
#undef LDKV
}

DI void branch_b(const Ctx& c, const u16* x, const u16* w, const u16* wphi, char* ws, u64& nonce) {
    u64 epoch, epoch_a; epoch_request(epoch, ws + WS_EPOCH_B); epoch_request(epoch_a, ws + WS_EPOCH_A);
    const unsigned voff = c.lane * 16;
    unsigned we[16];
    {
        const int f = c.r32 < 28 ? c.r32 : c.r32 - 4;
        const u16* p = wphi + c.h * 784 + f;
#pragma unroll
        for (int i = 0; i < 16; ++i) { const int d = 8 * (i >> 2) + 4 * c.hi + (i & 3); we[i] = p[(d < 28 ? d : d - 4) * 28]; }
    }
    asm volatile("s_waitcnt lgkmcnt(0)" : "+s"(epoch), "+s"(epoch_a) :: "memory");
    nonce = epoch + 1;
    await(c, ws + WS_FG + (size_t)(blockIdx.x - 256) * 32, 3, epoch_a + 1, 24);
    u32x4 wp[2];
#pragma unroll
    for (int i = 0; i < 8; ++i) wp[i >> 2][i & 3] = we[2 * i] | (we[2 * i + 1] << 16);
    asm volatile("" : "+v"(wp[0]), "+v"(wp[1]));
    for (int i = 0; i < BDELAY; ++i) __builtin_amdgcn_s_sleep(2);
    __builtin_amdgcn_s_setprio(2);
    STAMP(c, 1);
    u32x4 eq[4];
    const f32x16 zero = {};
    {
        static_assert(BAHEAD >= 2 && BAHEAD <= 3, "slot reuse distance");
        constexpr unsigned SLOT = 12288;
        const rsrc_t rx = make_rsrc(x, X_BYTES), rw = make_rsrc(w, W_BYTES);
        unsigned vw[3];
#pragma unroll
        for (int j = 0; j < 3; ++j) { const int p = 64 * j + c.lane, kr = p / 12, rem = p % 12; vw[j] = (16 * c.wid + kr) * WROW + (rem >> 2) * XROW + c.h * 56 + (rem & 3) * 16; }
        const unsigned vx = (c.rb * 128 + 32 * c.wid + (c.lane >> 3)) * XROW + (c.lane & 7) * 16;
        u32x4 xr[28];
        auto issue = [&](int ch) {
            const unsigned k = chunk_at(c, ch);
#pragma unroll
            for (int j = 0; j < 3; ++j) dma16(rw, c.lds + (ch % 5) * SLOT + c.wid * 3072 + j * 1024, vw[j], k * 64 * WROW);
#pragma unroll
            for (int i = 0; i < 4; ++i) ld128_async(xr[4 * ch + i], rx, vx, k * 128 + i * 8 * XROW);
        };
#pragma unroll
        for (int ch = 0; ch < BAHEAD; ++ch) issue(ch);
        STAMP(c, 2);
        const unsigned bounce = c.lds + 61440 + c.wid * 4096;
        unsigned xo[4]; x_frag_offsets(c, xo);
        const unsigned wo = c.lds + (8 * c.hi + ((c.lane & 15) >> 2)) * 192 + 32 * ((c.lane >> 4) & 1) + 8 * (c.lane & 3);
        f32x16 aq = {}, ak = {}, av = {};
        u32x4 xf[2][4], wf[2][12];
        auto step = [&](int set, int s) {
            aq = mfma(wf[set][3 * s], xf[set][s], aq); ak = mfma(wf[set][3 * s + 1], xf[set][s], ak); av = mfma(xf[set][s], wf[set][3 * s + 2], av);
        };
#pragma unroll
        for (int p = 0; p <= 7; ++p) {
            if (p < 7) {
                constexpr int A = BAHEAD - 1;
                if (p + A <= 6) landed4<7 * A>(xr, p); else if (p == 3) landed4<21>(xr, p); else if (p == 4) landed4<14>(xr, p); else if (p == 5) landed4<7>(xr, p); else landed4<0>(xr, p);
                x_bounce(c, bounce, xr, p);
            }
            SBAR();
            if (p > 0) { step((p - 1) & 1, 0); step((p - 1) & 1, 1); }
            SBAR();
            if (p < 7) {
                asm volatile("s_barrier" ::: "memory");
#pragma unroll
                for (int s = 0; s < 4; ++s) {
                    const unsigned wa = wo + (p % 5) * SLOT + s * 3072;
                    xf[p & 1][s] = lds128(bounce + xo[s]);
                    wf[p & 1][3 * s] = tr_frag(wa, wa + 768); wf[p & 1][3 * s + 1] = tr_frag(wa + 64, wa + 64 + 768); wf[p & 1][3 * s + 2] = tr_frag(wa + 128, wa + 128 + 768);
                }
            }
            SBAR();
            if (p > 0) { step((p - 1) & 1, 2); step((p - 1) & 1, 3); }
            SBAR();
            if (p + BAHEAD <= 6) issue(p + BAHEAD);
        }
        STAMP(c, 3);
        f32x16 kv0, kv1;
        {
            u32x4 k0 = pack_lo(ak), k1 = pack_hi(ak);
            if (c.hi) { k1[2] = 0; k1[3] = 0; }
            f32x16 p = mfma(wp[0], k0, zero); p = mfma(wp[1], k1, p);
            const float mx = both_max(max16(p)) * LOG2E, mn = both_min(min16(p)) * LOG2E;
            f32x16 ep, en;
#pragma unroll
            for (int i = 0; i < 16; ++i) { ep[i] = __builtin_amdgcn_exp2f(fmaf(p[i], LOG2E, -mx)); en[i] = __builtin_amdgcn_exp2f(fmaf(p[i], -LOG2E, mn)); }
            const float live = c.hi ? 0.f : 1.f;
            float sp = (ep[12] + ep[13] + ep[14] + ep[15]) * live, sn = (en[12] + en[13] + en[14] + en[15]) * live;
#pragma unroll
            for (int i = 0; i < 12; ++i) { sp += ep[i]; sn += en[i]; }
            const float rp = __builtin_amdgcn_rcpf(both_sum(sp)), rn = __builtin_amdgcn_rcpf(both_sum(sn));
            const float rp3 = rp * live, rn3 = rn * live;
            const unsigned img = c.lds + 36864 + c.wid * 4352, wr = img + c.r32 * 136 + c.hi * 8;
#pragma unroll
            for (int g = 0; g < 4; ++g) {
                const float a = g == 3 ? rp3 : rp, b = g == 3 ? rn3 : rn;
                sts64(wr + g * 16, u32x2{pk(ep[4 * g] * a, ep[4 * g + 1] * a), pk(ep[4 * g + 2] * a, ep[4 * g + 3] * a)});
                sts64(wr + 64 + g * 16, u32x2{pk(en[4 * g] * b, en[4 * g + 1] * b), pk(en[4 * g + 2] * b, en[4 * g + 3] * b)});
            }
            u32x4 v0 = pack_lo(av), v1 = pack_hi(av);
            if (c.r32 >= 28) { const unsigned v = c.r32 == 28 ? 0x3F803F80u : 0u; v0 = u32x4{v, v, v, v}; v1 = v0; }
            const unsigned rd = img + (4 * c.hi + ((c.lane & 15) >> 2)) * 136 + 32 * ((c.lane >> 4) & 1) + 8 * (c.lane & 3);
            kv0 = mfma(tr_frag(rd, rd + 8 * 136), v0, zero); kv0 = mfma(tr_frag(rd + 16 * 136, rd + 24 * 136), v1, kv0);
            kv1 = mfma(tr_frag(rd + 64, rd + 64 + 8 * 136), v0, zero); kv1 = mfma(tr_frag(rd + 64 + 16 * 136, rd + 64 + 24 * 136), v1, kv1);
        }
        lds_bar();
#pragma unroll
        for (int g = 0; g < 4; ++g) { sts128(c.lds + c.wid * 8192 + g * 1024 + voff, quad(kv0, g)); sts128(c.lds + c.wid * 8192 + 4096 + g * 1024 + voff, quad(kv1, g)); }
        lds_bar();
        const rsrc_t ro = make_rsrc(ws + WS_KVP + (size_t)(c.h * 16 + c.rb) * 8192, 8192);
#pragma unroll
        for (int i = 0; i < 2; ++i) {
            const unsigned off = (2 * c.wid + i) * 1024 + voff;
            f32x4 s = __builtin_bit_cast(f32x4, lds128(c.lds + off));
#pragma unroll
            for (int v = 1; v < 4; ++v) s += __builtin_bit_cast(f32x4, lds128(c.lds + v * 8192 + off));
            st128_dev(__builtin_bit_cast(u32x4, s), ro, off, 0);
        }
        STAMP(c, 4);
        {
            u32x4 q0 = pack_lo(aq), q1 = pack_hi(aq);
            if (c.hi) { q1[2] = 0; q1[3] = 0; }
            f32x16 p = mfma(wp[0], q0, zero); p = mfma(wp[1], q1, p);
            const float mx = both_max(max16(p)) * LOG2E, mn = both_min(min16(p)) * LOG2E;
            f32x16 ep, en;
#pragma unroll
            for (int i = 0; i < 16; ++i) { ep[i] = __builtin_amdgcn_exp2f(fmaf(p[i], LOG2E, -mx)); en[i] = __builtin_amdgcn_exp2f(fmaf(p[i], -LOG2E, mn)); }
            eq[0] = pack_lo(ep); eq[1] = pack_hi(ep); eq[2] = pack_lo(en); eq[3] = pack_hi(en);
            asm volatile("" : "+v"(eq[0]), "+v"(eq[1]), "+v"(eq[2]), "+v"(eq[3]));
        }
        wait_vm<0>();
        publish(c, ws + WS_FP + (size_t)((c.h * 4 + c.wid) * 16 + c.rb) * 8, nonce);
        STAMP(c, 5);
    }
    await(c, ws + WS_FP + (size_t)(c.h * 4 + c.wid) * 128, 15, nonce, SPIN_CAP);
    STAMP(c, 6);
    {
        const rsrc_t rp = make_rsrc(ws + WS_KVP + (size_t)c.h * 16 * 8192, 16 * 8192);
        const unsigned off = 2 * c.wid * 1024 + voff, st = c.lds + 65536;
        u32x4 pa[16], pb[16];
#pragma unroll
        for (int i = 0; i < 16; ++i) { pa[i] = ld128_dev(rp, off, i * 8192); pb[i] = ld128_dev(rp, off + 1024, i * 8192); }
        f32x4 sa = __builtin_bit_cast(f32x4, pa[0]), sb = __builtin_bit_cast(f32x4, pb[0]);
#pragma unroll
        for (int i = 1; i < 16; ++i) { sa += __builtin_bit_cast(f32x4, pa[i]); sb += __builtin_bit_cast(f32x4, pb[i]); }
        u32x4 f = {pk(sa[0], sa[1]), pk(sa[2], sa[3]), pk(sb[0], sb[1]), pk(sb[2], sb[3])};
        if (c.r32 == 29) {
            const unsigned tail = ((c.wid & 1) && c.hi) ? 0u : 0x3F803F80u;
            f = u32x4{0x3F803F80u, 0x3F803F80u, tail, tail};
        }
        sts128(st + c.wid * 1024 + voff, f);
        lds_bar();
        STAMP(c, 7);
        f32x16 np = mfma(lds128(st + voff), eq[0], zero); np = mfma(lds128(st + 1024 + voff), eq[1], np);
        f32x16 nn = mfma(lds128(st + 2048 + voff), eq[2], zero); nn = mfma(lds128(st + 3072 + voff), eq[3], nn);
        const float rp_ = __builtin_amdgcn_rcpf(np[13]), rn_ = __builtin_amdgcn_rcpf(nn[13]);
        const float inv = __builtin_amdgcn_rcpf(fmaf(np[12], rp_, fmaf(nn[12], rn_, 1e-6f)));
        const float ap = upper_val(rp_ * inv), an = upper_val(rn_ * inv);
        f32x16 o;
#pragma unroll
        for (int i = 0; i < 16; ++i) o[i] = fmaf(np[i], ap, nn[i] * an);
        u32x4 f0 = pack_lo(o), f1 = pack_hi(o);
        if (c.hi) { f1[2] = 0; f1[3] = 0; }
        const rsrc_t ro = make_rsrc(ws + WS_OB + (size_t)((c.rb * 4 + c.wid) * 16 + c.h) * 2048, 2048);
        st128_dev(f0, ro, voff, 0); st128_dev(f1, ro, voff + 1024, 0);
    }
}

__global__ __launch_bounds__(256, 2) void fused(const u16* xa, const u16* wa, const u16* woa, const u16* xb, const u16* wb, const u16* wphi, const u16* wob, u16* out, char* ws) {
    Ctx c;
    c.lane = threadIdx.x & 63; c.wid = __builtin_amdgcn_readfirstlane(threadIdx.x >> 6);
    c.hi = c.lane >> 5; c.r32 = c.lane & 31;
    const int b = blockIdx.x & 255, xcd = b & 7, i = b >> 3;
    c.h = (xcd & 1) * 8 + (i & 7); c.rb = (xcd >> 1) * 4 + (i >> 3);
    c.rot = ROT ? (2 * (i >> 3) + (i & 7)) % 7 : 0;
    STAMP(c, 0);
    const bool is_a = blockIdx.x < 256;
    u64 nonce;
    if (is_a) branch_a(fresh(c), xa, wa, ws, nonce);
    else branch_b(fresh(c), xb, wb, wphi, ws, nonce);
    c = fresh(c);
    char* flags = ws + (is_a ? WS_FOA : WS_FOB);
    char* oflag = flags + (size_t)((c.rb * 4 + c.wid) * 16 + c.h) * 8;
    wait_vm<0>();
    publish(c, oflag, nonce);
    STAMP(c, 10);
    if (c.h < 14) {
        out_prefetch(c, is_a ? woa : wob);
        phase_out(c, ws + (is_a ? WS_OA : WS_OB), flags, nonce, out + (is_a ? 0 : DM), ws + (is_a ? WS_EPOCH_A : WS_EPOCH_B));
    }
    STAMP(c, 13);
    if (blockIdx.x == DIAGBLK && c.wid == 0) {
        const u64 t0 = *(LAS u64*)(size_t)(c.lds + LDS_BYTES - 512), t1 = *(LAS u64*)(size_t)(c.lds + LDS_BYTES - 512 + 8);
        const u64 target = t0 + 10000 + (DIAG < 0 ? 0 : 4 * (t1 - t0));
        while (__builtin_amdgcn_s_memrealtime() < target) __builtin_amdgcn_s_sleep(1);
    }
}

}

extern "C" void kernel_launch(void* const* d_in, const int* in_sizes, int n_in, void* d_out, int out_size, void* d_ws, size_t ws_size, hipStream_t stream) {
    (void)hipFuncSetAttribute((const void*)fused, hipFuncAttributeMaxDynamicSharedMemorySize, LDS_BYTES);
    fused<<<512, 256, LDS_BYTES, stream>>>((const u16*)d_in[0], (const u16*)d_in[1], (const u16*)d_in[2], (const u16*)d_in[3], (const u16*)d_in[4], (const u16*)d_in[5],
                                           (const u16*)d_in[6], (u16*)d_out, (char*)d_ws);
}
```

```cpp
#include <hip/hip_runtime.h>
#include <stdint.h>

namespace {

typedef unsigned short u16;
typedef unsigned long long u64;
typedef __attribute__((ext_vector_type(16))) float f32x16;
typedef __attribute__((ext_vector_type(4))) float f32x4;
typedef __attribute__((ext_vector_type(2))) float f32x2;
typedef __attribute__((ext_vector_type(8))) __bf16 bf16x8;
typedef __attribute__((ext_vector_type(2))) __bf16 bf16x2;
typedef __attribute__((ext_vector_type(4))) unsigned u32x4;
typedef __attribute__((ext_vector_type(2))) unsigned u32x2;
typedef __attribute__((ext_vector_type(4))) short s16x4;
typedef __amdgpu_buffer_rsrc_t rsrc_t;
#define LAS __attribute__((address_space(3)))
#define GAS __attribute__((address_space(1)))
#define DI __device__ __forceinline__
#define SBAR() __builtin_amdgcn_sched_barrier(0)

constexpr int SEQ = 2048, DM = 448;
constexpr unsigned XROW = DM * 2;
constexpr unsigned WROW = 3 * DM * 2;
constexpr unsigned X_BYTES = SEQ * XROW, W_BYTES = DM * WROW, WO_BYTES = DM * XROW;
constexpr size_t WS_EPOCH_A = 0, WS_EPOCH_B = 256;
constexpr size_t WS_FK = 4096;
constexpr size_t WS_FP = WS_FK + 8192;
constexpr size_t WS_FOA = WS_FP + 8192;
constexpr size_t WS_FOB = WS_FOA + 8192;
constexpr size_t WS_FW = WS_FOB + 8192;
constexpr size_t WS_FWO = WS_FW + 2048;
constexpr size_t WS_KVA = 65536;
constexpr size_t WS_OA = WS_KVA + (4u << 20);
constexpr size_t WS_OB = WS_OA + (2u << 20);
constexpr size_t WS_KVP = WS_OB + (2u << 20);
constexpr size_t WS_WB = WS_KVP + (2u << 20);
constexpr size_t WS_WO = WS_WB + (2u << 20);
constexpr int LDS_BYTES = 80 * 1024;
constexpr float LOG2E = 1.4426950408889634f;
constexpr float QSCALE = 0.18898223650461362f * LOG2E;
#define REF_OFF 32.f
constexpr float REF_OFFSET = REF_OFF;
constexpr float L_LIMIT = 18446744073709551616.f;
constexpr int SC1 = 16;
constexpr unsigned SPIN_CAP = 1u << 18;
#define ROT 1
#define BAHEAD 4
#define PSLEEP 6
#define BSTART 500
#define OUTAUX 16
#define QSPLIT 12

DI f32x16 mfma(u32x4 a, u32x4 b, f32x16 c) {
    return __builtin_amdgcn_mfma_f32_32x32x16_bf16(__builtin_bit_cast(bf16x8, a), __builtin_bit_cast(bf16x8, b), c, 0, 0, 0);
}
DI unsigned pk(float lo, float hi) { f32x2 v = {lo, hi}; return __builtin_bit_cast(unsigned, __builtin_convertvector(v, bf16x2)); }
DI u32x4 pack_lo(const f32x16& x) { return u32x4{pk(x[0], x[1]), pk(x[2], x[3]), pk(x[4], x[5]), pk(x[6], x[7])}; }
DI u32x4 pack_hi(const f32x16& x) { return u32x4{pk(x[8], x[9]), pk(x[10], x[11]), pk(x[12], x[13]), pk(x[14], x[15])}; }
DI u32x4 quad(const f32x16& x, int g) { return u32x4{__float_as_uint(x[4 * g]), __float_as_uint(x[4 * g + 1]), __float_as_uint(x[4 * g + 2]), __float_as_uint(x[4 * g + 3])}; }
DI rsrc_t make_rsrc(const void* p, unsigned bytes) { return __builtin_amdgcn_make_buffer_rsrc((void*)p, 0, bytes, 0x00020000); }
DI u32x4 ld128(rsrc_t rs, unsigned voff, unsigned soff) { return __builtin_amdgcn_raw_buffer_load_b128(rs, voff, soff, 0); }
DI u32x4 ld128_dev(rsrc_t rs, unsigned voff, unsigned soff) { return __builtin_amdgcn_raw_buffer_load_b128(rs, voff, soff, SC1); }
DI void st128_dev(u32x4 v, rsrc_t rs, unsigned voff, unsigned soff) { __builtin_amdgcn_raw_buffer_store_b128(v, rs, voff, soff, SC1); }
DI void dma16(rsrc_t rs, unsigned lds_addr, unsigned voff, unsigned soff) {
    asm volatile("s_mov_b32 m0, %0\n\ts_nop 0\n\tbuffer_load_dwordx4 %1, %2, %3 offen lds" ::"s"(lds_addr), "v"(voff), "s"(rs), "s"(soff) : "memory");
}
DI void ld128_async(u32x4& d, rsrc_t rs, unsigned voff, unsigned soff) {
    asm volatile("buffer_load_dwordx4 %0, %1, %2, %3 offen" : "=v"(d) : "v"(voff), "s"(rs), "s"(soff) : "memory");
}
DI void dma16_dev(rsrc_t rs, unsigned lds_addr, unsigned voff, unsigned soff) {
    asm volatile("s_mov_b32 m0, %0\n\ts_nop 0\n\tbuffer_load_dwordx4 %1, %2, %3 offen sc1 lds" ::"s"(lds_addr), "v"(voff), "s"(rs), "s"(soff) : "memory");
}
template <int N> DI void wait_vm() { asm volatile("s_waitcnt vmcnt(%0)" ::"n"(N) : "memory"); }
template <int N> DI void wait_bar() { asm volatile("s_waitcnt vmcnt(%0) lgkmcnt(0)\n\ts_barrier" ::"n"(N) : "memory"); }
DI void lds_bar() { asm volatile("s_waitcnt lgkmcnt(0)\n\ts_barrier" ::: "memory"); }
DI u32x4 lds128(unsigned a) { return *(const LAS u32x4*)(size_t)a; }
DI void sts128(unsigned a, u32x4 v) { *(LAS u32x4*)(size_t)a = v; }
DI void sts64(unsigned a, u32x2 v) { *(LAS u32x2*)(size_t)a = v; }
DI u32x2 tr64(unsigned a) { return __builtin_bit_cast(u32x2, __builtin_amdgcn_ds_read_tr16_b64_v4i16((LAS s16x4*)(size_t)a)); }
DI u32x4 tr_frag(unsigned a0, unsigned a1) { u32x2 lo = tr64(a0), hi = tr64(a1); return u32x4{lo[0], lo[1], hi[0], hi[1]}; }
DI float both_max(float v) { auto r = __builtin_amdgcn_permlane32_swap(__float_as_uint(v), __float_as_uint(v), false, false); return fmaxf(__uint_as_float(r[0]), __uint_as_float(r[1])); }
DI float both_min(float v) { auto r = __builtin_amdgcn_permlane32_swap(__float_as_uint(v), __float_as_uint(v), false, false); return fminf(__uint_as_float(r[0]), __uint_as_float(r[1])); }
DI float both_sum(float v) { auto r = __builtin_amdgcn_permlane32_swap(__float_as_uint(v), __float_as_uint(v), false, false); return __uint_as_float(r[0]) + __uint_as_float(r[1]); }
DI float upper_val(float v) { auto r = __builtin_amdgcn_permlane32_swap(__float_as_uint(v), __float_as_uint(v), false, false); return __uint_as_float(r[1]); }
DI float max16(const f32x16& s) {
    float a = fmaxf(fmaxf(s[0], s[1]), s[2]), b = fmaxf(fmaxf(s[3], s[4]), s[5]);
    a = fmaxf(fmaxf(a, s[6]), s[7]); b = fmaxf(fmaxf(b, s[8]), s[9]);
    a = fmaxf(fmaxf(a, s[10]), s[11]); b = fmaxf(fmaxf(b, s[12]), s[13]);
    a = fmaxf(fmaxf(a, s[14]), s[15]);
    return fmaxf(a, b);
}
DI float min16(const f32x16& s) {
    float a = fminf(fminf(s[0], s[1]), s[2]), b = fminf(fminf(s[3], s[4]), s[5]);
    a = fminf(fminf(a, s[6]), s[7]); b = fminf(fminf(b, s[8]), s[9]);
    a = fminf(fminf(a, s[10]), s[11]); b = fminf(fminf(b, s[12]), s[13]);
    a = fminf(fminf(a, s[14]), s[15]);
    return fminf(a, b);
}

#define STAMP(c, id) do {} while (0)

struct Ctx {
    int lane, wid, hi, r32, h, rb, rot;
    static constexpr unsigned lds = 0;
};

DI Ctx fresh(const Ctx& c) {
    Ctx n = c;
    asm volatile("v_mbcnt_lo_u32_b32 %0, -1, 0\n\tv_mbcnt_hi_u32_b32 %0, -1, %0" : "=v"(n.lane));
    n.hi = n.lane >> 5; n.r32 = n.lane & 31;
    return n;
}
DI int chunk_at(const Ctx& c, int p) {
    if (ROT == 0 || p == 6) return p;
    const int v = p % 3 + c.rot;
    return p - p % 3 + (v >= 3 ? v - 3 : v);
}
DI u64 load_word(const char* p) { return __hip_atomic_load((GAS u64*)(size_t)p, __ATOMIC_RELAXED, __HIP_MEMORY_SCOPE_AGENT); }
DI void store_word(char* p, u64 v) { __hip_atomic_store((GAS u64*)(size_t)p, v, __ATOMIC_RELAXED, __HIP_MEMORY_SCOPE_AGENT); }
DI void epoch_request(u64& v, const char* p) { asm volatile("s_load_dwordx2 %0, %1, 0x0" : "=s"(v) : "s"(p) : "memory"); }
DI void publish(const Ctx& c, char* word, u64 nonce) { if (c.lane == 0) store_word(word, nonce); }
template <int PER = 1> DI void await(const Ctx& c, const char* words, int n, u64 nonce, unsigned cap, int extra_sleeps = 0) {
    const char* p = words + c.lane * 8 * PER;
#pragma unroll 1
    for (unsigned spins = 0; spins < cap; ++spins) {
        bool ok = true;
        if (c.lane < n) {
#pragma unroll
            for (int i = 0; i < PER; ++i) ok &= load_word(p + 8 * i) == nonce;
        }
        if (__all(ok)) break;
        __builtin_amdgcn_s_sleep(PSLEEP);
        for (int i = 0; i < extra_sleeps; ++i) __builtin_amdgcn_s_sleep(PSLEEP);
    }
    __builtin_amdgcn_fence(__ATOMIC_ACQUIRE, "wavefront");
    asm volatile("" ::: "memory");
}

DI void phase_out(const Ctx& c, int br, char* ws, u64 nonce, u16* out) {
    const int qt = c.rb * 4 + (c.h & 3), nt = (c.h & 12) + c.wid, live = nt < 14;
    const unsigned voff = c.lane * 16;
    const bool keeper = c.h == 0 && c.rb == 0 && c.wid == 0;
    u32x4 wf[32];
    if (live) {
        await(c, ws + WS_FWO + (size_t)(br * 2 + nt / 7) * 128, keeper ? 32 : 16, nonce, SPIN_CAP);
        const rsrc_t rw = make_rsrc(ws + WS_WO + (size_t)(br * 14 + nt) * 32768, 32768);
#pragma unroll
        for (int i = 0; i < 32; ++i) wf[i] = ld128_dev(rw, voff + (i & 3) * 1024, (i >> 2) * 4096);
    }
    if (!br) await(c, ws + WS_FOA + (size_t)((c.rb * 4 + c.wid) * 16 + c.h) * 8, 1, nonce, SPIN_CAP, 2);
    await(c, ws + (br ? WS_FOB : WS_FOA) + (size_t)(qt * 16 + c.wid * 4) * 8, 4, nonce, SPIN_CAP);
    if (br) STAMP(c, 11); else STAMP(c, 9);
    {
        const rsrc_t ro = make_rsrc(ws + (br ? WS_OB : WS_OA) + (size_t)(qt * 16 + c.wid * 4) * 2048, 8192);
        u32x4 of[8];
#pragma unroll
        for (int i = 0; i < 8; ++i) of[i] = ld128_dev(ro, voff + (i & 3) * 1024, (i >> 2) * 4096);
#pragma unroll
        for (int i = 0; i < 8; ++i) sts128(c.lds + (c.wid * 8 + i) * 1024 + voff, of[i]);
    }
    lds_bar();
    if (keeper && c.lane == 0) store_word(ws + (br ? WS_EPOCH_B : WS_EPOCH_A), nonce);
    const unsigned img = c.lds + 32768;
    if (live) {
        f32x16 r = {};
#pragma unroll
        for (int i = 0; i < 32; ++i) r = mfma(wf[i], lds128(c.lds + i * 1024 + voff), r);
        if (br) STAMP(c, 12); else STAMP(c, 15);
        const u32x4 lo = pack_lo(r), hi = pack_hi(r);
        const unsigned wr = img + c.r32 * 272 + c.wid * 64 + c.hi * 8;
        sts64(wr, u32x2{lo[0], lo[1]}); sts64(wr + 16, u32x2{lo[2], lo[3]}); sts64(wr + 32, u32x2{hi[0], hi[1]}); sts64(wr + 48, u32x2{hi[2], hi[3]});
    }
    lds_bar();
    const unsigned width = c.h >= 12 ? 128 : 256;
    const rsrc_t rd = make_rsrc((char*)out + (size_t)qt * 32 * (2 * XROW) + br * XROW + (c.h >> 2) * 256, 31 * 2 * XROW + width);
    const unsigned colb = (c.lane & 15) * 16;
    if (colb < width) {
#pragma unroll
        for (int i = 0; i < 2; ++i) {
            const unsigned row = c.wid * 8 + i * 4 + (c.lane >> 4);
            __builtin_amdgcn_raw_buffer_store_b128(lds128(img + row * 272 + colb), rd, row * (2 * XROW) + colb, 0, OUTAUX);
        }
    }
}

DI void retile(const Ctx& c, int u, const u16* wb, const u16* woa, const u16* wob, char* ws, u64& epoch_a, u64& epoch_b) {
    const unsigned voff = c.lane * 16, piece = c.r32 < 29 ? c.r32 : 28;
    const unsigned rd = c.lds + ((c.lane & 15) >> 2) * 512 + 32 * ((c.lane >> 4) & 1) + 8 * (c.lane & 3);
    char* word;
    bool for_a = false;
    if (u < 168) {
        const int g = u % 28, cg = u / 28;
        const rsrc_t rw = make_rsrc(wb, W_BYTES);
#pragma unroll
        for (int j = 0; j < 2; ++j) {
            const int i = 4 * c.wid + 2 * j + c.hi;
            dma16(rw, c.lds + (2 * c.wid + j) * 1024, (64 * (g >> 2) + 8 * (g & 3) + (i & 7) + 32 * (i >> 3)) * WROW + cg * 448 + piece * 16, 0);
        }
        wait_bar<0>();
#pragma unroll
        for (int j = 0; j < 2; ++j) {
            const int hl = 2 * c.wid + j;
            const unsigned a = rd + 8 * c.hi * 512 + hl * 56;
            const rsrc_t ro = make_rsrc(ws + WS_WB + (size_t)((((cg & 1) * 8 + hl) * 28 + g) * 3 + (cg >> 1)) * 1024, 1024);
            st128_dev(tr_frag(a, a + 4 * 512), ro, voff, 0);
        }
        word = ws + WS_FW + (size_t)(((cg & 1) * 3 + (cg >> 1)) * 28 + g) * 8;
    } else if (u < 232) {
        const int v = u - 168, br = v >> 5, h = (v >> 1) & 15, half = v & 1;
        const rsrc_t rw = make_rsrc(br ? wob : woa, WO_BYTES);
        for (int j = c.wid; j < 14; j += 4) dma16(rw, c.lds + j * 1024, (28 * h + 2 * j + c.hi) * XROW + half * 448 + (c.r32 < 28 ? c.r32 : 27) * 16, 0);
        wait_bar<0>();
        for (int f = c.wid; f < 14; f += 4) {
            const int s = f & 1, t = f >> 1;
            const unsigned a = rd + (16 * s + 4 * c.hi) * 512 + t * 64;
            const rsrc_t ro = make_rsrc(ws + WS_WO + (size_t)(((br * 14 + half * 7 + t) * 16 + h) * 2 + s) * 1024, 1024);
            st128_dev(tr_frag(a, a + (s && c.hi ? 4 : 8) * 512), ro, voff, 0);
        }
        word = ws + WS_FWO + (size_t)((br * 2 + half) * 16 + h) * 8; for_a = !br;
    } else return;
    wait_bar<0>();
    asm volatile("" : "+s"(epoch_a), "+s"(epoch_b));
    if (c.wid == 0) publish(c, word, (for_a ? epoch_a : epoch_b) + 1);
}

#define CM(d, other, own) "v_cndmask_b32_dpp %" #d ", %" #other ", %" #own ", vcc quad_perm:" PERM " row_mask:0xf bank_mask:0xf\n\t"
#define BFLY(NAME, MASK) \
    DI void NAME(u32x4& d0, u32x4& d1, const u32x4& own0, const u32x4& oth0, const u32x4& own1, const u32x4& oth1) { \
        unsigned a0, a1, a2, a3, b0, b1, b2, b3; \
        asm volatile("s_mov_b32 vcc_lo, " MASK "\n\ts_mov_b32 vcc_hi, " MASK "\n\t" \
                     CM(0, 12, 8) CM(1, 13, 9) CM(2, 14, 10) CM(3, 15, 11) CM(4, 20, 16) CM(5, 21, 17) CM(6, 22, 18) CM(7, 23, 19) \
                     : "=&v"(a0), "=&v"(a1), "=&v"(a2), "=&v"(a3), "=&v"(b0), "=&v"(b1), "=&v"(b2), "=&v"(b3) \
                     : "v"(own0[0]), "v"(own0[1]), "v"(own0[2]), "v"(own0[3]), "v"(oth0[0]), "v"(oth0[1]), "v"(oth0[2]), "v"(oth0[3]), \
                       "v"(own1[0]), "v"(own1[1]), "v"(own1[2]), "v"(own1[3]), "v"(oth1[0]), "v"(oth1[1]), "v"(oth1[2]), "v"(oth1[3]) : "vcc"); \
        d0 = u32x4{a0, a1, a2, a3}; d1 = u32x4{b0, b1, b2, b3}; }
#define PERM "[1,0,3,2]"
BFLY(bfly1_lo, "0x55555555") BFLY(bfly1_hi, "0xaaaaaaaa")
#undef PERM
#define PERM "[2,3,0,1]"
BFLY(bfly2_lo, "0x33333333") BFLY(bfly2_hi, "0xcccccccc")
#undef PERM
#undef BFLY
#undef CM
DI void mfma_pair(int isv, u32x4 xf, u32x4 w0, u32x4 w1, f32x16& acc0, f32x16& acc1) {
    asm("s_cmp_lg_u32 %5, 0\n\ts_cbranch_scc1 1f\n\t"
        "v_mfma_f32_32x32x16_bf16 %0, %3, %2, %0\n\tv_mfma_f32_32x32x16_bf16 %1, %4, %2, %1\n\ts_branch 2f\n"
        "1:\n\tv_mfma_f32_32x32x16_bf16 %0, %2, %3, %0\n\tv_mfma_f32_32x32x16_bf16 %1, %2, %4, %1\n2:"
        : "+v"(acc0), "+v"(acc1) : "v"(xf), "v"(w0), "v"(w1), "s"(isv) : "scc");
}
template <int N> DI void landed4(u32x4 (&r)[28], int ch) {
    asm volatile("s_waitcnt vmcnt(%4)" : "+v"(r[4 * ch]), "+v"(r[4 * ch + 1]), "+v"(r[4 * ch + 2]), "+v"(r[4 * ch + 3]) : "n"(N) : "memory");
}
DI void kv_gemm(const Ctx& c, int isv, rsrc_t rx, rsrc_t rw, f32x16& acc0, f32x16& acc1, u32x4 (&xf)[28]) {
    const unsigned vwq = (16 * c.wid + (c.lane >> 2)) * WROW + c.h * 56 + (c.lane & 3) * 16;
    const int row = c.lane >> 3;
    const unsigned piece = (c.lane & 7) ^ (((row >> 1) & 1) << 2);
    const unsigned vw = (16 * c.wid + row) * WROW + (1 + isv) * XROW + (c.h >> 1) * 112 + ((piece < 7 ? piece : 6) << 4);
    const unsigned vx = (c.rb * 128 + 32 * c.wid + (c.r32 & 28)) * XROW + c.hi * 64 + (c.lane & 3) * 16;
    auto issue = [&](int ch) {
        const unsigned k = chunk_at(c, ch);
        dma16(rw, c.lds + ch * 8192 + c.wid * 2048, vw, k * 64 * WROW);
        dma16(rw, c.lds + ch * 8192 + c.wid * 2048 + 1024, vw, (k * 64 + 8) * WROW);
#pragma unroll
        for (int i = 0; i < 4; ++i) ld128_async(xf[4 * ch + i], rx, vx, k * 128 + i * XROW);
    };
    issue(0); issue(1); issue(2); issue(3);
    asm volatile("s_waitcnt lgkmcnt(0)" ::: "memory");
    STAMP(c, 1);
    const int q = (c.lane & 15) >> 2;
    const unsigned col = 32 * ((c.lane >> 4) & 1) + 8 * (c.lane & 3);
    const unsigned w0 = c.lds + (32 * c.hi + q) * 128 + (col ^ ((q >> 1) << 6)), w1 = c.lds + (32 * c.hi + q) * 128 + ((col + 56) ^ ((q >> 1) << 6));
    acc0 = f32x16{}; acc1 = f32x16{};
    asm volatile("s_nop 1" : "+v"(acc0), "+v"(acc1));
    u32x4 wf[2][8];
#pragma unroll
    for (int p = 0; p <= 7; ++p) {
        if (p < 7) {
            if (p <= 3) landed4<18>(xf, p); else if (p == 4) landed4<12 + 7>(xf, p); else if (p == 5) landed4<6 + 7>(xf, p); else landed4<7>(xf, p);
            asm volatile("s_barrier" ::: "memory");
            if (p == 0) STAMP(c, 2);
            if (p == 6) STAMP(c, 3);
        }
        u32x4 t0, t1, t2, t3;
#pragma unroll
        for (int s = 0; s < 4; ++s) {
            if (p < 7) {
                const unsigned o = p * 8192 + s * 1024;
                wf[p & 1][2 * s] = tr_frag(w0 + o, w0 + o + 512); wf[p & 1][2 * s + 1] = tr_frag(w1 + o, w1 + o + 512);
            }
            SBAR();
            if (p > 0) mfma_pair(isv, xf[4 * p - 4 + s], wf[(p - 1) & 1][2 * s], wf[(p - 1) & 1][2 * s + 1], acc0, acc1);
            SBAR();
            if (p < 7) {
                u32x4 &r0 = xf[4 * p], &r1 = xf[4 * p + 1], &r2 = xf[4 * p + 2], &r3 = xf[4 * p + 3];
                if (s == 0) bfly1_lo(t0, t2, r0, r1, r2, r3);
                if (s == 1) bfly1_hi(t1, t3, r1, r0, r3, r2);
                if (s == 2) bfly2_lo(r0, r1, t0, t2, t1, t3);
                if (s == 3) bfly2_hi(r2, r3, t2, t0, t3, t1);
            }
            SBAR();
        }
        if (p + 4 <= 6) issue(p + 4);
        if (p == 3) {
#pragma unroll
            for (int j = 0; j < 7; ++j) dma16(rw, c.lds + (j < 6 ? 57344 + j * 4096 : 0) + c.wid * 1024, vwq, chunk_at(c, j) * 64 * WROW);
        }
    }
    asm volatile("s_nop 11" : "+v"(acc0), "+v"(acc1));
}

struct KV { u32x4 k0, k1, v0, v1; };

DI void set_reference(u32x4& q1, float nm, int hi) {
    const unsigned a = __float_as_uint(nm) & 0xFFFF0000u;
    const float r1 = nm - __uint_as_float(a);
    const unsigned b = __float_as_uint(r1) & 0xFFFF0000u;
    const float r2 = r1 - __uint_as_float(b);
    if (hi) { q1[2] = (a >> 16) | b; q1[3] = __float_as_uint(r2) >> 16; }
}

DI void branch_a(const Ctx& c, const u16* x, const u16* w, char* ws, u64& nonce) {
    u64 epoch; epoch_request(epoch, ws + WS_EPOCH_A);
    __builtin_amdgcn_s_setprio(3);
    const rsrc_t rx = make_rsrc(x, X_BYTES), rw = make_rsrc(w, W_BYTES);
    const unsigned voff = c.lane * 16;
    u32x4 q[4][2];
    const rsrc_t rk = make_rsrc(ws + WS_KVA + (size_t)(c.h * 64 + c.wid * 16) * 4096, 16 * 4096);
#define LDKV(X, j) do { X.v1 = ld128_dev(rk, voff + 3072, (j) * 4096); X.v0 = ld128_dev(rk, voff + 2048, (j) * 4096); \
                        X.k1 = ld128_dev(rk, voff + 1024, (j) * 4096); X.k0 = ld128_dev(rk, voff, (j) * 4096); } while (0)
    KV a, b, cc, d;
    {
        u32x4 xf[28];
        f32x16 acc0, acc1;
        const int isv = c.h & 1;
        kv_gemm(c, isv, rx, rw, acc0, acc1, xf);
        asm volatile("" : "+s"(epoch));
        u32x4 f[4] = {pack_lo(acc0), pack_hi(acc0), pack_lo(acc1), pack_hi(acc1)};
        {
            const bool all = isv && c.r32 >= 28, tail = isv ? all : (bool)c.hi;
            const unsigned va = c.r32 == 28 ? 0x3F803F80u : 0u, v2 = isv ? va : 0x3F803F80u, v3 = isv ? va : 0x00003F80u;
#pragma unroll
            for (int i = 0; i < 4; ++i) {
                f[i][0] = all ? va : f[i][0]; f[i][1] = all ? va : f[i][1];
                if (i & 1) { f[i][2] = tail ? v2 : f[i][2]; f[i][3] = tail ? v3 : f[i][3]; }
                else       { f[i][2] = all ? va : f[i][2]; f[i][3] = all ? va : f[i][3]; }
            }
        }
        const int kt = c.rb * 4 + c.wid;
        const rsrc_t ro = make_rsrc(ws + WS_KVA + (size_t)((c.h & 14) * 64 + kt) * 4096 + isv * 2048, 64 * 4096 + 2048);
        st128_dev(f[0], ro, voff, 0); st128_dev(f[1], ro, voff + 1024, 0); st128_dev(f[2], ro, voff, 64 * 4096); st128_dev(f[3], ro, voff + 1024, 64 * 4096);
        STAMP(c, 4);
        const unsigned xb = c.lds + 32768;
        nonce = epoch + 1;
        wait_bar<4>();
        const unsigned wo = c.lds + (32 * c.hi + ((c.lane & 15) >> 2)) * 64 + 32 * ((c.lane >> 4) & 1) + 8 * (c.lane & 3);
        f32x16 acc = {};
#pragma unroll
        for (int i = 0; i < 28; ++i) {
            if (i == QSPLIT) {
                wait_vm<0>();
                publish(c, ws + WS_FK + (size_t)(((c.h >> 1) * 64 + kt) * 2 + isv) * 8, nonce);
                STAMP(c, 5);
            }
            const unsigned o = (i < 24 ? 57344 + (i >> 2) * 4096 : 0) + (i & 3) * 512;
            acc = mfma(tr_frag(wo + o, wo + o + 256), xf[i], acc);
        }
        acc *= QSCALE;
        u32x4 q0 = pack_lo(acc), q1 = pack_hi(acc);
        if (c.hi) { q1[2] = 0; q1[3] = 0; }
        sts128(xb + c.wid * 2048 + voff, q0); sts128(xb + c.wid * 2048 + 1024 + voff, q1);
        lds_bar();
#pragma unroll
        for (int t = 0; t < 4; ++t) { const unsigned a = xb + ((c.wid + t) & 3) * 2048 + voff; q[t][0] = lds128(a); q[t][1] = lds128(a + 1024); }
        STAMP(c, 6);
        await(c, ws + WS_FK + (size_t)((c.h >> 1) * 64 + c.wid * 16) * 16, 32, nonce, SPIN_CAP);
        STAMP(c, 7);
        LDKV(a, 0); LDKV(b, 1); LDKV(cc, 2);
    }
    const Ctx c0 = c;
    const unsigned comb = c.lds, stat = c.lds + 49152, flagw = c.lds + 57344;
    if (c.wid == 0 && c.lane == 0) *(LAS unsigned*)(size_t)flagw = 0;
    const f32x16 zero = {};
    for (int exact = 0;; ++exact) {
        float ref[4];
        if (exact) { LDKV(a, 0); LDKV(b, 1); LDKV(cc, 2); }
#pragma unroll
        for (int t = 0; t < 4; ++t) { f32x16 s = mfma(a.k0, q[t][0], zero); s = mfma(a.k1, q[t][1], s); ref[t] = max16(s); }
        if (exact) {
            for (int j = 1; j < 16; ++j) {
                const u32x4 k0 = ld128_dev(rk, voff, j * 4096), k1 = ld128_dev(rk, voff + 1024, j * 4096);
#pragma unroll
                for (int t = 0; t < 4; ++t) { f32x16 s = mfma(k0, q[t][0], zero); s = mfma(k1, q[t][1], s); ref[t] = fmaxf(ref[t], max16(s)); }
            }
        }
#pragma unroll
        for (int t = 0; t < 4; ++t) { ref[t] = both_max(ref[t]) + (exact ? 0.f : REF_OFFSET); set_reference(q[t][1], -ref[t], c.hi); }
        STAMP(c, 8);
        __builtin_amdgcn_s_setprio(1);
        f32x16 o0 = {}, o1 = {}, o2 = {}, o3 = {};
        f32x16 sa, sb; u32x4 pa, pb = {};
        float e12 = 0.f, e13 = 0.f, e14 = 0.f, e15 = 0.f;
        d.v1 = u32x4{};
        sa = mfma(a.k0, q[0][0], zero); sa = mfma(a.k1, q[0][1], sa);
#define EX(S, i) S[i] = __builtin_amdgcn_exp2f(S[i])
#define STEP(SC, SN, KN0, KN1, QN0, QN1, OP, VP1, OC, VC0) do { \
    SN = mfma(KN0, QN0, zero); SBAR(); \
    pb[2] = pk(e12, e13); SBAR(); EX(SC, 0); SBAR(); pb[3] = pk(e14, e15); SBAR(); EX(SC, 1); EX(SC, 2); EX(SC, 3); SBAR(); \
    OP = mfma(VP1, pb, OP); SBAR(); \
    EX(SC, 4); EX(SC, 5); SBAR(); pa[0] = pk(SC[0], SC[1]); SBAR(); EX(SC, 6); SBAR(); pa[1] = pk(SC[2], SC[3]); SBAR(); EX(SC, 7); SBAR(); \
    SN = mfma(KN1, QN1, SN); SBAR(); \
    EX(SC, 8); SBAR(); pa[2] = pk(SC[4], SC[5]); SBAR(); EX(SC, 9); SBAR(); pa[3] = pk(SC[6], SC[7]); SBAR(); EX(SC, 10); EX(SC, 11); SBAR(); \
    OC = mfma(VC0, pa, OC); SBAR(); \
    e12 = __builtin_amdgcn_exp2f(SC[12]); SBAR(); pb[0] = pk(SC[8], SC[9]); SBAR(); e13 = __builtin_amdgcn_exp2f(SC[13]); SBAR(); pb[1] = pk(SC[10], SC[11]); SBAR(); \
    e14 = __builtin_amdgcn_exp2f(SC[14]); e15 = __builtin_amdgcn_exp2f(SC[15]); SBAR(); } while (0)
#define TILE(CUR, PREV, NXT, jload) do { \
    STEP(sa, sb, CUR.k0, CUR.k1, q[1][0], q[1][1], o3, PREV.v1, o0, CUR.v0); LDKV(PREV, jload); SBAR(); \
    STEP(sb, sa, CUR.k0, CUR.k1, q[2][0], q[2][1], o0, CUR.v1, o1, CUR.v0); \
    STEP(sa, sb, CUR.k0, CUR.k1, q[3][0], q[3][1], o1, CUR.v1, o2, CUR.v0); \
    STEP(sb, sa, NXT.k0, NXT.k1, q[0][0], q[0][1], o2, CUR.v1, o3, CUR.v0); } while (0)
        for (int j = 0; j < 16; j += 4) { TILE(a, d, b, j + 3); TILE(b, a, cc, j + 4); TILE(cc, b, d, j + 5); TILE(d, cc, a, j + 6); }
        pb[2] = pk(e12, e13); pb[3] = pk(e14, e15);
        o3 = mfma(d.v1, pb, o3);
#undef TILE
#undef STEP
#undef EX
        __builtin_amdgcn_s_setprio(3);
        STAMP(c, 9);
        const Ctx c = fresh(c0);
        const unsigned voff = c.lane * 16;
        bool bad = false;
        float mine, e0;
        {
            const f32x16* ot[4] = {&o0, &o1, &o2, &o3};
#pragma unroll
            for (int t = 0; t < 4; ++t) {
                const float l = (*ot[t])[12], e = ref[t] + __builtin_amdgcn_logf(l);
                bad |= c.hi && !(l < L_LIMIT);
                if (t == 0) { mine = ref[0]; e0 = upper_val(e); }
                else {
                    const unsigned slot = ((c.wid + t) & 3) * 3 + (t - 1);
                    if (c.hi) sts64(stat + slot * 256 + c.r32 * 8, u32x2{__float_as_uint(ref[t]), __float_as_uint(e)});
#pragma unroll
                    for (int g = 0; g < 4; ++g) sts128(comb + slot * 4096 + g * 1024 + voff, quad(*ot[t], g));
                }
            }
        }
        f32x16 o = o0;
        lds_bar();
        {
            float m[3], e[3];
#pragma unroll
            for (int v = 0; v < 3; ++v) { const u32x2 s = *(const LAS u32x2*)(size_t)(stat + (c.wid * 3 + v) * 256 + c.r32 * 8); m[v] = __uint_as_float(s[0]); e[v] = __uint_as_float(s[1]); }
            const float top = fmaxf(fmaxf(e0, e[0]), fmaxf(e[1], e[2]));
            o *= __builtin_amdgcn_exp2f(mine - top);
#pragma unroll
            for (int v = 0; v < 3; ++v) {
                const float cv = __builtin_amdgcn_exp2f(m[v] - top);
#pragma unroll
                for (int g = 0; g < 4; ++g) {
                    const u32x4 p = lds128(comb + (c.wid * 3 + v) * 4096 + g * 1024 + voff);
#pragma unroll
                    for (int i = 0; i < 4; ++i) o[4 * g + i] = fmaf(__uint_as_float(p[i]), cv, o[4 * g + i]);
                }
            }
        }
        o *= __builtin_amdgcn_rcpf(upper_val(o[12]));
        float chk = 0.f;
#pragma unroll
        for (int i = 0; i < 16; ++i) chk = fmaf(o[i], 0.f, chk);
        bad |= !(chk == 0.f);
        u32x4 f0 = pack_lo(o), f1 = pack_hi(o);
        if (c.hi) { f1[2] = 0; f1[3] = 0; }
        const rsrc_t ro = make_rsrc(ws + WS_OA + (size_t)((c.rb * 4 + c.wid) * 16 + c.h) * 2048, 2048);
        st128_dev(f0, ro, voff, 0); st128_dev(f1, ro, voff + 1024, 0);
        if (!exact && __any(bad) && c.lane == 0) *(LAS unsigned*)(size_t)flagw = 1;
        lds_bar();
        if (exact || __builtin_amdgcn_readfirstlane(*(const LAS unsigned*)(size_t)flagw) == 0) break;
#pragma unroll
        for (int t = 0; t < 4; ++t) if (c.hi) { q[t][1][2] = 0; q[t][1][3] = 0; }
    }
#undef LDKV
}

DI void branch_b(const Ctx& c, const u16* x, const u16* w, const u16* wphi, const u16* woa, const u16* wob, char* ws, u64& nonce, u64& nonce_a) {
    __builtin_amdgcn_s_setprio(3);
    const u64 t0 = __builtin_amdgcn_s_memrealtime();
    u64 epoch, epoch_a; epoch_request(epoch, ws + WS_EPOCH_B); epoch_request(epoch_a, ws + WS_EPOCH_A);
    const unsigned voff = c.lane * 16;
    retile(c, blockIdx.x - 256, w, woa, wob, ws, epoch_a, epoch);
    asm volatile("s_waitcnt lgkmcnt(0)" : "+s"(epoch), "+s"(epoch_a) :: "memory");
    nonce = epoch + 1; nonce_a = epoch_a + 1;
    __builtin_amdgcn_s_setprio(2);
    STAMP(c, 8);
    while (__builtin_amdgcn_s_memrealtime() - t0 < BSTART) __builtin_amdgcn_s_sleep(4);
    __builtin_amdgcn_s_waitcnt(0xC07F);
    await<2>(c, ws + WS_FW + (size_t)(c.h >> 3) * 672, 42, nonce, SPIN_CAP);
    STAMP(c, 1);
    u32x4 eq[4];
    const f32x16 zero = {};
    {
        constexpr unsigned SLOT = 12288;
        const rsrc_t rx = make_rsrc(x, X_BYTES), rw = make_rsrc(ws + WS_WB + (size_t)c.h * 86016, 86016);
        const unsigned vx = (c.rb * 128 + 32 * c.wid + (c.r32 & 28)) * XROW + c.hi * 64 + (c.lane & 3) * 16;
        u32x4 xf[28];
        auto issue = [&](int ch) {
            const unsigned k = chunk_at(c, ch);
#pragma unroll
            for (int j = 0; j < 3; ++j) dma16_dev(rw, c.lds + (ch % 6) * SLOT + c.wid * 3072 + j * 1024, voff, k * SLOT + c.wid * 3072 + j * 1024);
#pragma unroll
            for (int i = 0; i < 4; ++i) ld128_async(xf[4 * ch + i], rx, vx, k * 128 + i * XROW);
        };
        {
            const rsrc_t rp = make_rsrc(wphi + c.h * 784, 1568);
            dma16(rp, c.lds + 77824, voff, 0); dma16(rp, c.lds + 78848, voff, 1024);
        }
        static_assert(BAHEAD >= 2 && BAHEAD <= 4, "chunk 6 takes chunk 0's slot no earlier than step 2");
#pragma unroll
        for (int ch = 0; ch < BAHEAD; ++ch) issue(ch);
        STAMP(c, 2);
        f32x16 aq = {}, ak = {}, av = {};
        u32x4 wf[2][12];
#pragma unroll
        for (int p = 0; p <= 7; ++p) {
            if (p < 7) {
                constexpr int A = BAHEAD - 1;
                if (p + A <= 6) landed4<7 * A>(xf, p); else if (p == 4) landed4<14>(xf, p); else if (p == 5) landed4<7>(xf, p); else landed4<0>(xf, p);
                asm volatile("s_barrier" ::: "memory");
            }
            u32x4 t0, t1, t2, t3;
#pragma unroll
            for (int s = 0; s < 4; ++s) {
                if (p < 7) {
                    const unsigned wa = c.lds + (p % 6) * SLOT + s * 3072 + voff;
                    wf[p & 1][3 * s] = lds128(wa); wf[p & 1][3 * s + 1] = lds128(wa + 1024); wf[p & 1][3 * s + 2] = lds128(wa + 2048);
                }
                SBAR();
                if (p > 0) {
                    const u32x4 xs = xf[4 * p - 4 + s];
                    aq = mfma(wf[(p - 1) & 1][3 * s], xs, aq); ak = mfma(wf[(p - 1) & 1][3 * s + 1], xs, ak); av = mfma(xs, wf[(p - 1) & 1][3 * s + 2], av);
                }
                SBAR();
                if (p < 7) {
                    u32x4 &r0 = xf[4 * p], &r1 = xf[4 * p + 1], &r2 = xf[4 * p + 2], &r3 = xf[4 * p + 3];
                    if (s == 0) bfly1_lo(t0, t2, r0, r1, r2, r3);
                    if (s == 1) bfly1_hi(t1, t3, r1, r0, r3, r2);
                    if (s == 2) bfly2_lo(r0, r1, t0, t2, t1, t3);
                    if (s == 3) { bfly2_hi(r2, r3, t2, t0, t3, t1); asm volatile("s_nop 1" : "+v"(r2), "+v"(r3)); }
                }
                SBAR();
            }
            if (p + BAHEAD <= 6) issue(p + BAHEAD);
        }
        STAMP(c, 3);
        u32x4 wp[2];
        {
            const unsigned col = 16 * ((c.lane >> 4) & 1) + 4 * (c.lane & 3);
            const unsigned a0 = c.lds + 77824 + (4 * c.hi + ((c.lane & 15) >> 2)) * 56 + (col < 28 ? col : 24) * 2;
            wp[0] = tr_frag(a0, a0 + 8 * 56); wp[1] = tr_frag(a0 + 16 * 56, a0 + (c.hi ? 20 : 24) * 56);
        }
        f32x16 kv0, kv1;
        {
            u32x4 k0 = pack_lo(ak), k1 = pack_hi(ak);
            if (c.hi) { k1[2] = 0; k1[3] = 0; }
            f32x16 p = mfma(wp[0], k0, zero); p = mfma(wp[1], k1, p);
            const float mx = both_max(max16(p)) * LOG2E, mn = both_min(min16(p)) * LOG2E;
            f32x16 ep, en;
#pragma unroll
            for (int i = 0; i < 16; ++i) { ep[i] = __builtin_amdgcn_exp2f(fmaf(p[i], LOG2E, -mx)); en[i] = __builtin_amdgcn_exp2f(fmaf(p[i], -LOG2E, mn)); }
            const float live = c.hi ? 0.f : 1.f;
            float sp = (ep[12] + ep[13] + ep[14] + ep[15]) * live, sn = (en[12] + en[13] + en[14] + en[15]) * live;
#pragma unroll
            for (int i = 0; i < 12; ++i) { sp += ep[i]; sn += en[i]; }
            const float rp = __builtin_amdgcn_rcpf(both_sum(sp)), rn = __builtin_amdgcn_rcpf(both_sum(sn));
            const float rp3 = rp * live, rn3 = rn * live;
            const unsigned img = c.lds + 36864 + c.wid * 4352, wr = img + c.r32 * 136 + c.hi * 8;
#pragma unroll
            for (int g = 0; g < 4; ++g) {
                const float a = g == 3 ? rp3 : rp, b = g == 3 ? rn3 : rn;
                sts64(wr + g * 16, u32x2{pk(ep[4 * g] * a, ep[4 * g + 1] * a), pk(ep[4 * g + 2] * a, ep[4 * g + 3] * a)});
                sts64(wr + 64 + g * 16, u32x2{pk(en[4 * g] * b, en[4 * g + 1] * b), pk(en[4 * g + 2] * b, en[4 * g + 3] * b)});
            }
            u32x4 v0 = pack_lo(av), v1 = pack_hi(av);
            if (c.r32 >= 28) { const unsigned v = c.r32 == 28 ? 0x3F803F80u : 0u; v0 = u32x4{v, v, v, v}; v1 = v0; }
            const unsigned rd = img + (4 * c.hi + ((c.lane & 15) >> 2)) * 136 + 32 * ((c.lane >> 4) & 1) + 8 * (c.lane & 3);
            kv0 = mfma(tr_frag(rd, rd + 8 * 136), v0, zero); kv0 = mfma(tr_frag(rd + 16 * 136, rd + 24 * 136), v1, kv0);
            kv1 = mfma(tr_frag(rd + 64, rd + 64 + 8 * 136), v0, zero); kv1 = mfma(tr_frag(rd + 64 + 16 * 136, rd + 64 + 24 * 136), v1, kv1);
        }
        lds_bar();
#pragma unroll
        for (int g = 0; g < 4; ++g) { sts128(c.lds + c.wid * 8192 + g * 1024 + voff, quad(kv0, g)); sts128(c.lds + c.wid * 8192 + 4096 + g * 1024 + voff, quad(kv1, g)); }
        lds_bar();
        const rsrc_t ro = make_rsrc(ws + WS_KVP + (size_t)(c.h * 16 + c.rb) * 8192, 8192);
#pragma unroll
        for (int i = 0; i < 2; ++i) {
            const unsigned off = (2 * c.wid + i) * 1024 + voff;
            f32x4 s = __builtin_bit_cast(f32x4, lds128(c.lds + off));
#pragma unroll
            for (int v = 1; v < 4; ++v) s += __builtin_bit_cast(f32x4, lds128(c.lds + v * 8192 + off));
            st128_dev(__builtin_bit_cast(u32x4, s), ro, off, 0);
        }
        STAMP(c, 4);
        {
            u32x4 q0 = pack_lo(aq), q1 = pack_hi(aq);
            if (c.hi) { q1[2] = 0; q1[3] = 0; }
            f32x16 p = mfma(wp[0], q0, zero); p = mfma(wp[1], q1, p);
            const float mx = both_max(max16(p)) * LOG2E, mn = both_min(min16(p)) * LOG2E;
            f32x16 ep, en;
#pragma unroll
            for (int i = 0; i < 16; ++i) { ep[i] = __builtin_amdgcn_exp2f(fmaf(p[i], LOG2E, -mx)); en[i] = __builtin_amdgcn_exp2f(fmaf(p[i], -LOG2E, mn)); }
            eq[0] = pack_lo(ep); eq[1] = pack_hi(ep); eq[2] = pack_lo(en); eq[3] = pack_hi(en);
            asm volatile("" : "+v"(eq[0]), "+v"(eq[1]), "+v"(eq[2]), "+v"(eq[3]));
        }
        wait_vm<0>();
        publish(c, ws + WS_FP + (size_t)((c.h * 4 + c.wid) * 16 + c.rb) * 8, nonce);
        STAMP(c, 5);
    }
    await(c, ws + WS_FP + (size_t)(c.h * 4 + c.wid) * 128, 16, nonce, SPIN_CAP);
    STAMP(c, 6);
    {
        const rsrc_t rp = make_rsrc(ws + WS_KVP + (size_t)c.h * 16 * 8192, 16 * 8192);
        const unsigned off = 2 * c.wid * 1024 + voff, st = c.lds + 65536;
        u32x4 pa[16], pb[16];
#pragma unroll
        for (int i = 0; i < 16; ++i) { pa[i] = ld128_dev(rp, off, i * 8192); pb[i] = ld128_dev(rp, off + 1024, i * 8192); }
        f32x4 sa = __builtin_bit_cast(f32x4, pa[0]), sb = __builtin_bit_cast(f32x4, pb[0]);
#pragma unroll
        for (int i = 1; i < 16; ++i) { sa += __builtin_bit_cast(f32x4, pa[i]); sb += __builtin_bit_cast(f32x4, pb[i]); }
        u32x4 f = {pk(sa[0], sa[1]), pk(sa[2], sa[3]), pk(sb[0], sb[1]), pk(sb[2], sb[3])};
        if (c.r32 == 29) {
            const unsigned tail = ((c.wid & 1) && c.hi) ? 0u : 0x3F803F80u;
            f = u32x4{0x3F803F80u, 0x3F803F80u, tail, tail};
        }
        sts128(st + c.wid * 1024 + voff, f);
        lds_bar();
        STAMP(c, 7);
        f32x16 np = mfma(lds128(st + voff), eq[0], zero); np = mfma(lds128(st + 1024 + voff), eq[1], np);
        f32x16 nn = mfma(lds128(st + 2048 + voff), eq[2], zero); nn = mfma(lds128(st + 3072 + voff), eq[3], nn);
        const float rp_ = __builtin_amdgcn_rcpf(np[13]), rn_ = __builtin_amdgcn_rcpf(nn[13]);
        const float inv = __builtin_amdgcn_rcpf(fmaf(np[12], rp_, fmaf(nn[12], rn_, 1e-6f)));
        const float ap = upper_val(rp_ * inv), an = upper_val(rn_ * inv);
        f32x16 o;
#pragma unroll
        for (int i = 0; i < 16; ++i) o[i] = fmaf(np[i], ap, nn[i] * an);
        u32x4 f0 = pack_lo(o), f1 = pack_hi(o);
        if (c.hi) { f1[2] = 0; f1[3] = 0; }
        const rsrc_t ro = make_rsrc(ws + WS_OB + (size_t)((c.rb * 4 + c.wid) * 16 + c.h) * 2048, 2048);
        st128_dev(f0, ro, voff, 0); st128_dev(f1, ro, voff + 1024, 0);
    }
}

__global__ __launch_bounds__(256, 2) void fused(const u16* xa, const u16* wa, const u16* woa, const u16* xb, const u16* wb, const u16* wphi, const u16* wob, u16* out, char* ws) {
    Ctx c;
    c.lane = threadIdx.x & 63; c.wid = __builtin_amdgcn_readfirstlane(threadIdx.x >> 6);
    c.hi = c.lane >> 5; c.r32 = c.lane & 31;
    const int b = blockIdx.x & 255, xcd = b & 7, i = b >> 3;
    c.h = (xcd & 1) * 8 + (i & 7); c.rb = (xcd >> 1) * 4 + (i >> 3);
    c.rot = ((i >> 3) + (i & 7)) % 3;
    STAMP(c, 0);
    const bool is_a = blockIdx.x < 256;
    u64 nonce, nonce_a = 0;
    if (is_a) branch_a(fresh(c), xa, wa, ws, nonce);
    else branch_b(fresh(c), xb, wb, wphi, woa, wob, ws, nonce, nonce_a);
    c = fresh(c);
    wait_vm<0>();
    publish(c, ws + (is_a ? WS_FOA : WS_FOB) + (size_t)((c.rb * 4 + c.wid) * 16 + c.h) * 8, nonce);
    STAMP(c, 10);
    if (!is_a) {
#pragma unroll 1
        for (int br = 1; br >= 0; --br) { phase_out(fresh(c), br, ws, br ? nonce : nonce_a, out); lds_bar(); }
    }
}

}

extern "C" void kernel_launch(void* const* d_in, const int* in_sizes, int n_in, void* d_out, int out_size, void* d_ws, size_t ws_size, hipStream_t stream) {
    (void)hipFuncSetAttribute((const void*)fused, hipFuncAttributeMaxDynamicSharedMemorySize, LDS_BYTES);
    fused<<<512, 256, LDS_BYTES, stream>>>((const u16*)d_in[0], (const u16*)d_in[1], (const u16*)d_in[2], (const u16*)d_in[3], (const u16*)d_in[4], (const u16*)d_in[5],
                                           (const u16*)d_in[6], (u16*)d_out, (char*)d_ws);
}
```

```cpp
#include <hip/hip_runtime.h>
#include <stdint.h>

namespace {

typedef unsigned short u16;
typedef unsigned long long u64;
typedef __attribute__((ext_vector_type(16))) float f32x16;
typedef __attribute__((ext_vector_type(4))) float f32x4;
typedef __attribute__((ext_vector_type(2))) float f32x2;
typedef __attribute__((ext_vector_type(8))) __bf16 bf16x8;
typedef __attribute__((ext_vector_type(2))) __bf16 bf16x2;
typedef __attribute__((ext_vector_type(4))) unsigned u32x4;
typedef __attribute__((ext_vector_type(2))) unsigned u32x2;
typedef __attribute__((ext_vector_type(4))) short s16x4;
typedef __amdgpu_buffer_rsrc_t rsrc_t;
#define LAS __attribute__((address_space(3)))
#define GAS __attribute__((address_space(1)))
#define DI __device__ __forceinline__
#define SBAR() __builtin_amdgcn_sched_barrier(0)

constexpr int SEQ = 2048, DM = 448;
constexpr unsigned XROW = DM * 2;
constexpr unsigned WROW = 3 * DM * 2;
constexpr unsigned X_BYTES = SEQ * XROW, W_BYTES = DM * WROW, WO_BYTES = DM * XROW;
constexpr unsigned WS_EPOCH = 0;
constexpr unsigned WS_FK = 4096;
constexpr unsigned WS_FP = WS_FK + 8192;
constexpr unsigned WS_FOA = WS_FP + 8192;
constexpr unsigned WS_FOB = WS_FOA + 8192;
constexpr unsigned WS_FW = WS_FOB + 8192;
constexpr unsigned WS_FWO = WS_FW + 2048;
constexpr unsigned WS_VOID = WS_FWO + 2048;
constexpr size_t WS_KVA = 65536;
constexpr size_t WS_OA = WS_KVA + (4u << 20);
constexpr size_t WS_OB = WS_OA + (2u << 20);
constexpr size_t WS_KVP = WS_OB + (2u << 20);
constexpr size_t WS_WB = WS_KVP + (2u << 20);
constexpr size_t WS_WO = WS_WB + (2u << 20);
constexpr int LDS_BYTES = 160 * 1024;
constexpr unsigned L_WKV = 0, L_WQ = 57344, L_RET = 57344, L_WB = 0;
constexpr unsigned L_X = 86016;
constexpr unsigned L_QX = 151552, L_STATE = 151552, L_WPHI = 159744, L_FLAG = 161792;
constexpr unsigned L_PHI = 0, L_RED = 24576;
constexpr unsigned L_COMB = 0, L_STAT = 49152, L_OIMG = 65536, L_OUT = 98304;
constexpr float LOG2E = 1.4426950408889634f;
constexpr float QSCALE = 0.18898223650461362f * LOG2E;
#define REF_OFF 32.f
constexpr float REF_OFFSET = REF_OFF;
constexpr float L_LIMIT = 18446744073709551616.f;
constexpr int SC1 = 16;
constexpr unsigned OOB = 0x80000000u;
constexpr unsigned SPIN_CAP = 1u << 18;
#define PSLEEP 4
#define CODE_LINES 384
#define OSPLIT 12
#define QSPLIT 12

DI f32x16 mfma(u32x4 a, u32x4 b, f32x16 c) {
    return __builtin_amdgcn_mfma_f32_32x32x16_bf16(__builtin_bit_cast(bf16x8, a), __builtin_bit_cast(bf16x8, b), c, 0, 0, 0);
}
DI unsigned pk(float lo, float hi) { f32x2 v = {lo, hi}; return __builtin_bit_cast(unsigned, __builtin_convertvector(v, bf16x2)); }
DI u32x4 pack_lo(const f32x16& x) { return u32x4{pk(x[0], x[1]), pk(x[2], x[3]), pk(x[4], x[5]), pk(x[6], x[7])}; }
DI u32x4 pack_hi(const f32x16& x) { return u32x4{pk(x[8], x[9]), pk(x[10], x[11]), pk(x[12], x[13]), pk(x[14], x[15])}; }
DI u32x4 quad(const f32x16& x, int g) { return u32x4{__float_as_uint(x[4 * g]), __float_as_uint(x[4 * g + 1]), __float_as_uint(x[4 * g + 2]), __float_as_uint(x[4 * g + 3])}; }
DI rsrc_t make_rsrc(const void* p, unsigned bytes) { return __builtin_amdgcn_make_buffer_rsrc((void*)p, 0, bytes, 0x00020000); }
DI u32x4 ld128(rsrc_t rs, unsigned voff, unsigned soff) { return __builtin_amdgcn_raw_buffer_load_b128(rs, voff, soff, 0); }
DI u32x4 ld128_dev(rsrc_t rs, unsigned voff, unsigned soff) { return __builtin_amdgcn_raw_buffer_load_b128(rs, voff, soff, SC1); }
DI void st128_dev(u32x4 v, rsrc_t rs, unsigned voff, unsigned soff) { __builtin_amdgcn_raw_buffer_store_b128(v, rs, voff, soff, SC1); }
DI void dma16(rsrc_t rs, unsigned lds_addr, unsigned voff, unsigned soff) {
    asm volatile("s_mov_b32 m0, %0\n\ts_nop 0\n\tbuffer_load_dwordx4 %1, %2, %3 offen lds" ::"s"(lds_addr), "v"(voff), "s"(rs), "s"(soff) : "memory");
}
DI void ld128_async(u32x4& d, rsrc_t rs, unsigned voff, unsigned soff) {
    asm volatile("buffer_load_dwordx4 %0, %1, %2, %3 offen" : "=v"(d) : "v"(voff), "s"(rs), "s"(soff) : "memory");
}
DI void dma16_dev(rsrc_t rs, unsigned lds_addr, unsigned voff, unsigned soff) {
    asm volatile("s_mov_b32 m0, %0\n\ts_nop 0\n\tbuffer_load_dwordx4 %1, %2, %3 offen sc1 lds" ::"s"(lds_addr), "v"(voff), "s"(rs), "s"(soff) : "memory");
}
template <int N> DI void wait_vm() { asm volatile("s_waitcnt vmcnt(%0)" ::"n"(N) : "memory"); }
template <int N> DI void wait_bar() { asm volatile("s_waitcnt vmcnt(%0) lgkmcnt(0)\n\ts_barrier" ::"n"(N) : "memory"); }
DI void lds_bar() { asm volatile("s_waitcnt lgkmcnt(0)\n\ts_barrier" ::: "memory"); }
DI u32x4 lds128(unsigned a) { return *(const LAS u32x4*)(size_t)a; }
DI void sts128(unsigned a, u32x4 v) { *(LAS u32x4*)(size_t)a = v; }
DI void sts64(unsigned a, u32x2 v) { *(LAS u32x2*)(size_t)a = v; }
DI u32x2 tr64(unsigned a) { return __builtin_bit_cast(u32x2, __builtin_amdgcn_ds_read_tr16_b64_v4i16((LAS s16x4*)(size_t)a)); }
DI u32x4 tr_frag(unsigned a0, unsigned a1) { u32x2 lo = tr64(a0), hi = tr64(a1); return u32x4{lo[0], lo[1], hi[0], hi[1]}; }
DI float both_max(float v) { auto r = __builtin_amdgcn_permlane32_swap(__float_as_uint(v), __float_as_uint(v), false, false); return fmaxf(__uint_as_float(r[0]), __uint_as_float(r[1])); }
DI float both_min(float v) { auto r = __builtin_amdgcn_permlane32_swap(__float_as_uint(v), __float_as_uint(v), false, false); return fminf(__uint_as_float(r[0]), __uint_as_float(r[1])); }
DI float both_sum(float v) { auto r = __builtin_amdgcn_permlane32_swap(__float_as_uint(v), __float_as_uint(v), false, false); return __uint_as_float(r[0]) + __uint_as_float(r[1]); }
DI float upper_val(float v) { auto r = __builtin_amdgcn_permlane32_swap(__float_as_uint(v), __float_as_uint(v), false, false); return __uint_as_float(r[1]); }
DI float max16(const f32x16& s) {
    float a = fmaxf(fmaxf(s[0], s[1]), s[2]), b = fmaxf(fmaxf(s[3], s[4]), s[5]);
    a = fmaxf(fmaxf(a, s[6]), s[7]); b = fmaxf(fmaxf(b, s[8]), s[9]);
    a = fmaxf(fmaxf(a, s[10]), s[11]); b = fmaxf(fmaxf(b, s[12]), s[13]);
    a = fmaxf(fmaxf(a, s[14]), s[15]);
    return fmaxf(a, b);
}
DI float min16(const f32x16& s) {
    float a = fminf(fminf(s[0], s[1]), s[2]), b = fminf(fminf(s[3], s[4]), s[5]);
    a = fminf(fminf(a, s[6]), s[7]); b = fminf(fminf(b, s[8]), s[9]);
    a = fminf(fminf(a, s[10]), s[11]); b = fminf(fminf(b, s[12]), s[13]);
    a = fminf(fminf(a, s[14]), s[15]);
    return fminf(a, b);
}

DI void ld128_async_dev(u32x4& d, rsrc_t rs, unsigned voff, unsigned soff) {
    asm volatile("buffer_load_dwordx4 %0, %1, %2, %3 offen sc1" : "=v"(d) : "v"(voff), "s"(rs), "s"(soff) : "memory");
}
DI void ld128_async_acc(u32x4& d, rsrc_t rs, unsigned voff, unsigned soff) {
    asm volatile("buffer_load_dwordx4 %0, %1, %2, %3 offen sc1" : "=a"(d) : "v"(voff), "s"(rs), "s"(soff) : "memory");
}
DI void st128_async(u32x4 v, rsrc_t rs, unsigned voff, unsigned soff) {
    asm volatile("buffer_store_dwordx4 %0, %1, %2, %3 offen sc1\n\ts_nop 1" ::"v"(v), "v"(voff), "s"(rs), "s"(soff) : "memory");
}
template <int N> DI void landed(u32x4& a, u32x4& b, u32x4& c, u32x4& d) { asm volatile("s_waitcnt vmcnt(%4)" : "+v"(a), "+v"(b), "+v"(c), "+v"(d) : "n"(N) : "memory"); }
template <int N> DI void landed8(u32x4 (&r)[8]) {
    asm volatile("s_waitcnt vmcnt(%8)" : "+v"(r[0]), "+v"(r[1]), "+v"(r[2]), "+v"(r[3]), "+v"(r[4]), "+v"(r[5]), "+v"(r[6]), "+v"(r[7]) : "n"(N) : "memory");
}
DI void mfma_v(f32x16& acc, u32x4 a, u32x4 b) { asm("v_mfma_f32_32x32x16_bf16 %0, %1, %2, %0" : "+v"(acc) : "v"(a), "v"(b)); }
DI void mfma_v0(f32x16& acc, u32x4 a, u32x4 b) { asm("s_nop 1\n\tv_mfma_f32_32x32x16_bf16 %0, %1, %2, 0" : "=&v"(acc) : "v"(a), "v"(b)); }
DI void settle(f32x16& x) { asm volatile("s_nop 11" : "+v"(x)); }
DI void barrier() { asm volatile("s_barrier" ::: "memory"); }
DI int lane_id() { int l; asm volatile("v_mbcnt_lo_u32_b32 %0, -1, 0\n\tv_mbcnt_hi_u32_b32 %0, -1, %0" : "=v"(l)); return l; }

#define STAMP(id) do {} while (0)
#define TSTAMP(id) do {} while (0)

DI void epoch_request(u64& v, const char* p) { asm volatile("s_load_dwordx2 %0, %1, 0x0" : "=s"(v) : "s"(p) : "memory"); }
DI void publish(char* ws, unsigned word, u64 nonce) {
    asm volatile("s_mov_b64 exec, 1\n\tglobal_store_dwordx2 %0, %1, %2 sc1\n\ts_mov_b64 exec, -1" ::"v"(word), "v"(nonce), "s"(ws) : "memory");
}
DI void peek(u32x2& v, const char* ws, unsigned word) { asm volatile("global_load_dwordx2 %0, %1, %2 sc1" : "=v"(v) : "v"(word), "s"(ws) : "memory"); }
DI void peek(u32x4& v, const char* ws, unsigned word) { asm volatile("global_load_dwordx4 %0, %1, %2 sc1" : "=v"(v) : "v"(word), "s"(ws) : "memory"); }
DI bool match(const u32x2& v, u64 n) { return v[0] == (unsigned)n && v[1] == (unsigned)(n >> 32); }
DI bool match(const u32x4& v, u64 n) { return v[0] == (unsigned)n && v[1] == (unsigned)(n >> 32) && v[2] == (unsigned)n && v[3] == (unsigned)(n >> 32); }
template <int N, class V> DI void seen(V& v, const char* ws, unsigned word, u64 nonce) {
    asm volatile("s_waitcnt vmcnt(%1)" : "+v"(v) : "n"(N) : "memory");
#pragma unroll 1
    for (unsigned spins = 0; spins < SPIN_CAP && !__all(match(v, nonce)); ++spins) {
        __builtin_amdgcn_s_sleep(PSLEEP);
        peek(v, ws, word);
        asm volatile("s_waitcnt vmcnt(0)" : "+v"(v) :: "memory");
    }
}

DI void mfma_pair(int isv, u32x4 xf, u32x4 w0, u32x4 w1, f32x16& acc0, f32x16& acc1) {
    asm("s_cmp_lg_u32 %5, 0\n\ts_cbranch_scc1 1f\n\t"
        "v_mfma_f32_32x32x16_bf16 %0, %3, %2, %0\n\tv_mfma_f32_32x32x16_bf16 %1, %4, %2, %1\n\ts_branch 2f\n"
        "1:\n\tv_mfma_f32_32x32x16_bf16 %0, %2, %3, %0\n\tv_mfma_f32_32x32x16_bf16 %1, %2, %4, %1\n2:"
        : "+v"(acc0), "+v"(acc1) : "v"(xf), "v"(w0), "v"(w1), "s"(isv) : "scc");
}
DI void pin(u32x4& a, u32x4& b, u32x4& c, u32x4& d) { asm volatile("" : "+v"(a), "+v"(b), "+v"(c), "+v"(d) :: "memory"); }

DI void set_reference(u32x4& q1, float nm, int hi) {
    const unsigned a = __float_as_uint(nm) & 0xFFFF0000u;
    const float r1 = nm - __uint_as_float(a);
    const unsigned b = __float_as_uint(r1) & 0xFFFF0000u;
    const float r2 = r1 - __uint_as_float(b);
    if (hi) { q1[2] = (a >> 16) | b; q1[3] = __float_as_uint(r2) >> 16; }
}

struct KV { u32x4 k0, k1, v0, v1; };

DI void feature_exps(const f32x16& p, f32x16& ep, f32x16& en) {
    const float mx = both_max(max16(p)) * LOG2E, mn = both_min(min16(p)) * LOG2E;
#pragma unroll
    for (int i = 0; i < 16; ++i) { ep[i] = __builtin_amdgcn_exp2f(fmaf(p[i], LOG2E, -mx)); en[i] = __builtin_amdgcn_exp2f(fmaf(p[i], -LOG2E, mn)); }
}

DI void take_reference(u32x4 (&q)[4][2], float (&ref)[4], float offset, int hi) {
#pragma unroll
    for (int t = 0; t < 4; ++t) { ref[t] = both_max(ref[t]) + offset; set_reference(q[t][1], -ref[t], hi); }
}

DI bool combine(int wid, const f32x16& o0, const f32x16& o1, const f32x16& o2, const f32x16& o3, const float (&ref)[4], u32x4& f0, u32x4& f1) {
    const int lane = lane_id(), hi = lane >> 5, r32 = lane & 31;
    const unsigned voff = lane * 16;
    bool bad = false;
    float e0 = 0.f;
    const f32x16* ot[4] = {&o0, &o1, &o2, &o3};
#pragma unroll
    for (int t = 0; t < 4; ++t) {
        const float l = (*ot[t])[12], e = ref[t] + __builtin_amdgcn_logf(l);
        bad |= hi && !(l < L_LIMIT);
        if (t == 0) e0 = upper_val(e);
        else {
            const unsigned slot = ((wid + t) & 3) * 3 + (t - 1);
            if (hi) sts64(L_STAT + slot * 256 + r32 * 8, u32x2{__float_as_uint(ref[t]), __float_as_uint(e)});
#pragma unroll
            for (int g = 0; g < 4; ++g) sts128(L_COMB + slot * 4096 + g * 1024 + voff, quad(*ot[t], g));
        }
    }
    f32x16 o = o0;
    lds_bar();
    float m[3], e[3];
#pragma unroll
    for (int v = 0; v < 3; ++v) { const u32x2 s = *(const LAS u32x2*)(size_t)(L_STAT + (wid * 3 + v) * 256 + r32 * 8); m[v] = __uint_as_float(s[0]); e[v] = __uint_as_float(s[1]); }
    const float top = fmaxf(fmaxf(e0, e[0]), fmaxf(e[1], e[2]));
    o *= __builtin_amdgcn_exp2f(ref[0] - top);
#pragma unroll
    for (int v = 0; v < 3; ++v) {
        const float cv = __builtin_amdgcn_exp2f(m[v] - top);
#pragma unroll
        for (int g = 0; g < 4; ++g) {
            const u32x4 p = lds128(L_COMB + (wid * 3 + v) * 4096 + g * 1024 + voff);
#pragma unroll
            for (int i = 0; i < 4; ++i) o[4 * g + i] = fmaf(__uint_as_float(p[i]), cv, o[4 * g + i]);
        }
    }
    o *= __builtin_amdgcn_rcpf(upper_val(o[12]));
    float chk = 0.f;
#pragma unroll
    for (int i = 0; i < 16; ++i) chk = fmaf(o[i], 0.f, chk);
    bad |= !(chk == 0.f);
    f0 = pack_lo(o); f1 = pack_hi(o);
    if (hi) { f1[2] = 0; f1[3] = 0; }
    return bad;
}

DI void stage_out(unsigned img, int wid, int lane, const f32x16& r) {
    const u32x4 lo = pack_lo(r), up = pack_hi(r);
    const unsigned wr = img + (lane & 31) * 272 + wid * 64 + (lane >> 5) * 8;
    sts64(wr, u32x2{lo[0], lo[1]}); sts64(wr + 16, u32x2{lo[2], lo[3]}); sts64(wr + 32, u32x2{up[0], up[1]}); sts64(wr + 48, u32x2{up[2], up[3]});
}
DI void flush_out(unsigned img, int wid, int lane, int h, int qt, int br, u16* out) {
    const unsigned width = h >= 12 ? 128 : 256, colb = (lane & 15) * 16;
    const rsrc_t rd = make_rsrc((char*)out + (size_t)qt * 32 * (2 * XROW) + br * XROW + (h >> 2) * 256, 31 * 2 * XROW + width);
#pragma unroll
    for (int i = 0; i < 2; ++i) {
        const unsigned row = wid * 8 + i * 4 + (lane >> 4);
        st128_async(lds128(img + row * 272 + colb), rd, colb < width ? row * (2 * XROW) + colb : OOB, 0);
    }
}

__global__ __launch_bounds__(256, 1) void fused(const u16* xa, const u16* wa, const u16* woa, const u16* xb, const u16* wb, const u16* wphi, const u16* wob, u16* out, char* ws) {
    STAMP(0);
    const int wid = __builtin_amdgcn_readfirstlane(threadIdx.x >> 6);
    const int blk = blockIdx.x, xcd = blk & 7, bi = blk >> 3;
    const int h = (xcd & 1) * 8 + (bi & 7), rb = (xcd >> 1) * 4 + (bi >> 3), tile = rb * 4 + wid;
    const int rot = ((bi >> 3) + (bi & 7)) % 3;
    auto chunk_at = [&](int p) { const int v = p % 3 + rot; return p == 6 ? 6 : p - p % 3 + (v >= 3 ? v - 3 : v); };
    unsigned code_probe;
    {
        u64 pc; asm volatile("s_getpc_b64 %0" : "=s"(pc));
        const unsigned line = bi * 16 + wid * 4 + (threadIdx.x & 3);
        asm volatile("global_load_dword %0, %1, %2" : "=v"(code_probe) : "v"((line < CODE_LINES ? line : 0) * 128), "s"(pc) : "memory");
    }
    u64 epoch; epoch_request(epoch, ws + WS_EPOCH);
    u64 nonce;
    const f32x16 zero = {};
    u32x4 q[4][2], eq[4];
    float ref[4];
    KV a, b, c, d;
    const rsrc_t rk = make_rsrc(ws + WS_KVA + (size_t)(h * 64 + wid * 16) * 4096, 16 * 4096);
#define LDKV(X, j) do { ld128_async_dev(X.v1, rk, voff + 3072, (j) * 4096); ld128_async_dev(X.v0, rk, voff + 2048, (j) * 4096); \
                        ld128_async_dev(X.k1, rk, voff + 1024, (j) * 4096); ld128_async_dev(X.k0, rk, voff, (j) * 4096); } while (0)
    {
        const int lane = lane_id(), hi = lane >> 5, r32 = lane & 31, q4 = (lane & 15) >> 2;
        const unsigned voff = lane * 16, col = 32 * ((lane >> 4) & 1) + 8 * (lane & 3);
        const rsrc_t rxa = make_rsrc(xa, X_BYTES), rwa = make_rsrc(wa, W_BYTES), rxb = make_rsrc(xb, X_BYTES);
        const int ug = blk & 31, ucg = blk >> 5, ubr = (blk >> 5) & 1, uh = (blk >> 1) & 15, uhalf = blk & 1;
        const bool unit_o = blk >= 192, unit_w = !unit_o && ug < 28;
        if (unit_w) {
            const rsrc_t rw = make_rsrc(wb, W_BYTES);
#pragma unroll
            for (int j = 0; j < 2; ++j) {
                const int i = 4 * wid + 2 * j + hi;
                dma16(rw, L_RET + (2 * wid + j) * 1024, (64 * (ug >> 2) + 8 * (ug & 3) + (i & 7) + 32 * (i >> 3)) * WROW + ucg * 448 + (r32 < 29 ? r32 : 28) * 16, 0);
            }
        } else if (unit_o) {
            const rsrc_t rw = make_rsrc(ubr ? wob : woa, WO_BYTES);
            for (int j = wid; j < 14; j += 4) dma16(rw, L_RET + j * 1024, (28 * uh + 2 * j + hi) * XROW + uhalf * 448 + (r32 < 28 ? r32 : 27) * 16, 0);
        }
        const int isv = h & 1, row8 = lane >> 3;
        u32x4 xf[28];
        f32x16 acc0 = {}, acc1 = {};
        u32x4 pw;
        const unsigned pw_word = WS_FW + (h >> 3) * 672 + (lane < 42 ? lane : 41) * 16;
        {
            const unsigned piece = (lane & 7) ^ (((row8 >> 1) & 1) << 2);
            const unsigned vw = (16 * wid + row8) * WROW + (1 + isv) * XROW + (h >> 1) * 112 + ((piece < 7 ? piece : 6) << 4);
            const unsigned vx0 = (tile * 32 + row8) * XROW + (((lane & 7) ^ (lane >> 4)) << 4), vx1 = (tile * 32 + 8 + row8) * XROW + (((lane & 7) ^ (4 + (lane >> 4))) << 4);
            auto req_x = [&](rsrc_t rx, int slot, unsigned k, int i) { dma16(rx, L_X + slot * 16384 + wid * 4096 + i * 1024, (i & 1) ? vx1 : vx0, k * 128 + (i >> 1) * 16 * XROW); };
            auto req_w = [&](int ch, int i) { dma16(rwa, L_WKV + ch * 8192 + wid * 2048 + i * 1024, vw, (chunk_at(ch) * 64 + 8 * i) * WROW); };
            auto issue = [&](int ch) {
                req_w(ch, 0); req_w(ch, 1);
#pragma unroll
                for (int i = 0; i < 4; ++i) req_x(rxa, ch & 3, chunk_at(ch), i);
            };
            issue(0); issue(1);
            unsigned rt_at[4], rt_2nd[4], rt_dst[4], rt_v[4];
            {
                const unsigned rd = L_RET + q4 * 512 + col + hi * (unit_w ? 4096 : 2048);
#pragma unroll
                for (int i = 0; i < 4; ++i) {
                    const int f = wid + 4 * i, s = f & 1, t = f >> 1, hl = 2 * wid + i;
                    const bool on = unit_w ? i < 2 : (unit_o && f < 14);
                    rt_at[i] = rd + (!on ? 0 : unit_w ? hl * 56 : s * 8192 + t * 64);
                    rt_2nd[i] = rt_at[i] + (unit_w || (s && hi) ? 2048 : 4096);
                    rt_dst[i] = !on ? 0 : unit_w ? ((((ucg & 1) * 8 + hl) * 28 + ug) * 3 + (ucg >> 1)) * 1024 : (2u << 20) + (((ubr * 14 + uhalf * 7 + t) * 16 + uh) * 2 + s) * 1024;
                    rt_v[i] = on ? voff : OOB;
                    asm volatile("" : "+v"(rt_at[i]), "+v"(rt_2nd[i]), "+s"(rt_dst[i]), "+v"(rt_v[i]));
                }
            }
            unsigned rt_word = wid == 0 && unit_w ? WS_FW + (((ucg & 1) * 3 + (ucg >> 1)) * 28 + ug) * 8 : wid == 0 && unit_o ? WS_FWO + ((ubr * 2 + uhalf) * 16 + uh) * 8 : WS_VOID + (blk * 4 + wid) * 8;
            unsigned w0 = L_WKV + (32 * hi + q4) * 128 + (col ^ ((q4 >> 1) << 6)), w1 = L_WKV + (32 * hi + q4) * 128 + ((col + 56) ^ ((q4 >> 1) << 6));
            unsigned xo[4];
#pragma unroll
            for (int s = 0; s < 4; ++s) xo[s] = L_X + wid * 4096 + r32 * 128 + (((4 * hi + s) ^ ((r32 >> 1) & 7)) << 4);
            unsigned vwq = (16 * wid + (lane >> 2)) * WROW + h * 56 + (lane & 3) * 16;
            asm volatile("" : "+s"(rt_word), "+v"(w0), "+v"(w1), "+v"(xo[0]), "+v"(xo[1]), "+v"(xo[2]), "+v"(xo[3]), "+v"(vwq), "+v"(acc0), "+v"(acc1));
            issue(2); issue(3);
            asm volatile("s_waitcnt lgkmcnt(0)" : "+s"(epoch) :: "memory");
            nonce = epoch + 1;
            STAMP(1);
            const rsrc_t rwb = make_rsrc(ws + WS_WB + (size_t)h * 86016, 86016);
            auto req_wb = [&](int ch, int j) { dma16_dev(rwb, L_WB + ch * 12288 + wid * 3072 + j * 1024, voff, ch * 12288 + wid * 3072 + j * 1024); };
            auto req_wq = [&](int j) { dma16(rwa, L_WQ + j * 4096 + wid * 1024, vwq, chunk_at(j) * 64 * WROW); };
            auto xb_slot = [](int ch) { return (ch + 3) & 3; };
            u32x4 wf[2][8];
#pragma unroll
            for (int p = 0; p <= 7; ++p) {
                if (p < 7) {
                    if (p == 0 || p == 3) wait_vm<18>(); else if (p <= 2) wait_vm<22>(); else if (p == 4) wait_vm<13>(); else if (p == 5) wait_vm<10>(); else wait_vm<8>();
                    barrier();
                    if (p == 0) asm volatile("" :: "v"(code_probe));
                    if (p == 0) { STAMP(2); }
                    if (p == 6) { STAMP(3); }
#pragma unroll
                    for (int s = 0; s < 4; ++s) xf[4 * p + s] = lds128(xo[s] + (p & 3) * 16384);
                }
#pragma unroll
                for (int s = 0; s < 4; ++s) {
                    if (p < 7) {
                        const unsigned o = p * 8192 + s * 1024;
                        wf[p & 1][2 * s] = tr_frag(w0 + o, w0 + o + 512); wf[p & 1][2 * s + 1] = tr_frag(w1 + o, w1 + o + 512);
                    }
                    SBAR();
                    if (p > 0) mfma_pair(isv, xf[4 * p - 4 + s], wf[(p - 1) & 1][2 * s], wf[(p - 1) & 1][2 * s + 1], acc0, acc1);
                    SBAR();
                    if (p == 0 && s == 0) {
                        const rsrc_t ro = make_rsrc(ws + WS_WB, 4u << 20);
#pragma unroll
                        for (int i = 0; i < 4; ++i) st128_async(tr_frag(rt_at[i], rt_2nd[i]), ro, rt_v[i], rt_dst[i]);
                    }
                    if (p < 7 && s == 1) pin(xf[4 * p], xf[4 * p + 1], xf[4 * p + 2], xf[4 * p + 3]);
                    if (p <= 2) {
                        if (s == 0) { req_w(p + 4, 0); req_w(p + 4, 1); }
                        if (s == 1 || s == 2) { req_x(rxa, p & 3, chunk_at(p + 4), 2 * s - 2); req_x(rxa, p & 3, chunk_at(p + 4), 2 * s - 1); }
                    }
                    if (p == 3 && s == 0) publish(ws, rt_word, nonce);
                    if (p == 4 && s < 3) req_wq(s);
                    if (p == 5) req_wq(3 + s);
                    if (p == 6 && s == 0) {
                        const rsrc_t rp = make_rsrc(wphi + h * 784, 1568);
                        dma16(rp, L_WPHI, voff, 0); dma16(rp, L_WPHI + 1024, voff, 1024);
                        peek(pw, ws, pw_word);
                    }
                    if (p == 6 && s >= 2) { req_x(rxb, xb_slot(0), 0, 2 * s - 4); req_x(rxb, xb_slot(0), 0, 2 * s - 3); }
                    if (p == 7) req_x(rxb, xb_slot(1), 1, s);
                    SBAR();
                }
            }
            asm volatile("s_nop 7" : "+v"(acc0), "+v"(acc1));
            {
                u32x4 f[4] = {pack_lo(acc0), pack_hi(acc0), pack_lo(acc1), pack_hi(acc1)};
                const bool all = isv && r32 >= 28, tail = isv ? all : (bool)hi;
                const unsigned va = r32 == 28 ? 0x3F803F80u : 0u, v2 = isv ? va : 0x3F803F80u, v3 = isv ? va : 0x00003F80u;
#pragma unroll
                for (int i = 0; i < 4; ++i) {
                    f[i][0] = all ? va : f[i][0]; f[i][1] = all ? va : f[i][1];
                    if (i & 1) { f[i][2] = tail ? v2 : f[i][2]; f[i][3] = tail ? v3 : f[i][3]; }
                    else       { f[i][2] = all ? va : f[i][2]; f[i][3] = all ? va : f[i][3]; }
                }
                const rsrc_t ro = make_rsrc(ws + WS_KVA + (size_t)((h & 14) * 64 + tile) * 4096 + isv * 2048, 64 * 4096 + 2048);
                st128_async(f[0], ro, voff, 0); st128_async(f[1], ro, voff + 1024, 0); st128_async(f[2], ro, voff, 64 * 4096); st128_async(f[3], ro, voff + 1024, 64 * 4096);
            }
            STAMP(4);
            if (wid == 0 && lane == 0) *(LAS unsigned*)(size_t)L_FLAG = 0;
            wait_bar<15>();
            {
                const unsigned wo = L_WQ + (32 * hi + q4) * 64 + col;
                f32x16 acc;
                u32x4 wq[28];
#pragma unroll
                for (int i = 0; i < 4; ++i) wq[i] = tr_frag(wo + i * 512, wo + i * 512 + 256);
#pragma unroll
                for (int i = 0; i < 28; ++i) {
                    if (i + 4 < 28) { const unsigned o = ((i + 4) >> 2) * 4096 + (i & 3) * 512; wq[i + 4] = tr_frag(wo + o, wo + o + 256); }
                    SBAR();
                    if (i == 0) mfma_v0(acc, wq[i], xf[i]); else mfma_v(acc, wq[i], xf[i]);
                    SBAR();
                    if (i < 8) req_x(rxb, xb_slot(2 + (i >> 2)), 2 + (i >> 2), i & 3);
                    if (i == QSPLIT) {
                        wait_vm<8>();
                        publish(ws, WS_FK + (((h >> 1) * 64 + tile) * 2 + isv) * 8, nonce);
                        STAMP(5);
                        seen<9>(pw, ws, pw_word, nonce);
                    }
                    if (i > QSPLIT && i <= QSPLIT + 12) req_wb((i - QSPLIT - 1) / 3, (i - QSPLIT - 1) % 3);
                    SBAR();
                }
                static_assert(QSPLIT >= 8 && QSPLIT + 12 < 28, "request slots");
                settle(acc);
                acc *= QSCALE;
                u32x4 q0 = pack_lo(acc), q1 = pack_hi(acc);
                if (hi) { q1[2] = 0; q1[3] = 0; }
                sts128(L_QX + wid * 2048 + voff, q0); sts128(L_QX + wid * 2048 + 1024 + voff, q1);
            }
            STAMP(6);
            u32x2 pk_;
            const unsigned pk_word = WS_FK + ((h >> 1) * 64 + wid * 16) * 16 + r32 * 8;
            f32x16 aq = {}, ak = {}, av = {};
            asm volatile("s_nop 1" : "+v"(aq), "+v"(ak), "+v"(av));
            {
                u32x4 wg[2][12], xg[2][4];
#pragma unroll
                for (int p = 0; p <= 7; ++p) {
                    if (p == 0) wait_bar<9>();
                    else if (p < 7) {
                        if (p == 1) wait_vm<13>(); else if (p == 2) wait_vm<17>(); else if (p == 3) wait_vm<21>(); else if (p == 4) wait_vm<14>(); else if (p == 5) wait_vm<8>(); else wait_vm<1>();
                        barrier();
                    }
                    if (p < 7) {
#pragma unroll
                        for (int s = 0; s < 4; ++s) xg[p & 1][s] = lds128(xo[s] + xb_slot(p) * 16384);
                    }
                    if (p == 0) {
#pragma unroll
                        for (int t = 0; t < 4; ++t) { const unsigned at = L_QX + ((wid + t) & 3) * 2048 + voff; q[t][0] = lds128(at); q[t][1] = lds128(at + 1024); }
                    }
                    if (p == 7) { seen<0>(pk_, ws, pk_word, nonce); STAMP(8); }
#pragma unroll
                    for (int s = 0; s < 4; ++s) {
                        if (p < 7) {
                            const unsigned at = L_WB + p * 12288 + s * 3072 + voff;
                            wg[p & 1][3 * s] = lds128(at); wg[p & 1][3 * s + 1] = lds128(at + 1024); wg[p & 1][3 * s + 2] = lds128(at + 2048);
                        }
                        SBAR();
                        if (p > 0) {
                            const u32x4 xs = xg[(p - 1) & 1][s];
                            mfma_v(ak, wg[(p - 1) & 1][3 * s + 1], xs); mfma_v(av, xs, wg[(p - 1) & 1][3 * s + 2]); mfma_v(aq, wg[(p - 1) & 1][3 * s], xs);
                        }
                        SBAR();
                        if (p <= 2) {
                            if (s == 0) { req_wb(p + 4, 0); req_wb(p + 4, 1); }
                            if (s == 1) { req_wb(p + 4, 2); pin(xg[p & 1][0], xg[p & 1][1], xg[p & 1][2], xg[p & 1][3]); }
                            if (s >= 2) { req_x(rxb, xb_slot(p), p + 4, 2 * s - 4); req_x(rxb, xb_slot(p), p + 4, 2 * s - 3); }
                        }
                        if (p == 4 && s == 3) peek(pk_, ws, pk_word);
                        if (p == 7) { if (s == 0) LDKV(a, 0); if (s == 1) LDKV(b, 1); if (s == 2) LDKV(c, 2); }
                        SBAR();
                    }
                }
            }
            u32x4 wp[2];
            {
                const unsigned fc = 16 * ((lane >> 4) & 1) + 4 * (lane & 3);
                const unsigned a0 = L_WPHI + (4 * hi + q4) * 56 + (fc < 28 ? fc : 24) * 2;
                wp[0] = tr_frag(a0, a0 + 8 * 56); wp[1] = tr_frag(a0 + 16 * 56, a0 + (hi ? 20 : 24) * 56);
            }
            asm volatile("s_nop 11" : "+v"(aq), "+v"(ak), "+v"(av));
            STAMP(7);
            f32x16 pkk, pqq;
            {
                u32x4 f0 = pack_lo(ak), f1 = pack_hi(ak), g0 = pack_lo(aq), g1 = pack_hi(aq);
                if (hi) { f1[2] = 0; f1[3] = 0; g1[2] = 0; g1[3] = 0; }
                mfma_v0(pkk, wp[0], f0); mfma_v(pkk, wp[1], f1); mfma_v0(pqq, wp[0], g0); mfma_v(pqq, wp[1], g1);
            }
            u32x4 v0 = pack_lo(av), v1 = pack_hi(av);
            if (r32 >= 28) { const unsigned v = r32 == 28 ? 0x3F803F80u : 0u; v0 = u32x4{v, v, v, v}; v1 = v0; }
            asm volatile("s_nop 7" : "+v"(pkk), "+v"(pqq), "+v"(v0), "+v"(v1));
            const unsigned img = L_PHI + wid * 4352;
            {
                f32x16 ep, en;
                feature_exps(pkk, ep, en);
                const float live = hi ? 0.f : 1.f;
                float sp = (ep[12] + ep[13] + ep[14] + ep[15]) * live, sn = (en[12] + en[13] + en[14] + en[15]) * live;
#pragma unroll
                for (int i = 0; i < 12; ++i) { sp += ep[i]; sn += en[i]; }
                const float rp = __builtin_amdgcn_rcpf(both_sum(sp)), rn = __builtin_amdgcn_rcpf(both_sum(sn));
                const float rp3 = rp * live, rn3 = rn * live;
                const unsigned wr = img + r32 * 136 + hi * 8;
#pragma unroll
                for (int g = 0; g < 4; ++g) {
                    const float u = g == 3 ? rp3 : rp, v = g == 3 ? rn3 : rn;
                    sts64(wr + g * 16, u32x2{pk(ep[4 * g] * u, ep[4 * g + 1] * u), pk(ep[4 * g + 2] * u, ep[4 * g + 3] * u)});
                    sts64(wr + 64 + g * 16, u32x2{pk(en[4 * g] * v, en[4 * g + 1] * v), pk(en[4 * g + 2] * v, en[4 * g + 3] * v)});
                }
            }
            u32x4 pf[4];
            {
                const unsigned rd = img + (4 * hi + q4) * 136 + col;
#pragma unroll
                for (int i = 0; i < 4; ++i) pf[i] = tr_frag(rd + (i >> 1) * 64 + (i & 1) * 16 * 136, rd + (i >> 1) * 64 + (i & 1) * 16 * 136 + 8 * 136);
            }
            SBAR();
            f32x16 eqp, eqn;
            feature_exps(pqq, eqp, eqn);
            eq[0] = pack_lo(eqp); eq[1] = pack_hi(eqp);
            asm volatile("" : "+v"(eq[0]), "+v"(eq[1]), "+v"(eqn));
            SBAR();
            f32x16 kv0, kv1;
            mfma_v0(kv0, pf[0], v0); mfma_v(kv0, pf[1], v1); mfma_v0(kv1, pf[2], v0); mfma_v(kv1, pf[3], v1);
            SBAR();
            eq[2] = pack_lo(eqn); eq[3] = pack_hi(eqn);
            asm volatile("s_nop 7" : "+v"(eq[2]), "+v"(eq[3]), "+v"(kv0), "+v"(kv1));
#pragma unroll
            for (int g = 0; g < 4; ++g) { sts128(L_RED + wid * 8192 + g * 1024 + voff, quad(kv0, g)); sts128(L_RED + wid * 8192 + 4096 + g * 1024 + voff, quad(kv1, g)); }
            landed<8>(a.k0, a.k1, a.v0, a.v1);
            {
                f32x16 s[4];
#pragma unroll
                for (int t = 0; t < 4; ++t) { mfma_v0(s[t], a.k0, q[t][0]); mfma_v(s[t], a.k1, q[t][1]); }
                lds_bar();
                const rsrc_t ro = make_rsrc(ws + WS_KVP + (size_t)(h * 16 + rb) * 8192, 8192);
#pragma unroll
                for (int i = 0; i < 2; ++i) {
                    const unsigned off = (2 * wid + i) * 1024 + voff;
                    f32x4 u = __builtin_bit_cast(f32x4, lds128(L_RED + off));
#pragma unroll
                    for (int v = 1; v < 4; ++v) u += __builtin_bit_cast(f32x4, lds128(L_RED + v * 8192 + off));
                    st128_async(__builtin_bit_cast(u32x4, u), ro, off, 0);
                }
                STAMP(9);
                asm volatile("s_nop 11" : "+v"(s[0]), "+v"(s[1]), "+v"(s[2]), "+v"(s[3]));
#pragma unroll
                for (int t = 0; t < 4; ++t) ref[t] = max16(s[t]);
                take_reference(q, ref, REF_OFFSET, hi);
            }
        }
    }
    const int qt = rb * 4 + (h & 3), nt = (h & 12) + wid;
    const bool live = nt < 14;
    u32x4 wob_f[32], f0, f1;
    u32x2 pob;
    bool bad;
    const rsrc_t roa = make_rsrc(ws + WS_OA + (size_t)(tile * 16 + h) * 2048, 2048);
    {
        const int lane = lane_id(), hi = lane >> 5, r32 = lane & 31;
        const unsigned voff = lane * 16;
        STAMP(10);
        f32x16 o0 = {}, o1 = {}, o2 = {}, o3 = {};
        f32x16 sa, sb; u32x4 pa, pb = {};
        float e12 = 0.f, e13 = 0.f, e14 = 0.f, e15 = 0.f;
        d.v1 = u32x4{};
#define QK0(S, K, Q) asm volatile("v_mfma_f32_32x32x16_bf16 %0, %1, %2, 0" : "=&v"(S) : "v"(K), "v"(Q))
#define QK1(S, K, Q) asm volatile("v_mfma_f32_32x32x16_bf16 %0, %1, %2, %0" : "+v"(S) : "v"(K), "v"(Q))
        asm volatile("s_nop 1" : "+v"(q[0][1]), "+v"(q[1][1]), "+v"(q[2][1]), "+v"(q[3][1]));
        QK0(sa, a.k0, q[0][0]); QK1(sa, a.k1, q[0][1]);
        asm volatile("s_nop 11" : "+v"(sa));
#define EX(S, i) S[i] = __builtin_amdgcn_exp2f(S[i])
#define STEP(SC, SN, KN0, KN1, QN0, QN1, OP, VP1, OC, VC0) do { \
    QK0(SN, KN0, QN0); SBAR(); \
    pb[2] = pk(e12, e13); SBAR(); EX(SC, 0); SBAR(); pb[3] = pk(e14, e15); SBAR(); EX(SC, 1); EX(SC, 2); EX(SC, 3); SBAR(); \
    OP = mfma(VP1, pb, OP); SBAR(); \
    EX(SC, 4); EX(SC, 5); SBAR(); pa[0] = pk(SC[0], SC[1]); SBAR(); EX(SC, 6); SBAR(); pa[1] = pk(SC[2], SC[3]); SBAR(); EX(SC, 7); SBAR(); \
    QK1(SN, KN1, QN1); SBAR(); \
    EX(SC, 8); SBAR(); pa[2] = pk(SC[4], SC[5]); SBAR(); EX(SC, 9); SBAR(); pa[3] = pk(SC[6], SC[7]); SBAR(); EX(SC, 10); EX(SC, 11); SBAR(); \
    OC = mfma(VC0, pa, OC); SBAR(); \
    e12 = __builtin_amdgcn_exp2f(SC[12]); SBAR(); pb[0] = pk(SC[8], SC[9]); SBAR(); e13 = __builtin_amdgcn_exp2f(SC[13]); SBAR(); pb[1] = pk(SC[10], SC[11]); SBAR(); \
    e14 = __builtin_amdgcn_exp2f(SC[14]); e15 = __builtin_amdgcn_exp2f(SC[15]); SBAR(); } while (0)
#define TILE(J, CUR, PREV, NXT, NW, SEAM, MORE) do { \
    STEP(sa, sb, CUR.k0, CUR.k1, q[1][0], q[1][1], o3, PREV.v1, o0, CUR.v0); \
    if (J + 3 <= 15) LDKV(PREV, J + 3); \
    SEAM; SBAR(); \
    STEP(sb, sa, CUR.k0, CUR.k1, q[2][0], q[2][1], o0, CUR.v1, o1, CUR.v0); \
    MORE(0, 3); SBAR(); \
    STEP(sa, sb, CUR.k0, CUR.k1, q[3][0], q[3][1], o1, CUR.v1, o2, CUR.v0); \
    MORE(3, 6); \
    if (J < 15) landed<NW>(NXT.k0, NXT.k1, NXT.v0, NXT.v1); \
    SBAR(); \
    STEP(sb, sa, NXT.k0, NXT.k1, q[0][0], q[0][1], o2, CUR.v1, o3, CUR.v0); \
    MORE(6, 8); SBAR(); } while (0)
        const rsrc_t rp = make_rsrc(ws + WS_KVP + (size_t)h * 16 * 8192, 16 * 8192);
        const rsrc_t rwo = make_rsrc(ws + WS_WO + (size_t)(14 + (live ? nt : 0)) * 32768, 32768);
        u32x4 pe[8], po[8];
        float sm[8] = {};
        u32x2 pp;
        const unsigned pp_word = lane < 16 ? WS_FP + (h * 4 + wid) * 128 + lane * 8 : WS_FWO + (((lane >> 5) & 1) * 2 + (live ? nt / 7 : 1)) * 128 + (lane & 15) * 8;
        const unsigned pob_word = WS_FOB + (qt * 16 + wid * 4 + (lane & 3)) * 8;
        auto part_load = [&](u32x4 (&r)[8], int n, int first, int last) {
#pragma unroll
            for (int i = 0; i < 8; ++i) if (i >= first && i < last) ld128_async_dev(r[i], rp, voff + (i & 1) * 1024, (4 * n + (i >> 1)) * 8192 + wid * 2048);
        };
        auto part_add = [&](const u32x4 (&r)[8]) {
#pragma unroll
            for (int i = 0; i < 8; ++i)
#pragma unroll
                for (int e = 0; e < 4; ++e) { sm[(i & 1) * 4 + e] += __uint_as_float(r[i][e]); asm volatile("" : "+v"(sm[(i & 1) * 4 + e])); }
        };
        auto wo_load = [&](int n, int first, int last) {
#pragma unroll
            for (int i = 0; i < 8; ++i) if (i >= first && i < last) ld128_async_acc(wob_f[8 * n + i], rwo, voff + (i & 3) * 1024, (2 * n + (i >> 2)) * 4096);
        };
        auto finish_b = [&]() {
            u32x4 f = {pk(sm[0], sm[1]), pk(sm[2], sm[3]), pk(sm[4], sm[5]), pk(sm[6], sm[7])};
            if (r32 == 29) {
                const unsigned tail = ((wid & 1) && hi) ? 0u : 0x3F803F80u;
                f = u32x4{0x3F803F80u, 0x3F803F80u, tail, tail};
            }
            sts128(L_STATE + wid * 1024 + voff, f);
            lds_bar();
            f32x16 np = mfma(lds128(L_STATE + voff), eq[0], zero); np = mfma(lds128(L_STATE + 1024 + voff), eq[1], np);
            f32x16 nn = mfma(lds128(L_STATE + 2048 + voff), eq[2], zero); nn = mfma(lds128(L_STATE + 3072 + voff), eq[3], nn);
            const float rp_ = __builtin_amdgcn_rcpf(np[13]), rn_ = __builtin_amdgcn_rcpf(nn[13]);
            const float inv = __builtin_amdgcn_rcpf(fmaf(np[12], rp_, fmaf(nn[12], rn_, 1e-6f)));
            const float ap = upper_val(rp_ * inv), an = upper_val(rn_ * inv);
            f32x16 o;
#pragma unroll
            for (int i = 0; i < 16; ++i) o[i] = fmaf(np[i], ap, nn[i] * an);
            u32x4 g0 = pack_lo(o), g1 = pack_hi(o);
            if (hi) { g1[2] = 0; g1[3] = 0; }
            const rsrc_t ro = make_rsrc(ws + WS_OB + (size_t)(tile * 16 + h) * 2048, 2048);
            st128_async(g0, ro, voff, 0); st128_async(g1, ro, voff + 1024, 0);
        };
#define NONE(x, y) (void)0
#define PE0(x, y) part_load(pe, 0, x, y)
#define PO1(x, y) part_load(po, 1, x, y)
#define PE2(x, y) part_load(pe, 2, x, y)
#define PO3(x, y) part_load(po, 3, x, y)
#define WO0(x, y) wo_load(0, x, y)
#define WO1(x, y) wo_load(1, x, y)
#define WO2(x, y) wo_load(2, x, y)
#define WO3(x, y) wo_load(3, x, y)
        TILE(0, a, d, b, 10, (void)0, NONE);
        TILE(1, b, a, c, 11, (wait_vm<8>(), publish(ws, WS_FP + ((h * 4 + wid) * 16 + rb) * 8, nonce)), NONE);
        TSTAMP(19); TILE(2, c, b, d, 9, (void)0, NONE);
        TILE(3, d, c, a, 9, (void)0, NONE);
        TSTAMP(20); TILE(4, a, d, b, 9, peek(pp, ws, pp_word), NONE);
        TSTAMP(21); TILE(5, b, a, c, 15, seen<4>(pp, ws, pp_word, nonce), PE0);
        TSTAMP(22); TILE(6, c, b, d, 23, (void)0, PO1);
        TSTAMP(23); TILE(7, d, c, a, 30, (landed8<16>(pe), part_add(pe)), PE2);
        TILE(8, a, d, b, 30, (landed8<16>(po), part_add(po)), PO3);
        TILE(9, b, a, c, 24, (landed8<16>(pe), part_add(pe)), NONE);
        TSTAMP(24); TILE(10, c, b, d, 18, (landed8<8>(po), part_add(po), finish_b()), NONE);
        TSTAMP(25); TILE(11, d, c, a, 16, (void)0, WO0);
        TSTAMP(26); TILE(12, a, d, b, 25, (wait_vm<16>(), publish(ws, WS_FOB + (tile * 16 + h) * 8, nonce)), WO1);
        TSTAMP(27); TILE(13, b, a, c, 27, (void)0, WO2);
        TILE(14, c, b, d, 23, (void)0, WO3);
        TSTAMP(28); TILE(15, d, c, a, 0, peek(pob, ws, pob_word), NONE);
        pb[2] = pk(e12, e13); pb[3] = pk(e14, e15);
        o3 = mfma(d.v1, pb, o3);
#undef TILE
#undef STEP
#undef EX
#undef QK0
#undef QK1
#undef NONE
#undef PE0
#undef PO1
#undef PE2
#undef PO3
#undef WO0
#undef WO1
#undef WO2
#undef WO3
        STAMP(11);
        seen<0>(pob, ws, pob_word, nonce);
        {
            const rsrc_t ro = make_rsrc(ws + WS_OB + (size_t)(qt * 16 + wid * 4) * 2048, 8192);
#pragma unroll
            for (int i = 0; i < 8; ++i) dma16_dev(ro, L_OIMG + (wid * 8 + i) * 1024, voff, i * 1024);
        }
        bad = combine(wid, o0, o1, o2, o3, ref, f0, f1);
        st128_async(f0, roa, voff, 0); st128_async(f1, roa, voff + 1024, 0);
        STAMP(12);
        if (__any(bad) && lane == 0) *(LAS unsigned*)(size_t)L_FLAG = 1;
        wait_bar<2>();
        if (__builtin_amdgcn_readfirstlane(*(const LAS unsigned*)(size_t)L_FLAG)) {
#pragma unroll
            for (int t = 0; t < 4; ++t) { if (hi) { q[t][1][2] = 0; q[t][1][3] = 0; } ref[t] = -__builtin_inff(); }
#pragma unroll 1
            for (int j = 0; j < 16; ++j) {
                const u32x4 k0 = ld128_dev(rk, voff, j * 4096), k1 = ld128_dev(rk, voff + 1024, j * 4096);
#pragma unroll
                for (int t = 0; t < 4; ++t) { f32x16 s = mfma(k0, q[t][0], zero); s = mfma(k1, q[t][1], s); ref[t] = fmaxf(ref[t], max16(s)); }
            }
            take_reference(q, ref, 0.f, hi);
            f32x16 x[4] = {};
#pragma unroll 1
            for (int j = 0; j < 16; ++j) {
                const u32x4 k0 = ld128_dev(rk, voff, j * 4096), k1 = ld128_dev(rk, voff + 1024, j * 4096);
                const u32x4 v0 = ld128_dev(rk, voff + 2048, j * 4096), v1 = ld128_dev(rk, voff + 3072, j * 4096);
#pragma unroll
                for (int t = 0; t < 4; ++t) {
                    f32x16 s = mfma(k0, q[t][0], zero); s = mfma(k1, q[t][1], s);
#pragma unroll
                    for (int i = 0; i < 16; ++i) s[i] = __builtin_amdgcn_exp2f(s[i]);
                    x[t] = mfma(v0, pack_lo(s), x[t]); x[t] = mfma(v1, pack_hi(s), x[t]);
                }
            }
            combine(wid, x[0], x[1], x[2], x[3], ref, f0, f1);
            st128_async(f0, roa, voff, 0); st128_async(f1, roa, voff + 1024, 0);
        }
    }
#undef LDKV
    {
        const int lane = lane_id();
        const unsigned voff = lane * 16;
#pragma unroll
        for (int i = 0; i < 32; i += 8)
            asm volatile("" : "+a"(wob_f[i]), "+a"(wob_f[i + 1]), "+a"(wob_f[i + 2]), "+a"(wob_f[i + 3]), "+a"(wob_f[i + 4]), "+a"(wob_f[i + 5]), "+a"(wob_f[i + 6]), "+a"(wob_f[i + 7]));
        f32x16 r = {};
        asm volatile("s_nop 1" : "+v"(r));
        u32x4 woa_f[32];
        const rsrc_t rw = make_rsrc(ws + WS_WO + (size_t)(live ? nt : 0) * 32768, 32768);
        u32x4 of[32];
#pragma unroll
        for (int i = 0; i < 6; ++i) of[i] = lds128(L_OIMG + i * 1024 + voff);
#pragma unroll
        for (int i = 0; i < 32; ++i) {
            if (i + 6 < 32) of[i + 6] = lds128(L_OIMG + (i + 6) * 1024 + voff);
            SBAR();
            if (i == OSPLIT) {
                wait_vm<0>();
                publish(ws, WS_FOA + (tile * 16 + h) * 8, nonce);
                STAMP(13);
            }
            asm volatile("v_mfma_f32_32x32x16_bf16 %0, %1, %2, %0" : "+v"(r) : "a"(wob_f[i]), "v"(of[i]));
            if (i >= OSPLIT) {
#pragma unroll
                for (int k = (i - OSPLIT) * 32 / (32 - OSPLIT); k < (i + 1 - OSPLIT) * 32 / (32 - OSPLIT); ++k) ld128_async_dev(woa_f[k], rw, voff + (k & 3) * 1024, (k >> 2) * 4096);
            }
            SBAR();
        }
        settle(r);
        stage_out(L_OUT, wid, lane, r);
        lds_bar();
        STAMP(14);
        u32x2 poa;
        const unsigned poa_word = WS_FOA + (qt * 16 + wid * 4 + (lane & 3)) * 8;
        peek(poa, ws, poa_word);
        seen<0>(poa, ws, poa_word, nonce);
        STAMP(15);
        {
            const rsrc_t ro = make_rsrc(ws + WS_OA + (size_t)(qt * 16 + wid * 4) * 2048, 8192);
#pragma unroll
            for (int i = 0; i < 8; ++i) dma16_dev(ro, L_OIMG + (wid * 8 + i) * 1024, voff, i * 1024);
        }
        flush_out(L_OUT, wid, lane, h, qt, 1, out);
        wait_bar<2>();
        if (blk == 0 && wid == 0) publish(ws, WS_EPOCH, nonce);
#pragma unroll
        for (int i = 0; i < 32; i += 8)
            asm volatile("" : "+v"(woa_f[i]), "+v"(woa_f[i + 1]), "+v"(woa_f[i + 2]), "+v"(woa_f[i + 3]), "+v"(woa_f[i + 4]), "+v"(woa_f[i + 5]), "+v"(woa_f[i + 6]), "+v"(woa_f[i + 7]));
        f32x16 s;
#pragma unroll
        for (int i = 0; i < 6; ++i) of[i] = lds128(L_OIMG + i * 1024 + voff);
#pragma unroll
        for (int i = 0; i < 32; ++i) {
            if (i + 6 < 32) of[i + 6] = lds128(L_OIMG + (i + 6) * 1024 + voff);
            SBAR();
            if (i == 0) mfma_v0(s, woa_f[0], of[0]); else mfma_v(s, woa_f[i], of[i]);
            SBAR();
        }
        settle(s);
        STAMP(16);
        stage_out(L_OUT + 16384, wid, lane, s);
        lds_bar();
        flush_out(L_OUT + 16384, wid, lane, h, qt, 0, out);
    }
}

}

extern "C" void kernel_launch(void* const* d_in, const int* in_sizes, int n_in, void* d_out, int out_size, void* d_ws, size_t ws_size, hipStream_t stream) {
    (void)hipFuncSetAttribute((const void*)fused, hipFuncAttributeMaxDynamicSharedMemorySize, LDS_BYTES);
    fused<<<256, 256, LDS_BYTES, stream>>>((const u16*)d_in[0], (const u16*)d_in[1], (const u16*)d_in[2], (const u16*)d_in[3], (const u16*)d_in[4], (const u16*)d_in[5],
                                           (const u16*)d_in[6], (u16*)d_out, (char*)d_ws);
}
```

```cpp
#include <hip/hip_runtime.h>
#include <stdint.h>

namespace {

typedef unsigned short u16;
typedef unsigned long long u64;
typedef __attribute__((ext_vector_type(16))) float f32x16;
typedef __attribute__((ext_vector_type(4))) float f32x4;
typedef __attribute__((ext_vector_type(2))) float f32x2;
typedef __attribute__((ext_vector_type(8))) __bf16 bf16x8;
typedef __attribute__((ext_vector_type(2))) __bf16 bf16x2;
typedef __attribute__((ext_vector_type(4))) unsigned u32x4;
typedef __attribute__((ext_vector_type(2))) unsigned u32x2;
typedef __attribute__((ext_vector_type(4))) short s16x4;
typedef __amdgpu_buffer_rsrc_t rsrc_t;
#define LAS __attribute__((address_space(3)))
#define GAS __attribute__((address_space(1)))
#define DI __device__ __forceinline__
#define SBAR() __builtin_amdgcn_sched_barrier(0)

constexpr int SEQ = 2048, DM = 448;
constexpr unsigned XROW = DM * 2;
constexpr unsigned WROW = 3 * DM * 2;
constexpr unsigned X_BYTES = SEQ * XROW, W_BYTES = DM * WROW, WO_BYTES = DM * XROW;
constexpr unsigned WS_EPOCH = 0;
constexpr unsigned WS_FK = 4096;
constexpr unsigned WS_FP = WS_FK + 8192;
constexpr unsigned WS_FOA = WS_FP + 8192;
constexpr unsigned WS_FOB = WS_FOA + 8192;
constexpr unsigned WS_FW = WS_FOB + 8192;
constexpr unsigned WS_FWO = WS_FW + 2048;
constexpr unsigned WS_FS = WS_FWO + 2048;
constexpr unsigned WS_VOID = WS_FS + 8192;
constexpr size_t WS_KVA = 65536;
constexpr size_t WS_OA = WS_KVA + (4u << 20);
constexpr size_t WS_OB = WS_OA + (2u << 20);
constexpr size_t WS_KVP = WS_OB + (2u << 20);
constexpr size_t WS_WB = WS_KVP + (2u << 20);
constexpr size_t WS_WO = WS_WB + (2u << 20);
constexpr size_t WS_ST = WS_WO + (1u << 20);
constexpr int LDS_BYTES = 160 * 1024;
constexpr unsigned L_WKV = 0, L_WQ = 57344, L_RET = 57344;
constexpr unsigned L_X = 86016;
constexpr unsigned l_kvb(int ch) { return ch == 0 ? 151552 : (ch - 1) * 8192; }
constexpr unsigned L_QX = 49152, L_REF = 40960, L_WPHI = 159744, L_FLAG = 161792;
constexpr unsigned L_PHI = 86016, L_RED = 0;
constexpr unsigned L_COMB = 0, L_OIMG = 65536, L_OUT = 98304;
constexpr float LOG2E = 1.4426950408889634f;
constexpr float QSCALE = 0.18898223650461362f * LOG2E;
#define REF_OFF 100.f
constexpr float REF_OFFSET = REF_OFF;
constexpr float L_LIMIT = 18446744073709551616.f;
constexpr int SC1 = 16;
constexpr unsigned OOB = 0x80000000u;
#define SPINLOG 16
constexpr unsigned SPIN_CAP = 1u << SPINLOG;
#define PSLEEP 4

DI f32x16 mfma(u32x4 a, u32x4 b, f32x16 c) {
    return __builtin_amdgcn_mfma_f32_32x32x16_bf16(__builtin_bit_cast(bf16x8, a), __builtin_bit_cast(bf16x8, b), c, 0, 0, 0);
}
DI unsigned pk(float lo, float hi) { f32x2 v = {lo, hi}; return __builtin_bit_cast(unsigned, __builtin_convertvector(v, bf16x2)); }
DI u32x4 pack_lo(const f32x16& x) { return u32x4{pk(x[0], x[1]), pk(x[2], x[3]), pk(x[4], x[5]), pk(x[6], x[7])}; }
DI u32x4 pack_hi(const f32x16& x) { return u32x4{pk(x[8], x[9]), pk(x[10], x[11]), pk(x[12], x[13]), pk(x[14], x[15])}; }
DI u32x4 quad(const f32x16& x, int g) { return u32x4{__float_as_uint(x[4 * g]), __float_as_uint(x[4 * g + 1]), __float_as_uint(x[4 * g + 2]), __float_as_uint(x[4 * g + 3])}; }
DI rsrc_t make_rsrc(const void* p, unsigned bytes) { return __builtin_amdgcn_make_buffer_rsrc((void*)p, 0, bytes, 0x00020000); }
DI u32x4 ld128(rsrc_t rs, unsigned voff, unsigned soff) { return __builtin_amdgcn_raw_buffer_load_b128(rs, voff, soff, 0); }
DI u32x4 ld128_dev(rsrc_t rs, unsigned voff, unsigned soff) { return __builtin_amdgcn_raw_buffer_load_b128(rs, voff, soff, SC1); }
DI void st128_dev(u32x4 v, rsrc_t rs, unsigned voff, unsigned soff) { __builtin_amdgcn_raw_buffer_store_b128(v, rs, voff, soff, SC1); }
DI void dma16(rsrc_t rs, unsigned lds_addr, unsigned voff, unsigned soff) {
    asm volatile("s_mov_b32 m0, %0\n\ts_nop 0\n\tbuffer_load_dwordx4 %1, %2, %3 offen lds" ::"s"(lds_addr), "v"(voff), "s"(rs), "s"(soff) : "memory");
}
DI void ld128_async(u32x4& d, rsrc_t rs, unsigned voff, unsigned soff) {
    asm volatile("buffer_load_dwordx4 %0, %1, %2, %3 offen" : "=v"(d) : "v"(voff), "s"(rs), "s"(soff) : "memory");
}
DI void dma16_dev(rsrc_t rs, unsigned lds_addr, unsigned voff, unsigned soff) {
    asm volatile("s_mov_b32 m0, %0\n\ts_nop 0\n\tbuffer_load_dwordx4 %1, %2, %3 offen sc1 lds" ::"s"(lds_addr), "v"(voff), "s"(rs), "s"(soff) : "memory");
}
template <int N> DI void wait_vm() { asm volatile("s_waitcnt vmcnt(%0)" ::"n"(N) : "memory"); }
template <int N> DI void wait_bar() { asm volatile("s_waitcnt vmcnt(%0) lgkmcnt(0)\n\ts_barrier" ::"n"(N) : "memory"); }
DI void lds_bar() { asm volatile("s_waitcnt lgkmcnt(0)\n\ts_barrier" ::: "memory"); }
DI u32x4 lds128(unsigned a) { return *(const LAS u32x4*)(size_t)a; }
DI void sts128(unsigned a, u32x4 v) { *(LAS u32x4*)(size_t)a = v; }
DI void sts64(unsigned a, u32x2 v) { *(LAS u32x2*)(size_t)a = v; }
DI u32x2 tr64(unsigned a) { return __builtin_bit_cast(u32x2, __builtin_amdgcn_ds_read_tr16_b64_v4i16((LAS s16x4*)(size_t)a)); }
DI u32x4 tr_frag(unsigned a0, unsigned a1) { u32x2 lo = tr64(a0), hi = tr64(a1); return u32x4{lo[0], lo[1], hi[0], hi[1]}; }
DI float max3(float a, float b, float c) { float r; asm("v_max3_f32 %0, %1, %2, %3" : "=v"(r) : "v"(a), "v"(b), "v"(c)); return r; }
DI float min3(float a, float b, float c) { float r; asm("v_min3_f32 %0, %1, %2, %3" : "=v"(r) : "v"(a), "v"(b), "v"(c)); return r; }
DI float both_max(float v) { auto r = __builtin_amdgcn_permlane32_swap(__float_as_uint(v), __float_as_uint(v), false, false); return max3(__uint_as_float(r[0]), __uint_as_float(r[1]), __uint_as_float(r[1])); }
DI float both_min(float v) { auto r = __builtin_amdgcn_permlane32_swap(__float_as_uint(v), __float_as_uint(v), false, false); return min3(__uint_as_float(r[0]), __uint_as_float(r[1]), __uint_as_float(r[1])); }
DI float both_sum(float v) { auto r = __builtin_amdgcn_permlane32_swap(__float_as_uint(v), __float_as_uint(v), false, false); return __uint_as_float(r[0]) + __uint_as_float(r[1]); }
DI float upper_val(float v) { auto r = __builtin_amdgcn_permlane32_swap(__float_as_uint(v), __float_as_uint(v), false, false); return __uint_as_float(r[1]); }
DI float max16(const f32x16& s) {
    const float a = max3(s[0], s[1], s[2]), b = max3(s[3], s[4], s[5]), c = max3(s[6], s[7], s[8]), d = max3(s[9], s[10], s[11]), e = max3(s[12], s[13], s[14]);
    return max3(max3(a, b, c), max3(d, e, s[15]), a);
}
DI float min16(const f32x16& s) {
    const float a = min3(s[0], s[1], s[2]), b = min3(s[3], s[4], s[5]), c = min3(s[6], s[7], s[8]), d = min3(s[9], s[10], s[11]), e = min3(s[12], s[13], s[14]);
    return min3(min3(a, b, c), min3(d, e, s[15]), a);
}

DI void ld128_async_dev(u32x4& d, rsrc_t rs, unsigned voff, unsigned soff) {
    asm volatile("buffer_load_dwordx4 %0, %1, %2, %3 offen sc1" : "=v"(d) : "v"(voff), "s"(rs), "s"(soff) : "memory");
}
DI void ld128_async_acc(u32x4& d, rsrc_t rs, unsigned voff, unsigned soff) {
    asm volatile("buffer_load_dwordx4 %0, %1, %2, %3 offen sc1" : "=a"(d) : "v"(voff), "s"(rs), "s"(soff) : "memory");
}
DI void st128_async(u32x4 v, rsrc_t rs, unsigned voff, unsigned soff) {
    asm volatile("buffer_store_dwordx4 %0, %1, %2, %3 offen sc1\n\ts_nop 1" ::"v"(v), "v"(voff), "s"(rs), "s"(soff) : "memory");
}
template <int N> DI void landed(u32x4& a, u32x4& b, u32x4& c, u32x4& d) { asm volatile("s_waitcnt vmcnt(%4)" : "+v"(a), "+v"(b), "+v"(c), "+v"(d) : "n"(N) : "memory"); }
template <int N> DI void landed8(u32x4 (&r)[8]) {
    asm volatile("s_waitcnt vmcnt(%8)" : "+v"(r[0]), "+v"(r[1]), "+v"(r[2]), "+v"(r[3]), "+v"(r[4]), "+v"(r[5]), "+v"(r[6]), "+v"(r[7]) : "n"(N) : "memory");
}
DI void mfma_v(f32x16& acc, u32x4 a, u32x4 b) { asm("v_mfma_f32_32x32x16_bf16 %0, %1, %2, %0" : "+v"(acc) : "v"(a), "v"(b)); }
DI void mfma_v0(f32x16& acc, u32x4 a, u32x4 b) { asm("s_nop 1\n\tv_mfma_f32_32x32x16_bf16 %0, %1, %2, 0" : "=&v"(acc) : "v"(a), "v"(b)); }
DI void settle(f32x16& x) { asm volatile("s_nop 11" : "+v"(x)); }
DI void barrier() { asm volatile("s_barrier" ::: "memory"); }
DI int lane_id() { int l; asm volatile("v_mbcnt_lo_u32_b32 %0, -1, 0\n\tv_mbcnt_hi_u32_b32 %0, -1, %0" : "=v"(l)); return l; }

#define STAMP(id) do {} while (0)
#define TSTAMP(id) do {} while (0)

DI void epoch_request(u64& v, const char* p) { asm volatile("s_load_dwordx2 %0, %1, 0x0" : "=s"(v) : "s"(p) : "memory"); }
DI void publish(char* ws, unsigned word, u64 nonce) {
    asm volatile("s_mov_b64 exec, 1\n\tglobal_store_dwordx2 %0, %1, %2 sc1\n\ts_mov_b64 exec, -1" ::"v"(word), "v"(nonce), "s"(ws) : "memory");
}
DI void peek(u32x2& v, const char* ws, unsigned word) { asm volatile("global_load_dwordx2 %0, %1, %2 sc1" : "=v"(v) : "v"(word), "s"(ws) : "memory"); }
DI void peek(u32x4& v, const char* ws, unsigned word) { asm volatile("global_load_dwordx4 %0, %1, %2 sc1" : "=v"(v) : "v"(word), "s"(ws) : "memory"); }
DI bool match(const u32x2& v, u64 n) { return v[0] == (unsigned)n && v[1] == (unsigned)(n >> 32); }
DI bool match(const u32x4& v, u64 n) { return v[0] == (unsigned)n && v[1] == (unsigned)(n >> 32) && v[2] == (unsigned)n && v[3] == (unsigned)(n >> 32); }
template <int N, class V> DI void seen(V& v, const char* ws, unsigned word, u64 nonce) {
    asm volatile("s_waitcnt vmcnt(%1)" : "+v"(v) : "n"(N) : "memory");
#pragma unroll 1
    for (unsigned spins = 0; spins < SPIN_CAP && !__all(match(v, nonce)); ++spins) {
        __builtin_amdgcn_s_sleep(PSLEEP);
        peek(v, ws, word);
        asm volatile("s_waitcnt vmcnt(0)" : "+v"(v) :: "memory");
    }
}

DI void mfma_pair(int isv, u32x4 xf, u32x4 w0, u32x4 w1, f32x16& acc0, f32x16& acc1) {
    asm("s_cmp_lg_u32 %5, 0\n\ts_cbranch_scc1 1f\n\t"
        "v_mfma_f32_32x32x16_bf16 %0, %3, %2, %0\n\tv_mfma_f32_32x32x16_bf16 %1, %4, %2, %1\n\ts_branch 2f\n"
        "1:\n\tv_mfma_f32_32x32x16_bf16 %0, %2, %3, %0\n\tv_mfma_f32_32x32x16_bf16 %1, %2, %4, %1\n2:"
        : "+v"(acc0), "+v"(acc1) : "v"(xf), "v"(w0), "v"(w1), "s"(isv) : "scc");
}
DI void pin(u32x4& a, u32x4& b, u32x4& c, u32x4& d) { asm volatile("" : "+v"(a), "+v"(b), "+v"(c), "+v"(d) :: "memory"); }

DI void set_reference(u32x4& q1, float nm, int hi) {
    const unsigned a = __float_as_uint(nm) & 0xFFFF0000u;
    const float r1 = nm - __uint_as_float(a);
    const unsigned b = __float_as_uint(r1) & 0xFFFF0000u;
    const float r2 = r1 - __uint_as_float(b);
    if (hi) { q1[2] = (a >> 16) | b; q1[3] = __float_as_uint(r2) >> 16; }
}

struct KV { u32x4 k0, k1, v0, v1; };

DI void ref_post(int wid, int lane, const float (&m)[4]) {
#pragma unroll
    for (int t = 0; t < 4; ++t) *(LAS float*)(size_t)(L_REF + ((wid + t) & 3) * 512 + (lane & 31) * 16 + wid * 4) = both_max(m[t]);
}
DI void ref_take(int wid, int lane, u32x4 (&q)[4][2], float offset) {
#pragma unroll
    for (int t = 0; t < 4; ++t) {
        const f32x4 m = __builtin_bit_cast(f32x4, lds128(L_REF + ((wid + t) & 3) * 512 + (lane & 31) * 16));
        set_reference(q[t][1], -(max3(max3(m[0], m[1], m[2]), m[3], m[3]) + offset), lane >> 5);
    }
}

DI void combine_post(int wid, int lane, const f32x16& o1, const f32x16& o2, const f32x16& o3) {
    const f32x16* ot[3] = {&o1, &o2, &o3};
#pragma unroll
    for (int t = 0; t < 3; ++t) {
        const unsigned at = L_COMB + (((wid + t + 1) & 3) * 3 + t) * 4096 + lane * 16;
#pragma unroll
        for (int g = 0; g < 4; ++g) sts128(at + g * 1024, quad(*ot[t], g));
    }
}
DI bool combine_take(int wid, int lane, f32x16 o, u32x4& f0, u32x4& f1) {
#pragma unroll
    for (int v = 0; v < 12; ++v) {
        const u32x4 p = lds128(L_COMB + wid * 12288 + v * 1024 + lane * 16);
#pragma unroll
        for (int i = 0; i < 4; ++i) o[4 * (v & 3) + i] += __uint_as_float(p[i]);
    }
    const float l = upper_val(o[12]);
    o *= __builtin_amdgcn_rcpf(l);
    float chk = 0.f;
#pragma unroll
    for (int i = 0; i < 16; ++i) chk = fmaf(o[i], 0.f, chk);
    f0 = pack_lo(o); f1 = pack_hi(o);
    if (lane >> 5) { f1[2] = 0; f1[3] = 0; }
    return !(l < L_LIMIT) || !(chk == 0.f);
}

DI void stage_out(unsigned img, int wid, int lane, const f32x16& r) {
    const u32x4 lo = pack_lo(r), up = pack_hi(r);
    const unsigned wr = img + (lane & 31) * 272 + wid * 64 + (lane >> 5) * 8;
    sts64(wr, u32x2{lo[0], lo[1]}); sts64(wr + 16, u32x2{lo[2], lo[3]}); sts64(wr + 32, u32x2{up[0], up[1]}); sts64(wr + 48, u32x2{up[2], up[3]});
}
DI void flush_out(unsigned img, int wid, int lane, int h, int qt, int br, u16* out) {
    const unsigned width = h >= 12 ? 128 : 256, colb = (lane & 15) * 16;
    const rsrc_t rd = make_rsrc((char*)out + (size_t)qt * 32 * (2 * XROW) + br * XROW + (h >> 2) * 256, 31 * 2 * XROW + width);
#pragma unroll
    for (int i = 0; i < 2; ++i) {
        const unsigned row = wid * 8 + i * 4 + (lane >> 4);
        st128_async(lds128(img + row * 272 + colb), rd, colb < width ? row * (2 * XROW) + colb : OOB, 0);
    }
}

__global__ __launch_bounds__(256, 1) void fused(const u16* xa, const u16* wa, const u16* woa, const u16* xb, const u16* wb, const u16* wphi, const u16* wob, u16* out, char* ws) {
    STAMP(0);
    const int wid = __builtin_amdgcn_readfirstlane(threadIdx.x >> 6);
    const int blk = blockIdx.x, xcd = blk & 7, bi = blk >> 3;
    const int h = (xcd & 1) * 8 + (bi & 7), rb = (xcd >> 1) * 4 + (bi >> 3), tile = rb * 4 + wid;
    const int rot = ((bi >> 3) + (bi & 7)) % 3;
    auto chunk_at = [&](int p) { const int v = p % 3 + rot; return p == 6 ? 6 : p - p % 3 + (v >= 3 ? v - 3 : v); };
    u64 epoch; epoch_request(epoch, ws + WS_EPOCH);
    unsigned code_probe;
    u64 code_pc;
    {
        unsigned code_len;
        asm volatile("s_getpc_b64 %0\n.Lfused_code_begin:\n\ts_mov_b32 %1, .Lfused_code_end-.Lfused_code_begin" : "=s"(code_pc), "=s"(code_len));
        const unsigned at = (bi * 16 + wid * 4 + (threadIdx.x & 3)) * 128;
        asm volatile("global_load_dword %0, %1, %2" : "=v"(code_probe) : "v"(at < code_len ? at : 0), "s"(code_pc) : "memory");
    }
    u64 nonce;
    const f32x16 zero = {};
    u32x4 q[4][2], eq[4];
    KV a, b, c, d;
    const rsrc_t rk = make_rsrc(ws + WS_KVA + (size_t)(h * 64 + wid * 16) * 4096, 16 * 4096);
#define LDKV(X, j) do { ld128_async_dev(X.v1, rk, voff + 3072, (j) * 4096); ld128_async_dev(X.v0, rk, voff + 2048, (j) * 4096); \
                        ld128_async_dev(X.k1, rk, voff + 1024, (j) * 4096); ld128_async_dev(X.k0, rk, voff, (j) * 4096); } while (0)
    {
        const int lane = lane_id(), hi = lane >> 5, r32 = lane & 31, q4 = (lane & 15) >> 2;
        const unsigned voff = lane * 16, col = 32 * ((lane >> 4) & 1) + 8 * (lane & 3);
        const rsrc_t rxa = make_rsrc(xa, X_BYTES), rwa = make_rsrc(wa, W_BYTES), rxb = make_rsrc(xb, X_BYTES);
        const int ug = blk & 31, ucg = blk >> 5, ubr = (blk >> 5) & 1, uh = (blk >> 1) & 15, uhalf = blk & 1;
        const bool unit_o = blk >= 192, unit_w = !unit_o && ug < 28;
        const unsigned mask_o = (unsigned)((191 - blk) >> 31), mask_w = ~mask_o & (unsigned)((ug - 28) >> 31);
        if (unit_w) {
            const rsrc_t rw = make_rsrc(wb, W_BYTES);
#pragma unroll
            for (int j = 0; j < 2; ++j) {
                const int i = 4 * wid + 2 * j + hi;
                dma16(rw, L_RET + (2 * wid + j) * 1024, (64 * (ug >> 2) + 8 * (ug & 3) + (i & 7) + 32 * (i >> 3)) * WROW + ucg * 448 + (r32 < 29 ? r32 : 28) * 16, 0);
            }
        } else if (unit_o) {
            const rsrc_t rw = make_rsrc(ubr ? wob : woa, WO_BYTES);
            for (int j = wid; j < 14; j += 4) dma16(rw, L_RET + j * 1024, (28 * uh + 2 * j + hi) * XROW + uhalf * 448 + (r32 < 28 ? r32 : 27) * 16, 0);
        }
        const int isv = h & 1, row8 = lane >> 3;
        u32x4 xf[28];
        f32x16 acc0 = {}, acc1 = {};
        u32x4 pw;
        const unsigned pw_word = WS_FW + (h >> 3) * 672 + (lane < 42 ? lane : 41) * 16;
        {
            const unsigned piece = (lane & 7) ^ (((row8 >> 1) & 1) << 2);
            const unsigned vw = (16 * wid + row8) * WROW + (1 + isv) * XROW + (h >> 1) * 112 + ((piece < 7 ? piece : 6) << 4);
            const unsigned vx0 = (tile * 32 + row8) * XROW + (((lane & 7) ^ (lane >> 4)) << 4), vx1 = (tile * 32 + 8 + row8) * XROW + (((lane & 7) ^ (4 + (lane >> 4))) << 4);
            auto req_x = [&](rsrc_t rx, int slot, unsigned k, int i) { dma16(rx, L_X + slot * 16384 + wid * 4096 + i * 1024, (i & 1) ? vx1 : vx0, k * 128 + (i >> 1) * 16 * XROW); };
            auto req_w = [&](int ch, int i) { dma16(rwa, L_WKV + ch * 8192 + wid * 2048 + i * 1024, vw, (chunk_at(ch) * 64 + 8 * i) * WROW); };
            auto issue = [&](int ch) {
                req_w(ch, 0); req_w(ch, 1);
#pragma unroll
                for (int i = 0; i < 4; ++i) req_x(rxa, ch & 3, chunk_at(ch), i);
            };
            issue(0); issue(1);
            unsigned rt_at[4], rt_2nd[4], rt_dst[4], rt_v[4];
            {
                const unsigned rd = L_RET + q4 * 512 + col + hi * (2048 + (mask_w & 2048));
                const unsigned mq = (unsigned)((ucg - 2) >> 31);
                const unsigned w_first = (ucg & 1) * 672 + ((mq & (56 + ug)) | (~mq & (2 * ug + (ucg >> 2)))), o_first = (ubr * 14 + uhalf * 7) * 32 + uh * 2;
#pragma unroll
                for (int i = 0; i < 4; ++i) {
                    const int f = wid + 4 * i, s = f & 1, t = f >> 1, hl = 2 * wid + i;
                    const unsigned mw = i < 2 ? mask_w : 0u, mo = mask_o & (unsigned)((f - 14) >> 31);
                    rt_at[i] = rd + ((mw & (hl * 56)) | (mo & (s * 8192 + t * 64)));
                    rt_2nd[i] = rt_at[i] + 4096 - ((mask_w | -(unsigned)(s & hi)) & 2048);
                    rt_dst[i] = (mw & ((w_first + hl * 84) * 1024)) | (mo & ((2u << 20) + (o_first + t * 32 + s) * 1024));
                    rt_v[i] = voff | (~(mw | mo) & OOB);
                    asm volatile("" : "+v"(rt_at[i]), "+v"(rt_2nd[i]), "+s"(rt_dst[i]), "+v"(rt_v[i]));
                }
            }
            const unsigned m0w = (unsigned)((wid - 1) >> 31);
            unsigned rt_word = (m0w & mask_w & (WS_FW + (((ucg & 1) * 3 + (ucg >> 1)) * 28 + ug) * 8)) | (m0w & mask_o & (WS_FWO + ((ubr * 2 + uhalf) * 16 + uh) * 8))
                             | (~(m0w & (mask_w | mask_o)) & (WS_VOID + (blk * 4 + wid) * 8));
            unsigned w0 = L_WKV + (32 * hi + q4) * 128 + (col ^ ((q4 >> 1) << 6)), w1 = L_WKV + (32 * hi + q4) * 128 + ((col + 56) ^ ((q4 >> 1) << 6));
            unsigned xo[4];
#pragma unroll
            for (int s = 0; s < 4; ++s) xo[s] = L_X + wid * 4096 + r32 * 128 + (((4 * hi + s) ^ ((r32 >> 1) & 7)) << 4);
            unsigned vwq = (16 * wid + (lane >> 2)) * WROW + h * 56 + (lane & 3) * 16;
            asm volatile("" : "+s"(rt_word), "+v"(w0), "+v"(w1), "+v"(xo[0]), "+v"(xo[1]), "+v"(xo[2]), "+v"(xo[3]), "+v"(vwq), "+v"(acc0), "+v"(acc1));
            issue(2); issue(3);
            asm volatile("s_waitcnt lgkmcnt(0)" : "+s"(epoch) :: "memory");
            nonce = epoch + 1;
            STAMP(1);
            const rsrc_t rwb = make_rsrc(ws + WS_WB + (size_t)h * 86016, 86016);
            auto req_kvb = [&](int ch, int j) { dma16_dev(rwb, l_kvb(ch) + wid * 2048 + j * 1024, voff, ch * 8192 + wid * 2048 + j * 1024); };
            auto req_qb = [&](int j) { dma16_dev(rwb, L_WQ + (wid * 7 + j) * 1024, voff, 57344 + (wid * 7 + j) * 1024); };
            auto req_wq = [&](int j) { dma16(rwa, L_WQ + j * 4096 + wid * 1024, vwq, chunk_at(j) * 64 * WROW); };
            auto xb_slot = [](int ch) { return (ch + 3) & 3; };
            u32x4 wf[2][8];
#pragma unroll
            for (int p = 0; p <= 7; ++p) {
                if (p < 7) {
                    if (p == 0 || p == 3) wait_vm<18>(); else if (p <= 2) wait_vm<22>(); else if (p == 4) wait_vm<13>(); else if (p == 5) wait_vm<11>(); else wait_vm<10>();
                    barrier();
                    if (p == 0) asm volatile("" :: "v"(code_probe), "s"(code_pc));
                    if (p == 0) { STAMP(2); }
                    if (p == 6) { STAMP(3); }
#pragma unroll
                    for (int s = 0; s < 4; ++s) xf[4 * p + s] = lds128(xo[s] + (p & 3) * 16384);
                }
#pragma unroll
                for (int s = 0; s < 4; ++s) {
                    if (p < 7) {
                        const unsigned o = p * 8192 + s * 1024;
                        wf[p & 1][2 * s] = tr_frag(w0 + o, w0 + o + 512); wf[p & 1][2 * s + 1] = tr_frag(w1 + o, w1 + o + 512);
                    }
                    SBAR();
                    if (p > 0) mfma_pair(isv, xf[4 * p - 4 + s], wf[(p - 1) & 1][2 * s], wf[(p - 1) & 1][2 * s + 1], acc0, acc1);
                    SBAR();
                    if (p == 0 && s == 0) {
                        const rsrc_t ro = make_rsrc(ws + WS_WB, 4u << 20);
#pragma unroll
                        for (int i = 0; i < 4; ++i) st128_async(tr_frag(rt_at[i], rt_2nd[i]), ro, rt_v[i], rt_dst[i]);
                    }
                    if (p < 7 && s == 1) pin(xf[4 * p], xf[4 * p + 1], xf[4 * p + 2], xf[4 * p + 3]);
                    if (p <= 2) {
                        if (s == 0) { req_w(p + 4, 0); req_w(p + 4, 1); }
                        if (s == 1 || s == 2) { req_x(rxa, p & 3, chunk_at(p + 4), 2 * s - 2); req_x(rxa, p & 3, chunk_at(p + 4), 2 * s - 1); }
                    }
                    if (p == 3 && s == 0) publish(ws, rt_word, nonce);
                    if (p == 4) req_wq(s);
                    if (p == 5 && s < 3) req_wq(4 + s);
                    if (p == 5 && s == 3) {
                        const rsrc_t rp = make_rsrc(wphi + h * 784, 1568);
                        dma16(rp, L_WPHI, voff, 0); dma16(rp, L_WPHI + 1024, voff, 1024);
                    }
                    if (p >= 6) { const int ch = 2 * (p - 6) + (s >> 1); req_x(rxb, xb_slot(ch), ch, 2 * (s & 1)); req_x(rxb, xb_slot(ch), ch, 2 * (s & 1) + 1); }
                    if (p == 6 && s == 3) peek(pw, ws, pw_word);
                    SBAR();
                }
            }
            asm volatile("s_nop 3" : "+v"(acc0), "+v"(acc1));
            {
                u32x4 f[4] = {pack_lo(acc0), pack_hi(acc0), pack_lo(acc1), pack_hi(acc1)};
                const bool all = isv && r32 >= 28, tail = isv ? all : (bool)hi;
                const unsigned va = r32 == 28 ? 0x3F803F80u : 0u, v2 = isv ? va : 0x3F803F80u, v3 = isv ? va : 0x00003F80u;
#pragma unroll
                for (int i = 0; i < 4; ++i) {
                    f[i][0] = all ? va : f[i][0]; f[i][1] = all ? va : f[i][1];
                    if (i & 1) { f[i][2] = tail ? v2 : f[i][2]; f[i][3] = tail ? v3 : f[i][3]; }
                    else       { f[i][2] = all ? va : f[i][2]; f[i][3] = all ? va : f[i][3]; }
                }
                const rsrc_t ro = make_rsrc(ws + WS_KVA + (size_t)((h & 14) * 64 + tile) * 4096 + isv * 2048, 64 * 4096 + 2048);
                st128_async(f[0], ro, voff, 0); st128_async(f[1], ro, voff + 1024, 0); st128_async(f[2], ro, voff, 64 * 4096); st128_async(f[3], ro, voff + 1024, 64 * 4096);
            }
            STAMP(4);
            if (wid == 0 && lane == 0) *(LAS unsigned*)(size_t)L_FLAG = 0;
            seen<12>(pw, ws, pw_word, nonce);
            lds_bar();
            u32x4 xg[28];
            {
                const unsigned wo = L_WQ + (32 * hi + q4) * 64 + col;
                f32x16 acc;
                u32x4 wq[28];
#pragma unroll
                for (int i = 0; i < 4; ++i) wq[i] = tr_frag(wo + i * 512, wo + i * 512 + 256);
#pragma unroll
                for (int i = 0; i < 28; ++i) {
                    if (i + 4 < 28) { const unsigned o = ((i + 4) >> 2) * 4096 + (i & 3) * 512; wq[i + 4] = tr_frag(wo + o, wo + o + 256); }
                    if (i < 8) xg[i] = lds128(xo[i & 3] + xb_slot(i >> 2) * 16384);
                    if (i >= 19 && i < 27) xg[i - 11] = lds128(xo[(i - 11) & 3] + xb_slot((i - 11) >> 2) * 16384);
                    SBAR();
                    if (i == 0) mfma_v0(acc, wq[i], xf[i]); else mfma_v(acc, wq[i], xf[i]);
                    SBAR();
                    if (i < 14) req_kvb(i >> 1, i & 1);
                    if (i == 13) { pin(xg[0], xg[1], xg[2], xg[3]); pin(xg[4], xg[5], xg[6], xg[7]); }
                    if (i >= 14 && i < 18) req_x(rxb, xb_slot(4), 4, i - 14);
                    if (i == 18) {
                        wait_vm<18>();
                        publish(ws, WS_FK + (((h >> 1) * 64 + tile) * 2 + isv) * 8, nonce);
                        STAMP(5);
                    }
                    if (i >= 19 && i < 23) req_x(rxb, xb_slot(5), 5, i - 19);
                    if (i == 23) pin(xg[8], xg[9], xg[10], xg[11]);
                    if (i >= 24) req_x(rxb, xb_slot(6), 6, i - 24);
                    SBAR();
                }
                settle(acc);
                acc *= QSCALE;
                u32x4 q0 = pack_lo(acc), q1 = pack_hi(acc);
                if (hi) { q1[2] = 0; q1[3] = 0; }
                sts128(L_QX + wid * 2048 + voff, q0); sts128(L_QX + wid * 2048 + 1024 + voff, q1);
            }
            STAMP(6);
            u32x2 pk_;
            const unsigned pk_word = WS_FK + ((h >> 1) * 64 + wid * 16) * 16 + r32 * 8;
            f32x16 ak = {}, av = {};
            asm volatile("s_nop 1" : "+v"(ak), "+v"(av));
            {
                u32x4 wg[28][2];
                wait_bar<19>();
#pragma unroll
                for (int i = -3; i < 28; ++i) {
                    if (i + 3 == 16) { wait_vm<8>(); barrier(); }
                    if (i + 3 < 28) {
                        const unsigned at = l_kvb((i + 3) >> 2) + ((i + 3) & 3) * 2048 + voff;
                        wg[i + 3][0] = lds128(at); wg[i + 3][1] = lds128(at + 1024);
                        if (i + 3 >= 16) xg[i + 3] = lds128(xo[(i + 3) & 3] + xb_slot((i + 3) >> 2) * 16384);
                    }
                    SBAR();
                    if (i >= 0) { mfma_v(ak, wg[i][0], xg[i]); mfma_v(av, xg[i], wg[i][1]); }
                    SBAR();
                    if (i >= 0 && i < 7) req_qb(i);
                    if (i == 12) peek(pk_, ws, pk_word);
                    if (i == 20) { seen<0>(pk_, ws, pk_word, nonce); STAMP(8); }
                    if (i == 21) LDKV(a, 0);
                    SBAR();
                }
            }
            u32x4 wp[2];
            {
                const unsigned fc = 16 * ((lane >> 4) & 1) + 4 * (lane & 3);
                const unsigned a0 = L_WPHI + (4 * hi + q4) * 56 + (fc < 28 ? fc : 24) * 2;
                wp[0] = tr_frag(a0, a0 + 8 * 56); wp[1] = tr_frag(a0 + 16 * 56, a0 + (hi ? 20 : 24) * 56);
            }
            barrier();
            u32x4 wqb[28];
            unsigned wqa = L_WQ + voff;
            asm volatile("" : "+v"(wqa));
#pragma unroll
            for (int i = 0; i < 8; ++i) wqb[i] = lds128(wqa + i * 1024);
            asm volatile("s_nop 1" : "+v"(ak), "+v"(av));
            STAMP(7);
            f32x16 pkk, aq;
            {
                u32x4 f0 = pack_lo(ak), f1 = pack_hi(ak);
                if (hi) { f1[2] = 0; f1[3] = 0; }
                asm volatile("" : "+v"(f0), "+v"(f1));
                mfma_v0(pkk, wp[0], f0); mfma_v(pkk, wp[1], f1);
            }
#define QSTEP(i) do { SBAR(); if (((i) & 3) == 0) { pin(wqb[i], wqb[(i) + 1], wqb[(i) + 2], wqb[(i) + 3]); \
                          if ((i) + 8 < 28) { _Pragma("unroll") for (int k_ = (i) + 8; k_ < (i) + 12; ++k_) wqb[k_] = lds128(wqa + k_ * 1024); } } \
                      if ((i) == 0) mfma_v0(aq, wqb[0], xg[0]); else mfma_v(aq, wqb[i], xg[i]); SBAR(); } while (0)
            QSTEP(0);
            u32x4 v0 = pack_lo(av);
            QSTEP(1);
            u32x4 v1 = pack_hi(av);
            if (r32 >= 28) { const unsigned v = r32 == 28 ? 0x3F803F80u : 0u; v0 = u32x4{v, v, v, v}; v1 = v0; }
            asm volatile("" : "+v"(pkk), "+v"(v0), "+v"(v1));
            QSTEP(2);
            const unsigned img = L_PHI + wid * 4096;
            {
                f32x16 ep, en;
                const float hx = max16(pkk), hn = min16(pkk);
                QSTEP(3);
                const float mx = both_max(hx) * LOG2E, mn = both_min(hn) * LOG2E;
                const float live = hi ? 0.f : 1.f;
                float sp = 0.f, sn = 0.f;
                float ap = fmaf(pkk[15], LOG2E, -mx), an = fmaf(pkk[15], -LOG2E, mn);
#pragma unroll
                for (int i = 0; i < 16; ++i) {
                    QSTEP(4 + i);
                    ep[15 - i] = __builtin_amdgcn_exp2f(ap); en[15 - i] = __builtin_amdgcn_exp2f(an);
                    SBAR();
                    if (i < 15) { ap = fmaf(pkk[14 - i], LOG2E, -mx); an = fmaf(pkk[14 - i], -LOG2E, mn); }
                    if (i == 1) { sp = ep[15]; sn = en[15]; }
                    if (i > 1) { sp += ep[16 - i]; sn += en[16 - i]; }
                    if (i == 4) { sp *= live; sn *= live; }
                }
                QSTEP(20);
                const float rp = __builtin_amdgcn_rcpf(both_sum(sp + ep[0])), rn = __builtin_amdgcn_rcpf(both_sum(sn + en[0]));
                const float rp3 = rp * live, rn3 = rn * live;
                const unsigned wr = img + (r32 & 15) * 136 + (r32 >> 4) * 16384 + hi * 8;
#pragma unroll
                for (int g = 0; g < 4; ++g) {
                    const float u = g == 3 ? rp3 : rp, v = g == 3 ? rn3 : rn;
                    QSTEP(21 + 2 * g);
                    sts64(wr + g * 16, u32x2{pk(ep[4 * g] * u, ep[4 * g + 1] * u), pk(ep[4 * g + 2] * u, ep[4 * g + 3] * u)});
                    if (g < 3) QSTEP(22 + 2 * g);
                    sts64(wr + 64 + g * 16, u32x2{pk(en[4 * g] * v, en[4 * g + 1] * v), pk(en[4 * g + 2] * v, en[4 * g + 3] * v)});
                }
            }
#undef QSTEP
            LDKV(b, 1); LDKV(c, 2);
#pragma unroll
            for (int t = 0; t < 4; ++t) { const unsigned at = L_QX + ((wid + t) & 3) * 2048 + voff; q[t][0] = lds128(at); q[t][1] = lds128(at + 1024); }
            u32x4 pf[4];
            {
                const unsigned rd = img + (4 * hi + q4) * 136 + col;
#pragma unroll
                for (int i = 0; i < 4; ++i) pf[i] = tr_frag(rd + (i >> 1) * 64 + (i & 1) * 16384, rd + (i >> 1) * 64 + (i & 1) * 16384 + 8 * 136);
            }
            asm volatile("s_nop 3" : "+v"(aq));
            f32x16 pqq, kv0, kv1;
            {
                u32x4 g0 = pack_lo(aq), g1 = pack_hi(aq);
                if (hi) { g1[2] = 0; g1[3] = 0; }
                asm volatile("" : "+v"(g0), "+v"(g1));
                mfma_v0(pqq, wp[0], g0); mfma_v(pqq, wp[1], g1);
            }
            SBAR();
            mfma_v0(kv0, pf[0], v0); mfma_v(kv0, pf[1], v1); mfma_v0(kv1, pf[2], v0); mfma_v(kv1, pf[3], v1);
            asm volatile("s_nop 3" : "+v"(pqq));
            SBAR();
            f32x16 eqp, eqn;
            const float qmx = both_max(max16(pqq)) * LOG2E, qmn = both_min(min16(pqq)) * LOG2E;
            asm volatile("" : "+v"(kv0), "+v"(kv1) : "v"(qmx), "v"(qmn));
#pragma unroll
            for (int g = 0; g < 4; ++g) {
                sts128(L_RED + wid * 8192 + g * 1024 + voff, quad(kv0, g)); sts128(L_RED + wid * 8192 + 4096 + g * 1024 + voff, quad(kv1, g));
#pragma unroll
                for (int i = 4 * g; i < 4 * g + 4; ++i) { eqp[i] = fmaf(pqq[i], LOG2E, -qmx); eqn[i] = fmaf(pqq[i], -LOG2E, qmn); }
                SBAR();
#pragma unroll
                for (int i = 4 * g; i < 4 * g + 4; ++i) { eqp[i] = __builtin_amdgcn_exp2f(eqp[i]); eqn[i] = __builtin_amdgcn_exp2f(eqn[i]); }
                SBAR();
            }
            landed<8>(a.k0, a.k1, a.v0, a.v1);
            {
                f32x16 s[4];
#pragma unroll
                for (int t = 0; t < 4; ++t) { mfma_v0(s[t], a.k0, q[t][0]); mfma_v(s[t], a.k1, q[t][1]); }
                SBAR();
                eq[0] = pack_lo(eqp); eq[1] = pack_hi(eqp); eq[2] = pack_lo(eqn); eq[3] = pack_hi(eqn);
                asm volatile("" : "+v"(eq[0]), "+v"(eq[1]), "+v"(eq[2]), "+v"(eq[3]), "+v"(s[0]), "+v"(s[1]), "+v"(s[2]), "+v"(s[3]));
                const float m[4] = {max16(s[0]), max16(s[1]), max16(s[2]), max16(s[3])};
                ref_post(wid, lane, m);
            }
            lds_bar();
            {
                const rsrc_t ro = make_rsrc(ws + WS_KVP + (size_t)(h * 16 + rb) * 8192, 8192);
#pragma unroll
                for (int i = 0; i < 2; ++i) {
                    const unsigned off = (2 * wid + i) * 1024 + voff;
                    f32x4 u = __builtin_bit_cast(f32x4, lds128(L_RED + off));
#pragma unroll
                    for (int v = 1; v < 4; ++v) u += __builtin_bit_cast(f32x4, lds128(L_RED + v * 8192 + off));
                    st128_async(__builtin_bit_cast(u32x4, u), ro, off, 0);
                }
                STAMP(9);
                ref_take(wid, lane, q, REF_OFFSET);
            }
        }
    }
    const int qt = rb * 4 + (h & 3), nt = (h & 12) + wid;
    const bool live = nt < 14;
    u32x4 wob_f[32], woa_f[32], f0, f1;
    const rsrc_t rwa_o = make_rsrc(ws + WS_WO + (size_t)(live ? nt : 0) * 32768, 32768);
    const rsrc_t roa = make_rsrc(ws + WS_OA + (size_t)(tile * 16 + h) * 2048, 2048);
    {
        const int lane = lane_id(), hi = lane >> 5, r32 = lane & 31;
        const unsigned voff = lane * 16;
        STAMP(10);
        f32x16 o0 = {}, o1 = {}, o2 = {}, o3 = {};
        f32x16 sa, sb; u32x4 pa, pb = {};
        float e12 = 0.f, e13 = 0.f, e14 = 0.f, e15 = 0.f;
        d.v1 = u32x4{};
#define QK0(S, K, Q) asm volatile("v_mfma_f32_32x32x16_bf16 %0, %1, %2, 0" : "=&v"(S) : "v"(K), "v"(Q))
#define QK1(S, K, Q) asm volatile("v_mfma_f32_32x32x16_bf16 %0, %1, %2, %0" : "+v"(S) : "v"(K), "v"(Q))
        asm volatile("s_nop 1" : "+v"(q[0][1]), "+v"(q[1][1]), "+v"(q[2][1]), "+v"(q[3][1]));
        QK0(sa, a.k0, q[0][0]); QK1(sa, a.k1, q[0][1]);
        asm volatile("s_nop 11" : "+v"(sa));
#define EX(S, i) S[i] = __builtin_amdgcn_exp2f(S[i])
#define STEP(SC, SN, KN0, KN1, QN0, QN1, OP, VP1, OC, VC0) do { \
    QK0(SN, KN0, QN0); SBAR(); \
    pb[2] = pk(e12, e13); SBAR(); EX(SC, 0); SBAR(); pb[3] = pk(e14, e15); SBAR(); EX(SC, 1); EX(SC, 2); EX(SC, 3); SBAR(); \
    OP = mfma(VP1, pb, OP); SBAR(); \
    EX(SC, 4); EX(SC, 5); SBAR(); pa[0] = pk(SC[0], SC[1]); SBAR(); EX(SC, 6); SBAR(); pa[1] = pk(SC[2], SC[3]); SBAR(); EX(SC, 7); SBAR(); \
    QK1(SN, KN1, QN1); SBAR(); \
    EX(SC, 8); SBAR(); pa[2] = pk(SC[4], SC[5]); SBAR(); EX(SC, 9); SBAR(); pa[3] = pk(SC[6], SC[7]); SBAR(); EX(SC, 10); EX(SC, 11); SBAR(); \
    OC = mfma(VC0, pa, OC); SBAR(); \
    e12 = __builtin_amdgcn_exp2f(SC[12]); SBAR(); pb[0] = pk(SC[8], SC[9]); SBAR(); e13 = __builtin_amdgcn_exp2f(SC[13]); SBAR(); pb[1] = pk(SC[10], SC[11]); SBAR(); \
    e14 = __builtin_amdgcn_exp2f(SC[14]); e15 = __builtin_amdgcn_exp2f(SC[15]); SBAR(); } while (0)
#define TILE(J, CUR, PREV, NXT, NW, SEAM, MORE) do { \
    STEP(sa, sb, CUR.k0, CUR.k1, q[1][0], q[1][1], o3, PREV.v1, o0, CUR.v0); \
    if (J + 3 <= 15) LDKV(PREV, J + 3); \
    SEAM; SBAR(); \
    STEP(sb, sa, CUR.k0, CUR.k1, q[2][0], q[2][1], o0, CUR.v1, o1, CUR.v0); \
    MORE(0, 3); SBAR(); \
    STEP(sa, sb, CUR.k0, CUR.k1, q[3][0], q[3][1], o1, CUR.v1, o2, CUR.v0); \
    MORE(3, 6); \
    if (J < 15) landed<NW>(NXT.k0, NXT.k1, NXT.v0, NXT.v1); \
    SBAR(); \
    STEP(sb, sa, NXT.k0, NXT.k1, q[0][0], q[0][1], o2, CUR.v1, o3, CUR.v0); \
    MORE(6, 8); SBAR(); } while (0)
        const rsrc_t rwb = make_rsrc(ws + WS_WO + (size_t)(14 + (live ? nt : 0)) * 32768, 32768);
        const rsrc_t rst = make_rsrc(ws + WS_ST + (size_t)h * 4096, 4096);
        u32x4 pl[2], sf[4];
        u32x2 pp;
        const unsigned pp_word = lane < 16 ? WS_FP + (h * 4 + (rb >> 2)) * 128 + lane * 8 : WS_FWO + (((lane >> 5) & 1) * 2 + (live ? nt / 7 : 1)) * 128 + (lane & 15) * 8;
        const unsigned ps_word = WS_FS + h * 512 + lane * 8;
        auto slice_load = [&]() {
            const rsrc_t rp = make_rsrc(ws + WS_KVP + (size_t)h * 16 * 8192 + rb * 512 + wid * 128, 15 * 8192 + 128);
            ld128_async_dev(pl[0], rp, (lane >> 3) * 8192 + (lane & 7) * 16, 0); ld128_async_dev(pl[1], rp, (lane >> 3) * 8192 + (lane & 7) * 16, 8 * 8192);
        };
        auto slice_sum = [&]() {
            asm volatile("s_waitcnt vmcnt(4)" : "+v"(pl[0]), "+v"(pl[1]) :: "memory");
            float u[4];
#pragma unroll
            for (int e = 0; e < 4; ++e) {
                u[e] = both_sum(__uint_as_float(pl[0][e]) + __uint_as_float(pl[1][e]));
                auto r = __builtin_amdgcn_permlane16_swap(__float_as_uint(u[e]), __float_as_uint(u[e]), false, false);
                u[e] = __uint_as_float(r[0]) + __uint_as_float(r[1]);
                u[e] += __uint_as_float(__builtin_amdgcn_update_dpp(0u, __float_as_uint(u[e]), 0x128, 0xf, 0xf, false));
            }
            const u32x2 v = {pk(u[0], u[1]), pk(u[2], u[3])};
            const unsigned at = (rb >> 2) * 1024 + ((rb & 1) * 32 + wid * 8 + lane) * 16 + ((rb >> 1) & 1) * 8;
            asm volatile("buffer_store_dwordx2 %0, %1, %2, 0 offen sc1" ::"v"(v), "v"(lane < 8 ? at : OOB), "s"(rst) : "memory");
        };
        auto state_load = [&]() {
#pragma unroll
            for (int i = 0; i < 4; ++i) ld128_async_dev(sf[i], rst, voff + i * 1024, 0);
        };
        auto wo_load = [&](int n, int first, int last) {
#pragma unroll
            for (int i = 0; i < 8; ++i) if (i >= first && i < last) ld128_async_acc(wob_f[8 * n + i], rwb, voff + (i & 3) * 1024, (2 * n + (i >> 2)) * 4096);
        };
        auto wa_load = [&](int n, int first, int last) {
#pragma unroll
            for (int i = 0; i < 8; ++i) if (i >= first && i < last) ld128_async_dev(woa_f[8 * n + i], rwa_o, voff + (i & 3) * 1024, (2 * n + (i >> 2)) * 4096);
        };
        auto finish_b = [&]() {
            landed<4>(sf[0], sf[1], sf[2], sf[3]);
            if (r32 == 29) {
                const unsigned tail = hi ? 0u : 0x3F803F80u;
                sf[0] = u32x4{0x3F803F80u, 0x3F803F80u, 0x3F803F80u, 0x3F803F80u}; sf[2] = sf[0];
                sf[1] = u32x4{0x3F803F80u, 0x3F803F80u, tail, tail}; sf[3] = sf[1];
            }
            asm volatile("" : "+v"(sf[0]), "+v"(sf[1]), "+v"(sf[2]), "+v"(sf[3]));
            f32x16 np, nn;
            mfma_v0(np, sf[0], eq[0]); mfma_v(np, sf[1], eq[1]); mfma_v0(nn, sf[2], eq[2]); mfma_v(nn, sf[3], eq[3]);
            asm volatile("s_nop 11" : "+v"(np), "+v"(nn));
            const float rp_ = __builtin_amdgcn_rcpf(np[13]), rn_ = __builtin_amdgcn_rcpf(nn[13]);
            const float inv = __builtin_amdgcn_rcpf(fmaf(np[12], rp_, fmaf(nn[12], rn_, 1e-6f)));
            const float ap = upper_val(rp_ * inv), an = upper_val(rn_ * inv);
            f32x16 o;
#pragma unroll
            for (int i = 0; i < 16; ++i) o[i] = fmaf(np[i], ap, nn[i] * an);
            u32x4 g0 = pack_lo(o), g1 = pack_hi(o);
            if (hi) { g1[2] = 0; g1[3] = 0; }
            const rsrc_t ro = make_rsrc(ws + WS_OB + (size_t)(tile * 16 + h) * 2048, 2048);
            st128_async(g0, ro, voff, 0); st128_async(g1, ro, voff + 1024, 0);
        };
#define NONE(x, y) (void)0
#define WB0(x, y) wo_load(0, x, y)
#define WB1(x, y) wo_load(1, x, y)
#define WB2(x, y) wo_load(2, x, y)
#define WB3(x, y) wo_load(3, x, y)
#define WA0(x, y) wa_load(0, x, y)
#define WA1(x, y) wa_load(1, x, y)
        TILE(0, a, d, b, 10, (void)0, NONE);
        TILE(1, b, a, c, 11, (wait_vm<8>(), publish(ws, WS_FP + ((h * 4 + wid) * 16 + rb) * 8, nonce)), NONE);
        TSTAMP(19); TILE(2, c, b, d, 9, (void)0, NONE);
        TILE(3, d, c, a, 9, (void)0, NONE);
        TILE(4, a, d, b, 9, peek(pp, ws, pp_word), NONE);
        TSTAMP(20); TILE(5, b, a, c, 11, (seen<4>(pp, ws, pp_word, nonce), slice_load()), NONE);
        TSTAMP(21); TILE(6, c, b, d, 18, slice_sum(), WB0);
        TSTAMP(22); TILE(7, d, c, a, 25, (void)0, WB1);
        TILE(8, a, d, b, 32, (wait_vm<24>(), publish(ws, WS_FS + (h * 64 + tile) * 8, nonce)), WB2);
        TILE(9, b, a, c, 31, (void)0, WB3);
        TILE(10, c, b, d, 26, peek(pp, ws, ps_word), NONE);
        TSTAMP(23); TILE(11, d, c, a, 21, (seen<4>(pp, ws, ps_word, nonce), state_load()), NONE);
        TSTAMP(24); TILE(12, a, d, b, 15, finish_b(), NONE);
        TSTAMP(25); TILE(13, b, a, c, 16, (void)0, WA0);
        TILE(14, c, b, d, 17, (wait_vm<8>(), publish(ws, WS_FOB + (tile * 16 + h) * 8, nonce)), WA1);
        TSTAMP(26); TILE(15, d, c, a, 0, (void)0, NONE);
        pb[2] = pk(e12, e13); pb[3] = pk(e14, e15);
        o3 = mfma(d.v1, pb, o3);
#undef TILE
#undef STEP
#undef EX
#undef QK0
#undef QK1
#undef NONE
#undef WB0
#undef WB1
#undef WB2
#undef WB3
#undef WA0
#undef WA1
        STAMP(11);
        combine_post(wid, lane, o1, o2, o3);
        u32x2 pob;
        const unsigned pob_word = WS_FOB + (qt * 16 + wid * 4 + (lane & 3)) * 8;
        peek(pob, ws, pob_word);
        lds_bar();
        const bool bad = combine_take(wid, lane, o0, f0, f1);
        st128_async(f0, roa, voff, 0); st128_async(f1, roa, voff + 1024, 0);
        STAMP(12);
        if (__any(bad) && lane == 0) *(LAS unsigned*)(size_t)L_FLAG = 1;
        seen<2>(pob, ws, pob_word, nonce);
        {
            const rsrc_t ro = make_rsrc(ws + WS_OB + (size_t)(qt * 16 + wid * 4) * 2048, 8192);
#pragma unroll
            for (int i = 0; i < 8; ++i) dma16_dev(ro, L_OIMG + (wid * 8 + i) * 1024, voff, i * 1024);
        }
        wait_bar<8>();
        if (__builtin_amdgcn_readfirstlane(*(const LAS unsigned*)(size_t)L_FLAG)) {
            float ref[4];
#pragma unroll
            for (int t = 0; t < 4; ++t) { if (hi) { q[t][1][2] = 0; q[t][1][3] = 0; } ref[t] = -__builtin_inff(); }
#pragma unroll 1
            for (int j = 0; j < 16; ++j) {
                const u32x4 k0 = ld128_dev(rk, voff, j * 4096), k1 = ld128_dev(rk, voff + 1024, j * 4096);
#pragma unroll
                for (int t = 0; t < 4; ++t) { f32x16 s = mfma(k0, q[t][0], zero); s = mfma(k1, q[t][1], s); ref[t] = fmaxf(ref[t], max16(s)); }
            }
            ref_post(wid, lane, ref);
            lds_bar();
            ref_take(wid, lane, q, 0.f);
            lds_bar();
            f32x16 x[4] = {};
#pragma unroll 1
            for (int j = 0; j < 16; ++j) {
                const u32x4 k0 = ld128_dev(rk, voff, j * 4096), k1 = ld128_dev(rk, voff + 1024, j * 4096);
                const u32x4 v0 = ld128_dev(rk, voff + 2048, j * 4096), v1 = ld128_dev(rk, voff + 3072, j * 4096);
#pragma unroll
                for (int t = 0; t < 4; ++t) {
                    f32x16 s = mfma(k0, q[t][0], zero); s = mfma(k1, q[t][1], s);
#pragma unroll
                    for (int i = 0; i < 16; ++i) s[i] = __builtin_amdgcn_exp2f(s[i]);
                    x[t] = mfma(v0, pack_lo(s), x[t]); x[t] = mfma(v1, pack_hi(s), x[t]);
                }
            }
            combine_post(wid, lane, x[1], x[2], x[3]);
            lds_bar();
            combine_take(wid, lane, x[0], f0, f1);
            st128_async(f0, roa, voff, 0); st128_async(f1, roa, voff + 1024, 0);
            wait_vm<0>();
        }
        publish(ws, WS_FOA + (tile * 16 + h) * 8, nonce);
        STAMP(13);
    }
#undef LDKV
    {
        const int lane = lane_id();
        const unsigned voff = lane * 16;
#pragma unroll
        for (int i = 0; i < 32; i += 8)
            asm volatile("" : "+a"(wob_f[i]), "+a"(wob_f[i + 1]), "+a"(wob_f[i + 2]), "+a"(wob_f[i + 3]), "+a"(wob_f[i + 4]), "+a"(wob_f[i + 5]), "+a"(wob_f[i + 6]), "+a"(wob_f[i + 7]));
        f32x16 r = {};
        asm volatile("s_nop 1" : "+v"(r));
        u32x4 of[32];
        wait_vm<1>();
        barrier();
#pragma unroll
        for (int i = 0; i < 6; ++i) of[i] = lds128(L_OIMG + i * 1024 + voff);
#pragma unroll
        for (int i = 0; i < 32; ++i) {
            if (i + 6 < 32) of[i + 6] = lds128(L_OIMG + (i + 6) * 1024 + voff);
            SBAR();
            asm volatile("v_mfma_f32_32x32x16_bf16 %0, %1, %2, %0" : "+v"(r) : "a"(wob_f[i]), "v"(of[i]));
            if (!(i & 1)) ld128_async_dev(woa_f[16 + i / 2], rwa_o, voff + ((i / 2) & 3) * 1024, (4 + i / 8) * 4096);
            SBAR();
        }
        settle(r);
        stage_out(L_OUT, wid, lane, r);
        lds_bar();
        STAMP(14);
        u32x2 poa;
        const unsigned poa_word = WS_FOA + (qt * 16 + wid * 4 + (lane & 3)) * 8;
        peek(poa, ws, poa_word);
        seen<0>(poa, ws, poa_word, nonce);
        STAMP(15);
        {
            const rsrc_t ro = make_rsrc(ws + WS_OA + (size_t)(qt * 16 + wid * 4) * 2048, 8192);
#pragma unroll
            for (int i = 0; i < 8; ++i) dma16_dev(ro, L_OIMG + (wid * 8 + i) * 1024, voff, i * 1024);
        }
        flush_out(L_OUT, wid, lane, h, qt, 1, out);
        wait_bar<2>();
        if (blk == 0 && wid == 0) publish(ws, WS_EPOCH, nonce);
#pragma unroll
        for (int i = 0; i < 32; i += 8)
            asm volatile("" : "+v"(woa_f[i]), "+v"(woa_f[i + 1]), "+v"(woa_f[i + 2]), "+v"(woa_f[i + 3]), "+v"(woa_f[i + 4]), "+v"(woa_f[i + 5]), "+v"(woa_f[i + 6]), "+v"(woa_f[i + 7]));
        f32x16 s;
#pragma unroll
        for (int i = 0; i < 6; ++i) of[i] = lds128(L_OIMG + i * 1024 + voff);
#pragma unroll
        for (int i = 0; i < 32; ++i) {
            if (i + 6 < 32) of[i + 6] = lds128(L_OIMG + (i + 6) * 1024 + voff);
            SBAR();
            if (i == 0) mfma_v0(s, woa_f[0], of[0]); else mfma_v(s, woa_f[i], of[i]);
            SBAR();
        }
        settle(s);
        STAMP(16);
        stage_out(L_OUT + 16384, wid, lane, s);
        lds_bar();
        asm volatile(".Lfused_code_end:");
        flush_out(L_OUT + 16384, wid, lane, h, qt, 0, out);
    }
}

}

extern "C" void kernel_launch(void* const* d_in, const int* in_sizes, int n_in, void* d_out, int out_size, void* d_ws, size_t ws_size, hipStream_t stream) {
    (void)hipFuncSetAttribute((const void*)fused, hipFuncAttributeMaxDynamicSharedMemorySize, LDS_BYTES);
    fused<<<256, 256, LDS_BYTES, stream>>>((const u16*)d_in[0], (const u16*)d_in[1], (const u16*)d_in[2], (const u16*)d_in[3], (const u16*)d_in[4], (const u16*)d_in[5],
                                           (const u16*)d_in[6], (u16*)d_out, (char*)d_ws);
}
```

```cpp
#include <hip/hip_runtime.h>
#include <stdint.h>

namespace {

typedef unsigned short u16;
typedef unsigned long long u64;
typedef __attribute__((ext_vector_type(16))) float f32x16;
typedef __attribute__((ext_vector_type(4))) float f32x4;
typedef __attribute__((ext_vector_type(2))) float f32x2;
typedef __attribute__((ext_vector_type(8))) __bf16 bf16x8;
typedef __attribute__((ext_vector_type(2))) __bf16 bf16x2;
typedef __attribute__((ext_vector_type(4))) unsigned u32x4;
typedef __attribute__((ext_vector_type(2))) unsigned u32x2;
typedef __attribute__((ext_vector_type(4))) short s16x4;
typedef __attribute__((ext_vector_type(8))) unsigned long long u64x8;
typedef __amdgpu_buffer_rsrc_t rsrc_t;
#define LAS __attribute__((address_space(3)))
#define GAS __attribute__((address_space(1)))
#define DI __device__ __forceinline__
#define SBAR() __builtin_amdgcn_sched_barrier(0)

constexpr int SEQ = 2048, DM = 448;
constexpr unsigned XROW = DM * 2;
constexpr unsigned WROW = 3 * DM * 2;
constexpr unsigned X_BYTES = SEQ * XROW, W_BYTES = DM * WROW, WO_BYTES = DM * XROW;
constexpr unsigned WS_EPOCH = 0;
constexpr unsigned WS_FK = 4096;
constexpr unsigned WS_FP = WS_FK + 8192;
constexpr unsigned WS_FOA = WS_FP + 8192;
constexpr unsigned WS_FOB = WS_FOA + 8192;
constexpr unsigned WS_FW = WS_FOB + 8192;
constexpr unsigned WS_FWO = WS_FW + 2048;
constexpr unsigned WS_FS = WS_FWO + 2048;
constexpr unsigned WS_VOID = WS_FS + 8192;
constexpr unsigned WS_KVA = 65536;
constexpr unsigned WS_OA = WS_KVA + (4u << 20);
constexpr unsigned WS_OB = WS_OA + (2u << 20);
constexpr unsigned WS_KVP = WS_OB + (2u << 20);
constexpr unsigned WS_WB = WS_KVP + (2u << 20);
constexpr unsigned WS_WO = WS_WB + (2u << 20);
constexpr unsigned WS_ST = WS_WO + (1u << 20);
constexpr unsigned WS_END = WS_ST + 65536;
constexpr int LDS_BYTES = 160 * 1024;
constexpr unsigned L_WKV = 0, L_WQ = 57344, L_RET = 57344;
constexpr unsigned L_X = 86016;
constexpr unsigned l_kvb(int ch) { return ch == 0 ? 151552 : (ch - 1) * 8192; }
constexpr unsigned L_QX = 49152, L_REF = 40960, L_WPHI = 159744, L_FLAG = 161792;
constexpr unsigned L_PHI = 86016, L_RED = 0;
constexpr unsigned L_COMB = 0, L_OIMG = 65536, L_OIMG2 = 0, L_OUT = 98304;
constexpr float LOG2E = 1.4426950408889634f;
constexpr float QSCALE = 0.18898223650461362f * LOG2E;
#define REF_OFF 100.f
constexpr float REF_OFFSET = REF_OFF;
constexpr float L_LIMIT = 18446744073709551616.f;
constexpr int SC1 = 16;
constexpr unsigned OOB = 0x80000000u;
#define SPINLOG 16
constexpr unsigned SPIN_CAP = 1u << SPINLOG;
#define PEEK_AT 4
#define SEEN_AT 18
#define PSLEEP 4

DI f32x16 mfma(u32x4 a, u32x4 b, f32x16 c) {
    return __builtin_amdgcn_mfma_f32_32x32x16_bf16(__builtin_bit_cast(bf16x8, a), __builtin_bit_cast(bf16x8, b), c, 0, 0, 0);
}
DI unsigned pk(float lo, float hi) { f32x2 v = {lo, hi}; return __builtin_bit_cast(unsigned, __builtin_convertvector(v, bf16x2)); }
DI u32x4 pack_lo(const f32x16& x) { return u32x4{pk(x[0], x[1]), pk(x[2], x[3]), pk(x[4], x[5]), pk(x[6], x[7])}; }
DI u32x4 pack_hi(const f32x16& x) { return u32x4{pk(x[8], x[9]), pk(x[10], x[11]), pk(x[12], x[13]), pk(x[14], x[15])}; }
DI u32x4 quad(const f32x16& x, int g) { return u32x4{__float_as_uint(x[4 * g]), __float_as_uint(x[4 * g + 1]), __float_as_uint(x[4 * g + 2]), __float_as_uint(x[4 * g + 3])}; }
DI rsrc_t make_rsrc(const void* p, unsigned bytes) { return __builtin_amdgcn_make_buffer_rsrc((void*)p, 0, bytes, 0x00020000); }
DI u32x4 ld128(rsrc_t rs, unsigned voff, unsigned soff) { return __builtin_amdgcn_raw_buffer_load_b128(rs, voff, soff, 0); }
DI u32x4 ld128_dev(rsrc_t rs, unsigned voff, unsigned soff) { return __builtin_amdgcn_raw_buffer_load_b128(rs, voff, soff, SC1); }
DI void st128_dev(u32x4 v, rsrc_t rs, unsigned voff, unsigned soff) { __builtin_amdgcn_raw_buffer_store_b128(v, rs, voff, soff, SC1); }
DI void dma16(rsrc_t rs, unsigned lds_addr, unsigned voff, unsigned soff) {
    asm volatile("s_mov_b32 m0, %0\n\ts_nop 0\n\tbuffer_load_dwordx4 %1, %2, %3 offen lds" ::"s"(lds_addr), "v"(voff), "s"(rs), "s"(soff) : "memory");
}
DI void ld128_async(u32x4& d, rsrc_t rs, unsigned voff, unsigned soff) {
    asm volatile("buffer_load_dwordx4 %0, %1, %2, %3 offen" : "=v"(d) : "v"(voff), "s"(rs), "s"(soff) : "memory");
}
DI void dma16_dev(rsrc_t rs, unsigned lds_addr, unsigned voff, unsigned soff) {
    asm volatile("s_mov_b32 m0, %0\n\ts_nop 0\n\tbuffer_load_dwordx4 %1, %2, %3 offen sc1 lds" ::"s"(lds_addr), "v"(voff), "s"(rs), "s"(soff) : "memory");
}
template <int N> DI void wait_vm() { asm volatile("s_waitcnt vmcnt(%0)" ::"n"(N) : "memory"); }
template <int N> DI void wait_bar() { asm volatile("s_waitcnt vmcnt(%0) lgkmcnt(0)\n\ts_barrier" ::"n"(N) : "memory"); }
DI void lds_bar() { asm volatile("s_waitcnt lgkmcnt(0)\n\ts_barrier" ::: "memory"); }
DI u32x4 lds128(unsigned a) { return *(const LAS u32x4*)(size_t)a; }
DI void sts128(unsigned a, u32x4 v) { *(LAS u32x4*)(size_t)a = v; }
DI void sts64(unsigned a, u32x2 v) { *(LAS u32x2*)(size_t)a = v; }
DI u32x2 tr64(unsigned a) { return __builtin_bit_cast(u32x2, __builtin_amdgcn_ds_read_tr16_b64_v4i16((LAS s16x4*)(size_t)a)); }
DI u32x4 tr_frag(unsigned a0, unsigned a1) { u32x2 lo = tr64(a0), hi = tr64(a1); return u32x4{lo[0], lo[1], hi[0], hi[1]}; }
DI float max3(float a, float b, float c) { float r; asm("v_max3_f32 %0, %1, %2, %3" : "=v"(r) : "v"(a), "v"(b), "v"(c)); return r; }
DI float min3(float a, float b, float c) { float r; asm("v_min3_f32 %0, %1, %2, %3" : "=v"(r) : "v"(a), "v"(b), "v"(c)); return r; }
DI float both_max(float v) { auto r = __builtin_amdgcn_permlane32_swap(__float_as_uint(v), __float_as_uint(v), false, false); return max3(__uint_as_float(r[0]), __uint_as_float(r[1]), __uint_as_float(r[1])); }
DI float both_min(float v) { auto r = __builtin_amdgcn_permlane32_swap(__float_as_uint(v), __float_as_uint(v), false, false); return min3(__uint_as_float(r[0]), __uint_as_float(r[1]), __uint_as_float(r[1])); }
DI float both_sum(float v) { auto r = __builtin_amdgcn_permlane32_swap(__float_as_uint(v), __float_as_uint(v), false, false); return __uint_as_float(r[0]) + __uint_as_float(r[1]); }
DI float upper_val(float v) { auto r = __builtin_amdgcn_permlane32_swap(__float_as_uint(v), __float_as_uint(v), false, false); return __uint_as_float(r[1]); }
DI float max16(const f32x16& s) {
    const float a = max3(s[0], s[1], s[2]), b = max3(s[3], s[4], s[5]), c = max3(s[6], s[7], s[8]), d = max3(s[9], s[10], s[11]), e = max3(s[12], s[13], s[14]);
    return max3(max3(a, b, c), max3(d, e, s[15]), a);
}
DI float min16(const f32x16& s) {
    const float a = min3(s[0], s[1], s[2]), b = min3(s[3], s[4], s[5]), c = min3(s[6], s[7], s[8]), d = min3(s[9], s[10], s[11]), e = min3(s[12], s[13], s[14]);
    return min3(min3(a, b, c), min3(d, e, s[15]), a);
}

template <int OFF = 0> DI void ld128_async_dev(u32x4& d, rsrc_t rs, unsigned voff, unsigned soff) {
    asm volatile("buffer_load_dwordx4 %0, %1, %2, %3 offen offset:%4 sc1" : "=v"(d) : "v"(voff), "s"(rs), "s"(soff), "n"(OFF) : "memory");
}
template <int OFF = 0> DI void ld128_async_acc(u32x4& d, rsrc_t rs, unsigned voff, unsigned soff) {
    asm volatile("buffer_load_dwordx4 %0, %1, %2, %3 offen offset:%4 sc1" : "=a"(d) : "v"(voff), "s"(rs), "s"(soff), "n"(OFF) : "memory");
}
template <bool ACC> DI void ld_frag(u32x4& d, rsrc_t rs, unsigned voff, unsigned base, int i) {
    const unsigned s = base + (i >> 2) * 4096;
    if (ACC) { if ((i & 3) == 0) ld128_async_acc(d, rs, voff, s); if ((i & 3) == 1) ld128_async_acc<1024>(d, rs, voff, s); if ((i & 3) == 2) ld128_async_acc<2048>(d, rs, voff, s); if ((i & 3) == 3) ld128_async_acc<3072>(d, rs, voff, s); }
    else     { if ((i & 3) == 0) ld128_async_dev(d, rs, voff, s); if ((i & 3) == 1) ld128_async_dev<1024>(d, rs, voff, s); if ((i & 3) == 2) ld128_async_dev<2048>(d, rs, voff, s); if ((i & 3) == 3) ld128_async_dev<3072>(d, rs, voff, s); }
}
template <int OFF = 0> DI void st128_async(u32x4 v, rsrc_t rs, unsigned voff, unsigned soff) {
    asm volatile("buffer_store_dwordx4 %0, %1, %2, %3 offen offset:%4 sc1\n\ts_nop 1" ::"v"(v), "v"(voff), "s"(rs), "s"(soff), "n"(OFF) : "memory");
}
template <int N> DI void landed(u32x4& a, u32x4& b, u32x4& c, u32x4& d) { asm volatile("s_waitcnt vmcnt(%4)" : "+v"(a), "+v"(b), "+v"(c), "+v"(d) : "n"(N) : "memory"); }
template <int N> DI void landed8(u32x4 (&r)[8]) {
    asm volatile("s_waitcnt vmcnt(%8)" : "+v"(r[0]), "+v"(r[1]), "+v"(r[2]), "+v"(r[3]), "+v"(r[4]), "+v"(r[5]), "+v"(r[6]), "+v"(r[7]) : "n"(N) : "memory");
}
DI void mfma_v(f32x16& acc, u32x4 a, u32x4 b) { asm("v_mfma_f32_32x32x16_bf16 %0, %1, %2, %0" : "+v"(acc) : "v"(a), "v"(b)); }
DI void mfma_v0(f32x16& acc, u32x4 a, u32x4 b) { asm("s_nop 1\n\tv_mfma_f32_32x32x16_bf16 %0, %1, %2, 0" : "=&v"(acc) : "v"(a), "v"(b)); }
DI void settle(f32x16& x) { asm volatile("s_nop 11" : "+v"(x)); }
DI void barrier() { asm volatile("s_barrier" ::: "memory"); }
DI int lane_id() { int l; asm volatile("v_mbcnt_lo_u32_b32 %0, -1, 0\n\tv_mbcnt_hi_u32_b32 %0, -1, %0" : "=v"(l)); return l; }

#define STAMP(id) do {} while (0)
#define TSTAMP(id) do {} while (0)

DI void epoch_request(u64& v, const char* p) { asm volatile("s_load_dwordx2 %0, %1, 0x0" : "=s"(v) : "s"(p) : "memory"); }
DI void publish(char* ws, unsigned word, u64 nonce) {
    asm volatile("s_mov_b64 exec, 1\n\tglobal_store_dwordx2 %0, %1, %2 sc1\n\ts_mov_b64 exec, -1" ::"v"(word), "v"(nonce), "s"(ws) : "memory");
}
DI void peek(u32x2& v, const char* ws, unsigned word) { asm volatile("global_load_dwordx2 %0, %1, %2 sc1" : "=v"(v) : "v"(word), "s"(ws) : "memory"); }
DI void peek(u32x4& v, const char* ws, unsigned word) { asm volatile("global_load_dwordx4 %0, %1, %2 sc1" : "=v"(v) : "v"(word), "s"(ws) : "memory"); }
DI bool differs(const u32x2& v, u64 n) { return __builtin_amdgcn_ballot_w64(__builtin_bit_cast(u64, v) != n) != 0; }
DI bool differs(const u32x4& v, u64 n) {
    return (__builtin_amdgcn_ballot_w64(__builtin_bit_cast(u64, u32x2{v[0], v[1]}) != n) | __builtin_amdgcn_ballot_w64(__builtin_bit_cast(u64, u32x2{v[2], v[3]}) != n)) != 0;
}
template <int N, class V> DI void seen(V& v, const char* ws, unsigned word, u64 nonce) {
    asm volatile("s_waitcnt vmcnt(%1)" : "+v"(v) : "n"(N) : "memory");
    if (__builtin_expect(differs(v, nonce), 0)) {
        unsigned spins = 0;
#pragma unroll 1
        do {
            __builtin_amdgcn_s_sleep(PSLEEP);
            peek(v, ws, word);
            asm volatile("s_waitcnt vmcnt(0)" : "+v"(v) :: "memory");
        } while (differs(v, nonce) && ++spins < SPIN_CAP);
    }
}

DI void mfma_pair(int isv, u32x4 xf, u32x4 w0, u32x4 w1, f32x16& acc0, f32x16& acc1) {
    asm("s_cmp_lg_u32 %5, 0\n\ts_cbranch_scc1 1f\n\t"
        "v_mfma_f32_32x32x16_bf16 %0, %3, %2, %0\n\tv_mfma_f32_32x32x16_bf16 %1, %4, %2, %1\n\ts_branch 2f\n"
        "1:\n\tv_mfma_f32_32x32x16_bf16 %0, %2, %3, %0\n\tv_mfma_f32_32x32x16_bf16 %1, %2, %4, %1\n2:"
        : "+v"(acc0), "+v"(acc1) : "v"(xf), "v"(w0), "v"(w1), "s"(isv) : "scc");
}
DI void pin(u32x4& a, u32x4& b, u32x4& c, u32x4& d) { asm volatile("" : "+v"(a), "+v"(b), "+v"(c), "+v"(d) :: "memory"); }

DI void set_reference(u32x4& q1, float nm, int hi) {
    const unsigned a = __float_as_uint(nm) & 0xFFFF0000u;
    const float r1 = nm - __uint_as_float(a);
    const unsigned b = __float_as_uint(r1) & 0xFFFF0000u;
    const float r2 = r1 - __uint_as_float(b);
    q1[2] = hi ? (a >> 16) | b : q1[2]; q1[3] = hi ? __float_as_uint(r2) >> 16 : q1[3];
}

struct KV { u32x4 k0, k1, v0, v1; };

DI void ref_post(int wid, int lane, const float (&m)[4]) {
#pragma unroll
    for (int t = 0; t < 4; ++t) *(LAS float*)(size_t)(L_REF + ((wid + t) & 3) * 512 + (lane & 31) * 16 + wid * 4) = both_max(m[t]);
}
DI void ref_take(int wid, int lane, u32x4 (&q)[4][2], float offset) {
#pragma unroll
    for (int t = 0; t < 4; ++t) {
        const f32x4 m = __builtin_bit_cast(f32x4, lds128(L_REF + ((wid + t) & 3) * 512 + (lane & 31) * 16));
        set_reference(q[t][1], -(max3(max3(m[0], m[1], m[2]), m[3], m[3]) + offset), lane >> 5);
    }
}

DI void combine_post(int wid, int lane, const f32x16& o1, const f32x16& o2, const f32x16& o3) {
    const f32x16* ot[3] = {&o1, &o2, &o3};
#pragma unroll
    for (int t = 0; t < 3; ++t) {
        const unsigned at = L_COMB + (((wid + t + 1) & 3) * 3 + t) * 4096 + lane * 16;
#pragma unroll
        for (int g = 0; g < 4; ++g) sts128(at + g * 1024, quad(*ot[t], g));
    }
}
DI bool combine_take(int wid, int lane, f32x16 o, u32x4& f0, u32x4& f1) {
#pragma unroll
    for (int v = 0; v < 12; ++v) {
        const u32x4 p = lds128(L_COMB + wid * 12288 + v * 1024 + lane * 16);
#pragma unroll
        for (int i = 0; i < 4; ++i) o[4 * (v & 3) + i] += __uint_as_float(p[i]);
    }
    const float l = upper_val(o[12]);
    o *= __builtin_amdgcn_rcpf(l);
    float chk = 0.f;
#pragma unroll
    for (int i = 0; i < 16; ++i) chk = fmaf(o[i], 0.f, chk);
    f0 = pack_lo(o); f1 = pack_hi(o);
    if (lane >> 5) { f1[2] = 0; f1[3] = 0; }
    return !(l < L_LIMIT) || !(chk == 0.f);
}

DI void stage_out(unsigned img, int wid, int lane, const f32x16& r) {
    const u32x4 lo = pack_lo(r), up = pack_hi(r);
    const unsigned wr = img + (lane & 31) * 272 + wid * 64 + (lane >> 5) * 8;
    sts64(wr, u32x2{lo[0], lo[1]}); sts64(wr + 16, u32x2{lo[2], lo[3]}); sts64(wr + 32, u32x2{up[0], up[1]}); sts64(wr + 48, u32x2{up[2], up[3]});
}
DI void flush_out(unsigned img, int wid, int lane, int h, int qt, int br, u16* out) {
    const unsigned width = h >= 12 ? 128 : 256, colb = (lane & 15) * 16;
    const rsrc_t rd = make_rsrc((char*)out + (size_t)qt * 32 * (2 * XROW) + br * XROW + (h >> 2) * 256, 31 * 2 * XROW + width);
#pragma unroll
    for (int i = 0; i < 2; ++i) {
        const unsigned row = wid * 8 + i * 4 + (lane >> 4);
        st128_async(lds128(img + row * 272 + colb), rd, colb < width ? row * (2 * XROW) + colb : OOB, 0);
    }
}

__global__ __launch_bounds__(256, 1) void fused(const u16*, const u16*, const u16*, const u16*, const u16*, const u16*, const u16*, u16*, char*) {
    u64x8 args;
    u64 arg_ws;
    const auto arg_base = __builtin_amdgcn_kernarg_segment_ptr();
    asm volatile("s_load_dwordx16 %0, %2, 0x0\n\ts_load_dwordx2 %1, %2, 0x40" : "=&s"(args), "=&s"(arg_ws) : "s"(arg_base) : "memory");
    SBAR();
    STAMP(0);
    const int wid = __builtin_amdgcn_readfirstlane(threadIdx.x >> 6);
    const int blk = blockIdx.x, xcd = blk & 7, bi = blk >> 3;
    const int h = (xcd & 1) * 8 + (bi & 7), rb = (xcd >> 1) * 4 + (bi >> 3), tile = rb * 4 + wid;
    const int rot = ((bi >> 3) + (bi & 7)) % 3;
    auto chunk_at = [&](int p) { const int v = p % 3 + rot; return p == 6 ? 6 : p - p % 3 + (v >= 3 ? v - 3 : v); };
    u64 epoch, code_pc;
    unsigned code_probe;
    u64 nonce;
    const f32x16 zero = {};
    u32x4 q[4][2], eq[4];
    KV a, b, c, d;
    char* ws;
    rsrc_t rws;
    const unsigned kv_at = WS_KVA + (h * 64 + wid * 16) * 4096;
#define LDKV(X, j) do { const unsigned s_ = kv_at + (j) * 4096; ld128_async_dev<3072>(X.v1, rws, voff, s_); ld128_async_dev<2048>(X.v0, rws, voff, s_); \
                        ld128_async_dev<1024>(X.k1, rws, voff, s_); ld128_async_dev(X.k0, rws, voff, s_); } while (0)
    {
        const int lane = lane_id(), hi = lane >> 5, r32 = lane & 31, q4 = (lane & 15) >> 2;
        const unsigned voff = lane * 16, col = 32 * ((lane >> 4) & 1) + 8 * (lane & 3);
        const int isv = h & 1, row8 = lane >> 3;
        u32x4 xf[28];
        f32x16 acc0 = {}, acc1 = {};
        u32x4 pw;
        {
            const unsigned piece = (lane & 7) ^ (((row8 >> 1) & 1) << 2);
            unsigned vw = (16 * wid + row8) * WROW + (1 + isv) * XROW + (h >> 1) * 112 + ((piece < 7 ? piece : 6) << 4);
            unsigned vx0 = (tile * 32 + row8) * XROW + (((lane & 7) ^ (lane >> 4)) << 4), vx1 = (tile * 32 + 8 + row8) * XROW + (((lane & 7) ^ (4 + (lane >> 4))) << 4);
            int rot0 = rot;
            asm volatile("s_waitcnt lgkmcnt(0)" : "+s"(args), "+s"(arg_ws), "+v"(vw), "+v"(vx0), "+v"(vx1), "+s"(rot0) : "s"(arg_base) : "memory");
            const rsrc_t rxa = make_rsrc((const void*)args[0], X_BYTES), rwa = make_rsrc((const void*)args[1], W_BYTES);
            auto req_x = [&](rsrc_t rx, int slot, unsigned k, int i) { dma16(rx, L_X + slot * 16384 + wid * 4096 + i * 1024, (i & 1) ? vx1 : vx0, k * 128 + (i >> 1) * 16 * XROW); };
            auto req_w = [&](int ch, int i) { dma16(rwa, L_WKV + ch * 8192 + wid * 2048 + i * 1024, vw, (chunk_at(ch) * 64 + 8 * i) * WROW); };
            auto issue = [&](int ch) {
                req_w(ch, 0); req_w(ch, 1);
#pragma unroll
                for (int i = 0; i < 4; ++i) req_x(rxa, ch & 3, chunk_at(ch), i);
            };
            issue(0);
            SBAR();
            ws = (char*)arg_ws;
            rws = make_rsrc(ws, WS_END);
            const rsrc_t rxb = make_rsrc((const void*)args[3], X_BYTES);
            epoch_request(epoch, ws + WS_EPOCH);
            {
                unsigned code_len;
                asm volatile("s_getpc_b64 %0\n.Lfused_code_begin:\n\ts_mov_b32 %1, .Lfused_code_end-.Lfused_code_begin" : "=s"(code_pc), "=s"(code_len));
                const unsigned at = (bi * 16 + wid * 4 + (threadIdx.x & 3)) * 128;
                asm volatile("global_load_dword %0, %1, %2" : "=v"(code_probe) : "v"(at < code_len ? at : 0), "s"(code_pc) : "memory");
            }
            int ub = blk, uw = wid;
            asm volatile("" : "+s"(ub), "+s"(uw));
            const int ug = ub & 31, ucg = ub >> 5, ubr = (ub >> 5) & 1, uh = (ub >> 1) & 15, uhalf = ub & 1;
            const unsigned mask_o = (unsigned)((191 - ub) >> 31), mask_w = ~mask_o & (unsigned)((ug - 28) >> 31);
            {
                const u64 wo_p = ubr ? args[6] : args[2];
                const rsrc_t rr = make_rsrc((const void*)(mask_o ? wo_p : args[4]), mask_o ? WO_BYTES : W_BYTES);
                const unsigned i0 = 4 * uw + hi;
                const unsigned vw0 = (64 * (ug >> 2) + 8 * (ug & 3) + (i0 & 7) + 32 * (i0 >> 3)) * WROW + ucg * 448 + (r32 < 29 ? r32 : 28) * 16;
                const unsigned vo0 = (28 * uh + 2 * uw + hi) * XROW + uhalf * 448 + (r32 < 28 ? r32 : 27) * 16;
#pragma unroll
                for (int j = 0; j < 4; ++j) {
                    const unsigned mw = j < 2 ? mask_w : 0u, mo = mask_o & (unsigned)((uw + 4 * j - 14) >> 31);
                    dma16(rr, L_RET + ((mw & ((2 * uw + j) * 1024)) | (mo & ((uw + 4 * j) * 1024)) | (~(mw | mo) & (16384 + uw * 1024))),
                          (mw & (vw0 + 2 * j * WROW)) | (mo & (vo0 + 8 * j * XROW)) | (~(mw | mo) & OOB), 0);
                }
            }
            issue(1);
            unsigned rt_at[4], rt_2nd[4], rt_dst[4], rt_v[4];
            {
                const unsigned rd = L_RET + q4 * 512 + col + hi * (2048 + (mask_w & 2048));
                const unsigned mq = (unsigned)((ucg - 2) >> 31);
                const unsigned w_first = (ucg & 1) * 672 + ((mq & (56 + ug)) | (~mq & (2 * ug + (ucg >> 2)))), o_first = (ubr * 14 + uhalf * 7) * 32 + uh * 2;
#pragma unroll
                for (int i = 0; i < 4; ++i) {
                    const int f = uw + 4 * i, s = f & 1, t = f >> 1, hl = 2 * uw + i;
                    const unsigned mw = i < 2 ? mask_w : 0u, mo = mask_o & (unsigned)((f - 14) >> 31);
                    rt_at[i] = rd + ((mw & (hl * 56)) | (mo & (s * 8192 + t * 64)));
                    rt_2nd[i] = rt_at[i] + 4096 - ((mask_w | -(unsigned)(s & hi)) & 2048);
                    rt_dst[i] = (mw & (WS_WB + (w_first + hl * 84) * 1024)) | (mo & (WS_WO + (o_first + t * 32 + s) * 1024));
                    rt_v[i] = voff | (~(mw | mo) & OOB);
                    asm volatile("" : "+v"(rt_at[i]), "+v"(rt_2nd[i]), "+s"(rt_dst[i]), "+v"(rt_v[i]));
                }
            }
            const unsigned m0w = (unsigned)((uw - 1) >> 31);
            unsigned rt_word = (m0w & mask_w & (WS_FW + (((ucg & 1) * 3 + (ucg >> 1)) * 28 + ug) * 8)) | (m0w & mask_o & (WS_FWO + ((ubr * 2 + uhalf) * 16 + uh) * 8))
                             | (~(m0w & (mask_w | mask_o)) & (WS_VOID + (ub * 4 + uw) * 8));
            unsigned w0 = L_WKV + (32 * hi + q4) * 128 + (col ^ ((q4 >> 1) << 6)), w1 = L_WKV + (32 * hi + q4) * 128 + ((col + 56) ^ ((q4 >> 1) << 6));
            unsigned xo[4];
#pragma unroll
            for (int s = 0; s < 4; ++s) xo[s] = L_X + wid * 4096 + r32 * 128 + (((4 * hi + s) ^ ((r32 >> 1) & 7)) << 4);
            const unsigned pw_word = WS_FW + (h >> 3) * 672 + (lane < 42 ? lane : 41) * 16;
            unsigned vwq = (16 * wid + (lane >> 2)) * WROW + h * 56 + (lane & 3) * 16;
            asm volatile("" : "+s"(rt_word), "+v"(w0), "+v"(w1), "+v"(xo[0]), "+v"(xo[1]), "+v"(xo[2]), "+v"(xo[3]), "+v"(vwq), "+v"(acc0), "+v"(acc1));
            issue(2); issue(3);
            asm volatile("s_waitcnt lgkmcnt(0)" : "+s"(epoch) :: "memory");
            nonce = epoch + 1;
            STAMP(1);
            const unsigned wb_at = WS_WB + h * 86016;
            auto req_kvb = [&](int ch, int j) { dma16_dev(rws, l_kvb(ch) + wid * 2048 + j * 1024, voff, wb_at + chunk_at(ch) * 8192 + wid * 2048 + j * 1024); };
            auto req_qb = [&](int ch) { dma16_dev(rws, L_WQ + ch * 4096 + wid * 1024, voff, wb_at + 57344 + chunk_at(ch) * 4096 + wid * 1024); };
            auto req_wq = [&](int j) { dma16(rwa, L_WQ + j * 4096 + wid * 1024, vwq, chunk_at(j) * 64 * WROW); };
            auto xb_slot = [](int ch) { return (ch + 3) & 3; };
            u32x4 wf[2][8];
#pragma unroll
            for (int p = 0; p <= 7; ++p) {
                if (p < 7) {
                    if (p == 0 || p == 3) wait_vm<18>(); else if (p <= 2) wait_vm<22>(); else if (p == 4) wait_vm<13>(); else if (p == 5) wait_vm<11>(); else wait_vm<10>();
                    barrier();
                    if (p == 0) asm volatile("" :: "v"(code_probe), "s"(code_pc));
                    if (p == 0) { STAMP(2); }
                    if (p == 6) { STAMP(3); }
#pragma unroll
                    for (int s = 0; s < 4; ++s) xf[4 * p + s] = lds128(xo[s] + (p & 3) * 16384);
                }
#pragma unroll
                for (int s = 0; s < 4; ++s) {
                    if (p < 7) {
                        const unsigned o = p * 8192 + s * 1024;
                        wf[p & 1][2 * s] = tr_frag(w0 + o, w0 + o + 512); wf[p & 1][2 * s + 1] = tr_frag(w1 + o, w1 + o + 512);
                    }
                    SBAR();
                    if (p > 0) mfma_pair(isv, xf[4 * p - 4 + s], wf[(p - 1) & 1][2 * s], wf[(p - 1) & 1][2 * s + 1], acc0, acc1);
                    SBAR();
                    if (p == 0 && s == 0) {
#pragma unroll
                        for (int i = 0; i < 4; ++i) st128_async(tr_frag(rt_at[i], rt_2nd[i]), rws, rt_v[i], rt_dst[i]);
                    }
                    if (p < 7 && s == 1) pin(xf[4 * p], xf[4 * p + 1], xf[4 * p + 2], xf[4 * p + 3]);
                    if (p <= 2) {
                        if (s == 0) { req_w(p + 4, 0); req_w(p + 4, 1); }
                        if (s == 1 || s == 2) { req_x(rxa, p & 3, chunk_at(p + 4), 2 * s - 2); req_x(rxa, p & 3, chunk_at(p + 4), 2 * s - 1); }
                    }
                    if (p == 3 && s == 0) publish(ws, rt_word, nonce);
                    if (p == 4) req_wq(s);
                    if (p == 5 && s < 3) req_wq(4 + s);
                    if (p == 5 && s == 3) {
                        const rsrc_t rp = make_rsrc((const char*)args[5] + h * 1568, 1568);
                        dma16(rp, L_WPHI, voff, 0); dma16(rp, L_WPHI + 1024, voff, 1024);
                    }
                    if (p >= 6) { const int ch = 2 * (p - 6) + (s >> 1); req_x(rxb, xb_slot(ch), chunk_at(ch), 2 * (s & 1)); req_x(rxb, xb_slot(ch), chunk_at(ch), 2 * (s & 1) + 1); }
                    if (p == 6 && s == 3) peek(pw, ws, pw_word);
                    SBAR();
                }
            }
            asm volatile("s_nop 3" : "+v"(acc0), "+v"(acc1));
            {
                u32x4 f[4] = {pack_lo(acc0), pack_hi(acc0), pack_lo(acc1), pack_hi(acc1)};
                const bool all = isv && r32 >= 28, tail = isv ? all : (bool)hi;
                const unsigned va = r32 == 28 ? 0x3F803F80u : 0u, v2 = isv ? va : 0x3F803F80u, v3 = isv ? va : 0x00003F80u;
#pragma unroll
                for (int i = 0; i < 4; ++i) {
                    f[i][0] = all ? va : f[i][0]; f[i][1] = all ? va : f[i][1];
                    if (i & 1) { f[i][2] = tail ? v2 : f[i][2]; f[i][3] = tail ? v3 : f[i][3]; }
                    else       { f[i][2] = all ? va : f[i][2]; f[i][3] = all ? va : f[i][3]; }
                }
                const unsigned at = WS_KVA + ((h & 14) * 64 + tile) * 4096 + isv * 2048;
                st128_async(f[0], rws, voff, at); st128_async<1024>(f[1], rws, voff, at); st128_async(f[2], rws, voff, at + 64 * 4096); st128_async<1024>(f[3], rws, voff, at + 64 * 4096);
            }
            STAMP(4);
            if (wid == 0 && lane == 0) *(LAS unsigned*)(size_t)L_FLAG = 0;
            seen<12>(pw, ws, pw_word, nonce);
            lds_bar();
            u32x4 xg[28];
            {
                const unsigned wo = L_WQ + (32 * hi + q4) * 64 + col;
                f32x16 acc;
                u32x4 wq[28];
#pragma unroll
                for (int i = 0; i < 4; ++i) wq[i] = tr_frag(wo + i * 512, wo + i * 512 + 256);
#pragma unroll
                for (int i = 0; i < 28; ++i) {
                    if (i + 4 < 28) { const unsigned o = ((i + 4) >> 2) * 4096 + (i & 3) * 512; wq[i + 4] = tr_frag(wo + o, wo + o + 256); }
                    if (i < 8) xg[i] = lds128(xo[i & 3] + xb_slot(i >> 2) * 16384);
                    if (i >= 19 && i < 27) xg[i - 11] = lds128(xo[(i - 11) & 3] + xb_slot((i - 11) >> 2) * 16384);
                    SBAR();
                    if (i == 0) mfma_v0(acc, wq[i], xf[i]); else mfma_v(acc, wq[i], xf[i]);
                    SBAR();
                    if (i < 14) req_kvb(i >> 1, i & 1);
                    if (i == 13) { pin(xg[0], xg[1], xg[2], xg[3]); pin(xg[4], xg[5], xg[6], xg[7]); }
                    if (i >= 14 && i < 18) req_x(rxb, xb_slot(4), chunk_at(4), i - 14);
                    if (i == 18) {
                        wait_vm<18>();
                        publish(ws, WS_FK + (((h >> 1) * 64 + tile) * 2 + isv) * 8, nonce);
                        STAMP(5);
                    }
                    if (i >= 19 && i < 23) req_x(rxb, xb_slot(5), chunk_at(5), i - 19);
                    if (i == 23) pin(xg[8], xg[9], xg[10], xg[11]);
                    if (i >= 24) req_x(rxb, xb_slot(6), 6, i - 24);
                    SBAR();
                }
                settle(acc);
                acc *= QSCALE;
                u32x4 q0 = pack_lo(acc), q1 = pack_hi(acc);
                if (hi) { q1[2] = 0; q1[3] = 0; }
                sts128(L_QX + wid * 2048 + voff, q0); sts128(L_QX + wid * 2048 + 1024 + voff, q1);
            }
            STAMP(6);
            u32x2 pk_;
            const unsigned pk_word = WS_FK + ((h >> 1) * 64 + wid * 16) * 16 + r32 * 8;
            f32x16 ak = {}, av = {};
            asm volatile("s_nop 1" : "+v"(ak), "+v"(av));
            {
                u32x4 wg[28][2];
                wait_bar<19>();
#pragma unroll
                for (int i = -3; i < 28; ++i) {
                    if (i + 3 == 16) { wait_vm<8>(); barrier(); }
                    if (i + 3 < 28) {
                        const unsigned at = l_kvb((i + 3) >> 2) + ((i + 3) & 3) * 2048 + voff;
                        wg[i + 3][0] = lds128(at); wg[i + 3][1] = lds128(at + 1024);
                        if (i + 3 >= 16) xg[i + 3] = lds128(xo[(i + 3) & 3] + xb_slot((i + 3) >> 2) * 16384);
                    }
                    SBAR();
                    if (i >= 0) { mfma_v(ak, wg[i][0], xg[i]); mfma_v(av, xg[i], wg[i][1]); }
                    SBAR();
                    if (i >= 0 && i < 7) req_qb(i);
                    if (i == 12) peek(pk_, ws, pk_word);
                    if (i == 20) { seen<0>(pk_, ws, pk_word, nonce); STAMP(8); }
                    if (i == 21) LDKV(a, 0);
                    SBAR();
                }
            }
            u32x4 wp[2];
            {
                const unsigned fc = 16 * ((lane >> 4) & 1) + 4 * (lane & 3);
                const unsigned a0 = L_WPHI + (4 * hi + q4) * 56 + (fc < 28 ? fc : 24) * 2;
                wp[0] = tr_frag(a0, a0 + 8 * 56); wp[1] = tr_frag(a0 + 16 * 56, a0 + (hi ? 20 : 24) * 56);
            }
            barrier();
            u32x4 wqb[28];
            unsigned wqa = L_WQ + voff;
            asm volatile("" : "+v"(wqa));
#pragma unroll
            for (int i = 0; i < 8; ++i) wqb[i] = lds128(wqa + i * 1024);
            asm volatile("s_nop 1" : "+v"(ak), "+v"(av));
            STAMP(7);
            f32x16 pkk, aq;
            {
                u32x4 f0 = pack_lo(ak), f1 = pack_hi(ak);
                if (hi) { f1[2] = 0; f1[3] = 0; }
                asm volatile("" : "+v"(f0), "+v"(f1));
                mfma_v0(pkk, wp[0], f0); mfma_v(pkk, wp[1], f1);
            }
#define QSTEP(i) do { SBAR(); if (((i) & 3) == 0) { pin(wqb[i], wqb[(i) + 1], wqb[(i) + 2], wqb[(i) + 3]); \
                          if ((i) + 8 < 28) { _Pragma("unroll") for (int k_ = (i) + 8; k_ < (i) + 12; ++k_) wqb[k_] = lds128(wqa + k_ * 1024); } } \
                      if ((i) == 0) mfma_v0(aq, wqb[0], xg[0]); else mfma_v(aq, wqb[i], xg[i]); SBAR(); } while (0)
            QSTEP(0);
            u32x4 v0 = pack_lo(av);
            QSTEP(1);
            u32x4 v1 = pack_hi(av);
            if (r32 >= 28) { const unsigned v = r32 == 28 ? 0x3F803F80u : 0u; v0 = u32x4{v, v, v, v}; v1 = v0; }
            asm volatile("" : "+v"(pkk), "+v"(v0), "+v"(v1));
            QSTEP(2);
            const unsigned img = L_PHI + wid * 4096;
            {
                f32x16 ep, en;
                const float hx = max16(pkk), hn = min16(pkk);
                QSTEP(3);
                const float mx = both_max(hx) * LOG2E, mn = both_min(hn) * LOG2E;
                const float live = hi ? 0.f : 1.f;
                float sp = 0.f, sn = 0.f;
                float ap = fmaf(pkk[15], LOG2E, -mx), an = fmaf(pkk[15], -LOG2E, mn);
#pragma unroll
                for (int i = 0; i < 16; ++i) {
                    QSTEP(4 + i);
                    ep[15 - i] = __builtin_amdgcn_exp2f(ap); en[15 - i] = __builtin_amdgcn_exp2f(an);
                    SBAR();
                    if (i < 15) { ap = fmaf(pkk[14 - i], LOG2E, -mx); an = fmaf(pkk[14 - i], -LOG2E, mn); }
                    if (i == 1) { sp = ep[15]; sn = en[15]; }
                    if (i > 1) { sp += ep[16 - i]; sn += en[16 - i]; }
                    if (i == 4) { sp *= live; sn *= live; }
                }
                QSTEP(20);
                const float rp = __builtin_amdgcn_rcpf(both_sum(sp + ep[0])), rn = __builtin_amdgcn_rcpf(both_sum(sn + en[0]));
                const float rp3 = rp * live, rn3 = rn * live;
                const unsigned wr = img + (r32 & 15) * 136 + (r32 >> 4) * 16384 + hi * 8;
#pragma unroll
                for (int g = 0; g < 4; ++g) {
                    const float u = g == 3 ? rp3 : rp, v = g == 3 ? rn3 : rn;
                    QSTEP(21 + 2 * g);
                    sts64(wr + g * 16, u32x2{pk(ep[4 * g] * u, ep[4 * g + 1] * u), pk(ep[4 * g + 2] * u, ep[4 * g + 3] * u)});
                    if (g < 3) QSTEP(22 + 2 * g);
                    sts64(wr + 64 + g * 16, u32x2{pk(en[4 * g] * v, en[4 * g + 1] * v), pk(en[4 * g + 2] * v, en[4 * g + 3] * v)});
                    { const unsigned at = L_QX + ((wid + g) & 3) * 2048 + voff; q[g][0] = lds128(at); q[g][1] = lds128(at + 1024); }
                }
            }
#undef QSTEP
            u32x4 pf[4];
            {
                const unsigned rd = img + (4 * hi + q4) * 136 + col;
#pragma unroll
                for (int i = 0; i < 4; ++i) pf[i] = tr_frag(rd + (i >> 1) * 64 + (i & 1) * 16384, rd + (i >> 1) * 64 + (i & 1) * 16384 + 8 * 136);
            }
            asm volatile("" : "+v"(aq));
            f32x16 pqq, kv0, kv1, sc[4];
            {
                u32x4 g0 = pack_lo(aq), g1 = pack_hi(aq);
                if (hi) { g1[2] = 0; g1[3] = 0; }
                asm volatile("" : "+v"(g0), "+v"(g1));
                mfma_v0(pqq, wp[0], g0); SBAR(); LDKV(b, 1); SBAR(); mfma_v(pqq, wp[1], g1); SBAR(); LDKV(c, 2); SBAR();
            }
            mfma_v0(kv0, pf[0], v0);
            landed<8>(a.k0, a.k1, a.v0, a.v1);
            asm volatile("s_nop 4" : "+v"(pqq));
            SBAR();
            const float qhx = max16(pqq);
            SBAR(); mfma_v(kv0, pf[1], v1); SBAR();
            const float qhn = min16(pqq);
            SBAR(); mfma_v0(kv1, pf[2], v0); SBAR();
            const float qmx = both_max(qhx) * LOG2E, qmn = both_min(qhn) * LOG2E;
            SBAR(); mfma_v(kv1, pf[3], v1); SBAR();
            f32x16 eqp, eqn;
#pragma unroll
            for (int i = 0; i < 8; ++i) {
                const float a0 = fmaf(pqq[2 * i], LOG2E, -qmx), a1 = fmaf(pqq[2 * i + 1], LOG2E, -qmx), n0 = fmaf(pqq[2 * i], -LOG2E, qmn), n1 = fmaf(pqq[2 * i + 1], -LOG2E, qmn);
                SBAR();
                if (i & 1) mfma_v(sc[i >> 1], a.k1, q[i >> 1][1]); else mfma_v0(sc[i >> 1], a.k0, q[i >> 1][0]);
                SBAR();
                eqp[2 * i] = __builtin_amdgcn_exp2f(a0); eqp[2 * i + 1] = __builtin_amdgcn_exp2f(a1); eqn[2 * i] = __builtin_amdgcn_exp2f(n0); eqn[2 * i + 1] = __builtin_amdgcn_exp2f(n1);
                if (i == 0) asm volatile("" : "+v"(kv0));
                if (i == 4) asm volatile("" : "+v"(kv1));
                sts128(L_RED + wid * 8192 + i * 1024 + voff, quad(i < 4 ? kv0 : kv1, i & 3));
                SBAR();
            }
            eq[0] = pack_lo(eqp); eq[1] = pack_hi(eqp); eq[2] = pack_lo(eqn); eq[3] = pack_hi(eqn);
            asm volatile("" : "+v"(eq[0]), "+v"(eq[1]), "+v"(eq[2]), "+v"(eq[3]), "+v"(sc[0]), "+v"(sc[1]), "+v"(sc[2]), "+v"(sc[3]));
            {
                const float m[4] = {max16(sc[0]), max16(sc[1]), max16(sc[2]), max16(sc[3])};
                ref_post(wid, lane, m);
            }
            lds_bar();
            {
                ref_take(wid, lane, q, REF_OFFSET);
#pragma unroll
                for (int i = 0; i < 2; ++i) {
                    const unsigned off = (2 * wid + i) * 1024 + voff;
                    f32x4 u = __builtin_bit_cast(f32x4, lds128(L_RED + off));
#pragma unroll
                    for (int v = 1; v < 4; ++v) u += __builtin_bit_cast(f32x4, lds128(L_RED + v * 8192 + off));
                    st128_async(__builtin_bit_cast(u32x4, u), rws, off, WS_KVP + (h * 16 + rb) * 8192);
                }
                STAMP(9);
            }
        }
    }
    const int qt = rb * 4 + (h & 3), nt = (h & 12) + wid;
    const bool live = nt < 14;
    u32x4 wob_f[32], woa_f[32], f0, f1;
    const unsigned wo_at = WS_WO + (live ? nt : 0) * 32768;
    const unsigned o_mine = (tile * 16 + h) * 2048, o_need = (qt * 16 + wid * 4) * 2048;
    {
        const int lane = lane_id(), hi = lane >> 5, r32 = lane & 31;
        const unsigned voff = lane * 16;
        STAMP(10);
        f32x16 o0 = {}, o1 = {}, o2 = {}, o3 = {};
        f32x16 sa, sb; u32x4 pa, pb = {};
        float e12 = 0.f, e13 = 0.f, e14 = 0.f, e15 = 0.f;
        d.v1 = u32x4{};
#define QK0(S, K, Q) asm volatile("v_mfma_f32_32x32x16_bf16 %0, %1, %2, 0" : "=&v"(S) : "v"(K), "v"(Q))
#define QK1(S, K, Q) asm volatile("v_mfma_f32_32x32x16_bf16 %0, %1, %2, %0" : "+v"(S) : "v"(K), "v"(Q))
        asm volatile("s_nop 1" : "+v"(q[0][1]), "+v"(q[1][1]), "+v"(q[2][1]), "+v"(q[3][1]));
        QK0(sa, a.k0, q[0][0]); QK1(sa, a.k1, q[0][1]);
        asm volatile("s_nop 11" : "+v"(sa));
#define EX(S, i) S[i] = __builtin_amdgcn_exp2f(S[i])
#define STEP(NEXT, SC, SN, KN0, KN1, QN0, QN1, OP, VP1, OC, VC0) do { \
    if (NEXT) QK0(SN, KN0, QN0); SBAR(); \
    pb[2] = pk(e12, e13); SBAR(); EX(SC, 0); SBAR(); pb[3] = pk(e14, e15); SBAR(); EX(SC, 1); EX(SC, 2); EX(SC, 3); SBAR(); \
    OP = mfma(VP1, pb, OP); SBAR(); \
    EX(SC, 4); EX(SC, 5); SBAR(); pa[0] = pk(SC[0], SC[1]); SBAR(); EX(SC, 6); SBAR(); pa[1] = pk(SC[2], SC[3]); SBAR(); EX(SC, 7); SBAR(); \
    if (NEXT) QK1(SN, KN1, QN1); SBAR(); \
    EX(SC, 8); SBAR(); pa[2] = pk(SC[4], SC[5]); SBAR(); EX(SC, 9); SBAR(); pa[3] = pk(SC[6], SC[7]); SBAR(); EX(SC, 10); EX(SC, 11); SBAR(); \
    OC = mfma(VC0, pa, OC); SBAR(); \
    e12 = __builtin_amdgcn_exp2f(SC[12]); SBAR(); pb[0] = pk(SC[8], SC[9]); SBAR(); e13 = __builtin_amdgcn_exp2f(SC[13]); SBAR(); pb[1] = pk(SC[10], SC[11]); SBAR(); \
    e14 = __builtin_amdgcn_exp2f(SC[14]); e15 = __builtin_amdgcn_exp2f(SC[15]); SBAR(); } while (0)
#define TILE(J, CUR, PREV, NXT, NW, SEAM, MORE) do { \
    STEP(true, sa, sb, CUR.k0, CUR.k1, q[1][0], q[1][1], o3, PREV.v1, o0, CUR.v0); \
    if (J + 3 <= 15) LDKV(PREV, J + 3); \
    SEAM; SBAR(); \
    STEP(true, sb, sa, CUR.k0, CUR.k1, q[2][0], q[2][1], o0, CUR.v1, o1, CUR.v0); \
    MORE(0, 3); SBAR(); \
    STEP(true, sa, sb, CUR.k0, CUR.k1, q[3][0], q[3][1], o1, CUR.v1, o2, CUR.v0); \
    MORE(3, 6); \
    if (J < 15) landed<NW>(NXT.k0, NXT.k1, NXT.v0, NXT.v1); \
    SBAR(); \
    STEP(J < 15, sb, sa, NXT.k0, NXT.k1, q[0][0], q[0][1], o2, CUR.v1, o3, CUR.v0); \
    MORE(6, 8); SBAR(); } while (0)
        u32x4 pl[2], sf[4];
        u32x2 pp;
        const unsigned pp_word = lane < 16 ? WS_FP + (h * 4 + (rb >> 2)) * 128 + lane * 8 : WS_FWO + (((lane >> 5) & 1) * 2 + (live ? nt / 7 : 1)) * 128 + (lane & 15) * 8;
        const unsigned ps_word = WS_FS + h * 512 + lane * 8;
        auto slice_load = [&]() {
            const unsigned at = WS_KVP + h * 16 * 8192 + tile * 128, v = (lane >> 3) * 8192 + (lane & 7) * 16;
            ld128_async_dev(pl[0], rws, v, at); ld128_async_dev(pl[1], rws, v, at + 8 * 8192);
        };
        auto slice_sum = [&]() {
            asm volatile("s_waitcnt vmcnt(4)" : "+v"(pl[0]), "+v"(pl[1]) :: "memory");
            float u[4];
#pragma unroll
            for (int e = 0; e < 4; ++e) {
                u[e] = both_sum(__uint_as_float(pl[0][e]) + __uint_as_float(pl[1][e]));
                auto r = __builtin_amdgcn_permlane16_swap(__float_as_uint(u[e]), __float_as_uint(u[e]), false, false);
                u[e] = __uint_as_float(r[0]) + __uint_as_float(r[1]);
                u[e] += __uint_as_float(__builtin_amdgcn_update_dpp(0u, __float_as_uint(u[e]), 0x128, 0xf, 0xf, false));
            }
            const u32x2 v = {pk(u[0], u[1]), pk(u[2], u[3])};
            const unsigned at = WS_ST + h * 4096 + (rb >> 2) * 1024 + ((rb & 1) * 32 + wid * 8) * 16 + ((rb >> 1) & 1) * 8;
            asm volatile("buffer_store_dwordx2 %0, %1, %2, %3 offen sc1" ::"v"(v), "v"(lane < 8 ? voff : OOB), "s"(rws), "s"(at) : "memory");
        };
        auto state_load = [&]() {
#pragma unroll
            for (int i = 0; i < 4; ++i) ld_frag<false>(sf[i], rws, voff, WS_ST + h * 4096, i);
        };
        auto wo_load = [&](int n, int first, int last) {
#pragma unroll
            for (int i = 0; i < 8; ++i) if (i >= first && i < last) ld_frag<true>(wob_f[8 * n + i], rws, voff, wo_at + 14 * 32768, 8 * n + i);
        };
        auto wa_load = [&](int n, int first, int last) {
#pragma unroll
            for (int i = 0; i < 8; ++i) if (i >= first && i < last) {
                if (n == 3) ld_frag<true>(woa_f[8 * n + i], rws, voff, wo_at, 8 * n + i); else ld_frag<false>(woa_f[8 * n + i], rws, voff, wo_at, 8 * n + i);
            }
        };
        f32x16 np, nn;
        auto start_b = [&]() {
            landed<4>(sf[0], sf[1], sf[2], sf[3]);
            if (r32 == 29) {
                const unsigned tail = hi ? 0u : 0x3F803F80u;
                sf[0] = u32x4{0x3F803F80u, 0x3F803F80u, 0x3F803F80u, 0x3F803F80u}; sf[2] = sf[0];
                sf[1] = u32x4{0x3F803F80u, 0x3F803F80u, tail, tail}; sf[3] = sf[1];
            }
            asm volatile("" : "+v"(sf[0]), "+v"(sf[1]), "+v"(sf[2]), "+v"(sf[3]));
            mfma_v0(np, sf[0], eq[0]); mfma_v(np, sf[1], eq[1]); mfma_v0(nn, sf[2], eq[2]); mfma_v(nn, sf[3], eq[3]);
        };
        auto finish_b = [&](int first, int last) {
            if (first != 0) return;
            asm volatile("" : "+v"(np), "+v"(nn));
            const float rp_ = __builtin_amdgcn_rcpf(np[13]), rn_ = __builtin_amdgcn_rcpf(nn[13]);
            const float inv = __builtin_amdgcn_rcpf(fmaf(np[12], rp_, fmaf(nn[12], rn_, 1e-6f)));
            const float ap = upper_val(rp_ * inv), an = upper_val(rn_ * inv);
            f32x16 o;
#pragma unroll
            for (int i = 0; i < 16; ++i) o[i] = fmaf(np[i], ap, nn[i] * an);
            u32x4 g0 = pack_lo(o), g1 = pack_hi(o);
            if (hi) { g1[2] = 0; g1[3] = 0; }
            st128_async(g0, rws, voff, WS_OB + o_mine); st128_async<1024>(g1, rws, voff, WS_OB + o_mine);
        };
#define NONE(x, y) (void)0
#define WB0(x, y) wo_load(0, x, y)
#define WB1(x, y) wo_load(1, x, y)
#define WB2(x, y) wo_load(2, x, y)
#define WB3(x, y) wo_load(3, x, y)
#define WA0(x, y) wa_load(0, x, y)
#define WA1(x, y) wa_load(1, x, y)
#define WA2(x, y) wa_load(2, x, y)
#define WA3(x, y) wa_load(3, x, y)
        TILE(0, a, d, b, 10, (void)0, NONE);
        TILE(1, b, a, c, 11, (wait_vm<8>(), publish(ws, WS_FP + ((h * 4 + wid) * 16 + rb) * 8, nonce)), NONE);
        TSTAMP(19); TILE(2, c, b, d, 9, (void)0, NONE);
        TILE(3, d, c, a, 9, (void)0, NONE);
        TILE(4, a, d, b, 9, peek(pp, ws, pp_word), NONE);
        TSTAMP(20); TILE(5, b, a, c, 11, (seen<4>(pp, ws, pp_word, nonce), slice_load()), NONE);
        TSTAMP(21); TILE(6, c, b, d, 18, slice_sum(), WB0);
        TSTAMP(22); TILE(7, d, c, a, 25, (void)0, WB1);
        TILE(8, a, d, b, 32, (wait_vm<24>(), publish(ws, WS_FS + (h * 64 + tile) * 8, nonce)), WB2);
        TILE(9, b, a, c, 31, (void)0, WB3);
        TILE(10, c, b, d, 32, peek(pp, ws, ps_word), WA3);
        TSTAMP(23); TILE(11, d, c, a, 29, (seen<12>(pp, ws, ps_word, nonce), state_load()), NONE);
        TSTAMP(24); TILE(12, a, d, b, 23, start_b(), finish_b);
        TSTAMP(25); TILE(13, b, a, c, 16, (void)0, WA0);
        TILE(14, c, b, d, 17, (wait_vm<8>(), publish(ws, WS_FOB + (tile * 16 + h) * 8, nonce)), WA1);
        TSTAMP(26); TILE(15, d, c, a, 0, (void)0, WA2);
        pb[2] = pk(e12, e13); pb[3] = pk(e14, e15);
        o3 = mfma(d.v1, pb, o3);
#undef TILE
#undef STEP
#undef EX
#undef QK0
#undef QK1
#undef NONE
#undef WB0
#undef WB1
#undef WB2
#undef WB3
#undef WA0
#undef WA1
#undef WA2
#undef WA3
        STAMP(11);
        combine_post(wid, lane, o1, o2, o3);
        u32x2 pob;
        const unsigned pob_word = WS_FOB + (qt * 16 + wid * 4 + (lane & 3)) * 8;
        peek(pob, ws, pob_word);
        lds_bar();
        const bool bad = combine_take(wid, lane, o0, f0, f1);
        st128_async(f0, rws, voff, WS_OA + o_mine); st128_async<1024>(f1, rws, voff, WS_OA + o_mine);
        STAMP(12);
        if (__any(bad) && lane == 0) *(LAS unsigned*)(size_t)L_FLAG = 1;
        seen<2>(pob, ws, pob_word, nonce);
#pragma unroll
        for (int i = 0; i < 8; ++i) dma16_dev(rws, L_OIMG + (wid * 8 + i) * 1024, voff, WS_OB + o_need + i * 1024);
        wait_bar<8>();
        if (__builtin_amdgcn_readfirstlane(*(const LAS unsigned*)(size_t)L_FLAG)) {
            float ref[4];
#pragma unroll
            for (int t = 0; t < 4; ++t) { if (hi) { q[t][1][2] = 0; q[t][1][3] = 0; } ref[t] = -__builtin_inff(); }
#pragma unroll 1
            for (int j = 0; j < 16; ++j) {
                const u32x4 k0 = ld128_dev(rws, voff, kv_at + j * 4096), k1 = ld128_dev(rws, voff + 1024, kv_at + j * 4096);
#pragma unroll
                for (int t = 0; t < 4; ++t) { f32x16 s = mfma(k0, q[t][0], zero); s = mfma(k1, q[t][1], s); ref[t] = fmaxf(ref[t], max16(s)); }
            }
            ref_post(wid, lane, ref);
            lds_bar();
            ref_take(wid, lane, q, 0.f);
            lds_bar();
            f32x16 x[4] = {};
#pragma unroll 1
            for (int j = 0; j < 16; ++j) {
                const u32x4 k0 = ld128_dev(rws, voff, kv_at + j * 4096), k1 = ld128_dev(rws, voff + 1024, kv_at + j * 4096);
                const u32x4 v0 = ld128_dev(rws, voff + 2048, kv_at + j * 4096), v1 = ld128_dev(rws, voff + 3072, kv_at + j * 4096);
#pragma unroll
                for (int t = 0; t < 4; ++t) {
                    f32x16 s = mfma(k0, q[t][0], zero); s = mfma(k1, q[t][1], s);
#pragma unroll
                    for (int i = 0; i < 16; ++i) s[i] = __builtin_amdgcn_exp2f(s[i]);
                    x[t] = mfma(v0, pack_lo(s), x[t]); x[t] = mfma(v1, pack_hi(s), x[t]);
                }
            }
            combine_post(wid, lane, x[1], x[2], x[3]);
            lds_bar();
            combine_take(wid, lane, x[0], f0, f1);
            st128_async(f0, rws, voff, WS_OA + o_mine); st128_async<1024>(f1, rws, voff, WS_OA + o_mine);
            wait_vm<0>();
        }
        publish(ws, WS_FOA + (tile * 16 + h) * 8, nonce);
        STAMP(13);
    }
#undef LDKV
    {
        const int lane = lane_id();
        const unsigned voff = lane * 16;
#pragma unroll
        for (int i = 0; i < 32; i += 8)
            asm volatile("" : "+a"(wob_f[i]), "+a"(wob_f[i + 1]), "+a"(wob_f[i + 2]), "+a"(wob_f[i + 3]), "+a"(wob_f[i + 4]), "+a"(wob_f[i + 5]), "+a"(wob_f[i + 6]), "+a"(wob_f[i + 7]));
        f32x16 r = {};
        asm volatile("s_nop 1" : "+v"(r));
        u32x4 of[32];
        u32x2 poa;
        const unsigned poa_word = WS_FOA + (qt * 16 + wid * 4 + (lane & 3)) * 8;
        wait_vm<1>();
        barrier();
#pragma unroll
        for (int i = 0; i < 6; ++i) of[i] = lds128(L_OIMG + i * 1024 + voff);
#pragma unroll
        for (int i = 0; i < 32; ++i) {
            if (i + 6 < 32) of[i + 6] = lds128(L_OIMG + (i + 6) * 1024 + voff);
            SBAR();
            asm volatile("v_mfma_f32_32x32x16_bf16 %0, %1, %2, %0" : "+v"(r) : "a"(wob_f[i]), "v"(of[i]));
            if (i == PEEK_AT) peek(poa, ws, poa_word);
            if (i == SEEN_AT) { seen<0>(poa, ws, poa_word, nonce); STAMP(15); }
            if (i >= SEEN_AT && i < SEEN_AT + 8) dma16_dev(rws, L_OIMG2 + (wid * 8 + i - SEEN_AT) * 1024, voff, WS_OA + o_need + (i - SEEN_AT) * 1024);
            SBAR();
        }
        settle(r);
        stage_out(L_OUT, wid, lane, r);
        STAMP(14);
        wait_bar<0>();
        if (blk == 0 && wid == 0) publish(ws, WS_EPOCH, nonce);
#pragma unroll
        for (int i = 0; i < 24; i += 8)
            asm volatile("" : "+v"(woa_f[i]), "+v"(woa_f[i + 1]), "+v"(woa_f[i + 2]), "+v"(woa_f[i + 3]), "+v"(woa_f[i + 4]), "+v"(woa_f[i + 5]), "+v"(woa_f[i + 6]), "+v"(woa_f[i + 7]));
        asm volatile("" : "+a"(woa_f[24]), "+a"(woa_f[25]), "+a"(woa_f[26]), "+a"(woa_f[27]), "+a"(woa_f[28]), "+a"(woa_f[29]), "+a"(woa_f[30]), "+a"(woa_f[31]));
        f32x16 s;
#pragma unroll
        for (int i = 0; i < 6; ++i) of[i] = lds128(L_OIMG2 + i * 1024 + voff);
#pragma unroll
        for (int i = 0; i < 32; ++i) {
            if (i + 6 < 32) of[i + 6] = lds128(L_OIMG2 + (i + 6) * 1024 + voff);
            SBAR();
            if (i == 0) mfma_v0(s, woa_f[0], of[0]); else if (i < 24) mfma_v(s, woa_f[i], of[i]);
            else asm volatile("v_mfma_f32_32x32x16_bf16 %0, %1, %2, %0" : "+v"(s) : "a"(woa_f[i]), "v"(of[i]));
            if (i == 4) flush_out(L_OUT, wid, lane, h, qt, 1, (u16*)args[7]);
            SBAR();
        }
        settle(s);
        STAMP(16);
        stage_out(L_OUT + 16384, wid, lane, s);
        lds_bar();
        asm volatile(".Lfused_code_end:");
        flush_out(L_OUT + 16384, wid, lane, h, qt, 0, (u16*)args[7]);
    }
}

}

extern "C" void kernel_launch(void* const* d_in, const int* in_sizes, int n_in, void* d_out, int out_size, void* d_ws, size_t ws_size, hipStream_t stream) {
    (void)hipFuncSetAttribute((const void*)fused, hipFuncAttributeMaxDynamicSharedMemorySize, LDS_BYTES);
    fused<<<256, 256, LDS_BYTES, stream>>>((const u16*)d_in[0], (const u16*)d_in[1], (const u16*)d_in[2], (const u16*)d_in[3], (const u16*)d_in[4], (const u16*)d_in[5],
                                           (const u16*)d_in[6], (u16*)d_out, (char*)d_ws);
}
```

```cpp
#include <hip/hip_runtime.h>
#include <stdint.h>

namespace {

typedef unsigned short u16;
typedef unsigned long long u64;
typedef __attribute__((ext_vector_type(16))) float f32x16;
typedef __attribute__((ext_vector_type(4))) float f32x4;
typedef __attribute__((ext_vector_type(2))) float f32x2;
typedef __attribute__((ext_vector_type(8))) __bf16 bf16x8;
typedef __attribute__((ext_vector_type(2))) __bf16 bf16x2;
typedef __attribute__((ext_vector_type(4))) unsigned u32x4;
typedef __attribute__((ext_vector_type(2))) unsigned u32x2;
typedef __attribute__((ext_vector_type(4))) short s16x4;
typedef __attribute__((ext_vector_type(8))) unsigned long long u64x8;
typedef __amdgpu_buffer_rsrc_t rsrc_t;
#define LAS __attribute__((address_space(3)))
#define GAS __attribute__((address_space(1)))
#define DI __device__ __forceinline__
#define SBAR() __builtin_amdgcn_sched_barrier(0)

constexpr int SEQ = 2048, DM = 448;
constexpr unsigned XROW = DM * 2;
constexpr unsigned WROW = 3 * DM * 2;
constexpr unsigned X_BYTES = SEQ * XROW, W_BYTES = DM * WROW, WO_BYTES = DM * XROW;
constexpr unsigned WS_EPOCH = 0;
constexpr unsigned WS_FK = 4096;
constexpr unsigned WS_FP = WS_FK + 8192;
constexpr unsigned WS_FOA = WS_FP + 8192;
constexpr unsigned WS_FOB = WS_FOA + 8192;
constexpr unsigned WS_FW = WS_FOB + 8192;
constexpr unsigned WS_FWO = WS_FW + 2048;
constexpr unsigned WS_FS = WS_FWO + 2048;
constexpr unsigned WS_VOID = WS_FS + 8192;
constexpr unsigned WS_KVA = 65536;
constexpr unsigned WS_OA = WS_KVA + (4u << 20);
constexpr unsigned WS_OB = WS_OA + (2u << 20);
constexpr unsigned WS_KVP = WS_OB + (2u << 20);
constexpr unsigned WS_WB = WS_KVP + (2u << 20);
constexpr unsigned WS_WO = WS_WB + (2u << 20);
constexpr unsigned WS_ST = WS_WO + (1u << 20);
constexpr unsigned WS_WQ = WS_ST + 65536;
constexpr unsigned WS_END = WS_WQ + 16 * 28 * 1024;
constexpr int LDS_BYTES = 160 * 1024;
constexpr unsigned L_WKV = 0, L_RET = 57344, L_QB = 57344;
constexpr unsigned L_X = 86016;
constexpr unsigned l_kvb(int ch) { return ch == 0 ? 151552 : (ch - 1) * 8192; }
constexpr unsigned l_wq(int ch) { return ch < 6 ? ch * 4096 : 49152; }
constexpr unsigned L_QX = 151552, L_REF = 40960, L_WPHI = 159744, L_FLAG = 161792;
constexpr unsigned L_PHI = 86016, L_RED = 118784;
constexpr unsigned L_COMB = 0, L_OIMG = 65536, L_OIMG2 = 0, L_OUT = 98304;
constexpr float LOG2E = 1.4426950408889634f;
constexpr float QSCALE = 0.18898223650461362f * LOG2E;
#define REF_OFF 100.f
constexpr float REF_OFFSET = REF_OFF;
constexpr float L_LIMIT = 18446744073709551616.f;
constexpr int SC1 = 16;
constexpr unsigned OOB = 0x80000000u;
#define SPINLOG 16
constexpr unsigned SPIN_CAP = 1u << SPINLOG;
#define PEEK_AT 4
#define SEEN_AT 18
#define KP_PEEK 1
#define KP_SEEN 13
#define PSLEEP 4

DI f32x16 mfma(u32x4 a, u32x4 b, f32x16 c) {
    return __builtin_amdgcn_mfma_f32_32x32x16_bf16(__builtin_bit_cast(bf16x8, a), __builtin_bit_cast(bf16x8, b), c, 0, 0, 0);
}
DI unsigned pk(float lo, float hi) { f32x2 v = {lo, hi}; return __builtin_bit_cast(unsigned, __builtin_convertvector(v, bf16x2)); }
DI u32x4 pack_lo(const f32x16& x) { return u32x4{pk(x[0], x[1]), pk(x[2], x[3]), pk(x[4], x[5]), pk(x[6], x[7])}; }
DI u32x4 pack_hi(const f32x16& x) { return u32x4{pk(x[8], x[9]), pk(x[10], x[11]), pk(x[12], x[13]), pk(x[14], x[15])}; }
DI u32x4 quad(const f32x16& x, int g) { return u32x4{__float_as_uint(x[4 * g]), __float_as_uint(x[4 * g + 1]), __float_as_uint(x[4 * g + 2]), __float_as_uint(x[4 * g + 3])}; }
DI rsrc_t make_rsrc(const void* p, unsigned bytes) { return __builtin_amdgcn_make_buffer_rsrc((void*)p, 0, bytes, 0x00020000); }
DI u32x4 ld128(rsrc_t rs, unsigned voff, unsigned soff) { return __builtin_amdgcn_raw_buffer_load_b128(rs, voff, soff, 0); }
DI u32x4 ld128_dev(rsrc_t rs, unsigned voff, unsigned soff) { return __builtin_amdgcn_raw_buffer_load_b128(rs, voff, soff, SC1); }
DI void st128_dev(u32x4 v, rsrc_t rs, unsigned voff, unsigned soff) { __builtin_amdgcn_raw_buffer_store_b128(v, rs, voff, soff, SC1); }
DI void dma16(rsrc_t rs, unsigned lds_addr, unsigned voff, unsigned soff) {
    asm volatile("s_mov_b32 m0, %0\n\ts_nop 0\n\tbuffer_load_dwordx4 %1, %2, %3 offen lds" ::"s"(lds_addr), "v"(voff), "s"(rs), "s"(soff) : "memory");
}
DI void ld128_async(u32x4& d, rsrc_t rs, unsigned voff, unsigned soff) {
    asm volatile("buffer_load_dwordx4 %0, %1, %2, %3 offen" : "=v"(d) : "v"(voff), "s"(rs), "s"(soff) : "memory");
}
DI void dma16_dev(rsrc_t rs, unsigned lds_addr, unsigned voff, unsigned soff) {
    asm volatile("s_mov_b32 m0, %0\n\ts_nop 0\n\tbuffer_load_dwordx4 %1, %2, %3 offen sc1 lds" ::"s"(lds_addr), "v"(voff), "s"(rs), "s"(soff) : "memory");
}
template <int N> DI void wait_vm() { asm volatile("s_waitcnt vmcnt(%0)" ::"n"(N) : "memory"); }
template <int N> DI void wait_bar() { asm volatile("s_waitcnt vmcnt(%0) lgkmcnt(0)\n\ts_barrier" ::"n"(N) : "memory"); }
DI void lds_bar() { asm volatile("s_waitcnt lgkmcnt(0)\n\ts_barrier" ::: "memory"); }
DI u32x4 lds128(unsigned a) { return *(const LAS u32x4*)(size_t)a; }
DI void sts128(unsigned a, u32x4 v) { *(LAS u32x4*)(size_t)a = v; }
DI void sts64(unsigned a, u32x2 v) { *(LAS u32x2*)(size_t)a = v; }
DI u32x2 tr64(unsigned a) { return __builtin_bit_cast(u32x2, __builtin_amdgcn_ds_read_tr16_b64_v4i16((LAS s16x4*)(size_t)a)); }
DI u32x4 tr_frag(unsigned a0, unsigned a1) { u32x2 lo = tr64(a0), hi = tr64(a1); return u32x4{lo[0], lo[1], hi[0], hi[1]}; }
DI float max3(float a, float b, float c) { float r; asm("v_max3_f32 %0, %1, %2, %3" : "=v"(r) : "v"(a), "v"(b), "v"(c)); return r; }
DI float min3(float a, float b, float c) { float r; asm("v_min3_f32 %0, %1, %2, %3" : "=v"(r) : "v"(a), "v"(b), "v"(c)); return r; }
DI float both_max(float v) { auto r = __builtin_amdgcn_permlane32_swap(__float_as_uint(v), __float_as_uint(v), false, false); return max3(__uint_as_float(r[0]), __uint_as_float(r[1]), __uint_as_float(r[1])); }
DI float both_min(float v) { auto r = __builtin_amdgcn_permlane32_swap(__float_as_uint(v), __float_as_uint(v), false, false); return min3(__uint_as_float(r[0]), __uint_as_float(r[1]), __uint_as_float(r[1])); }
DI float both_sum(float v) { auto r = __builtin_amdgcn_permlane32_swap(__float_as_uint(v), __float_as_uint(v), false, false); return __uint_as_float(r[0]) + __uint_as_float(r[1]); }
DI float upper_val(float v) { auto r = __builtin_amdgcn_permlane32_swap(__float_as_uint(v), __float_as_uint(v), false, false); return __uint_as_float(r[1]); }
DI float max16(const f32x16& s) {
    const float a = max3(s[0], s[1], s[2]), b = max3(s[3], s[4], s[5]), c = max3(s[6], s[7], s[8]), d = max3(s[9], s[10], s[11]), e = max3(s[12], s[13], s[14]);
    return max3(max3(a, b, c), max3(d, e, s[15]), a);
}
DI float min16(const f32x16& s) {
    const float a = min3(s[0], s[1], s[2]), b = min3(s[3], s[4], s[5]), c = min3(s[6], s[7], s[8]), d = min3(s[9], s[10], s[11]), e = min3(s[12], s[13], s[14]);
    return min3(min3(a, b, c), min3(d, e, s[15]), a);
}

template <int OFF = 0> DI void ld128_async_dev(u32x4& d, rsrc_t rs, unsigned voff, unsigned soff) {
    asm volatile("buffer_load_dwordx4 %0, %1, %2, %3 offen offset:%4 sc1" : "=v"(d) : "v"(voff), "s"(rs), "s"(soff), "n"(OFF) : "memory");
}
template <int OFF = 0> DI void ld128_async_acc(u32x4& d, rsrc_t rs, unsigned voff, unsigned soff) {
    asm volatile("buffer_load_dwordx4 %0, %1, %2, %3 offen offset:%4 sc1" : "=a"(d) : "v"(voff), "s"(rs), "s"(soff), "n"(OFF) : "memory");
}
template <bool ACC> DI void ld_frag(u32x4& d, rsrc_t rs, unsigned voff, unsigned base, int i) {
    const unsigned s = base + (i >> 2) * 4096;
    if (ACC) { if ((i & 3) == 0) ld128_async_acc(d, rs, voff, s); if ((i & 3) == 1) ld128_async_acc<1024>(d, rs, voff, s); if ((i & 3) == 2) ld128_async_acc<2048>(d, rs, voff, s); if ((i & 3) == 3) ld128_async_acc<3072>(d, rs, voff, s); }
    else     { if ((i & 3) == 0) ld128_async_dev(d, rs, voff, s); if ((i & 3) == 1) ld128_async_dev<1024>(d, rs, voff, s); if ((i & 3) == 2) ld128_async_dev<2048>(d, rs, voff, s); if ((i & 3) == 3) ld128_async_dev<3072>(d, rs, voff, s); }
}
template <int OFF = 0> DI void st128_async(u32x4 v, rsrc_t rs, unsigned voff, unsigned soff) {
    asm volatile("buffer_store_dwordx4 %0, %1, %2, %3 offen offset:%4 sc1\n\ts_nop 1" ::"v"(v), "v"(voff), "s"(rs), "s"(soff), "n"(OFF) : "memory");
}
template <int N> DI void landed(u32x4& a, u32x4& b, u32x4& c, u32x4& d) { asm volatile("s_waitcnt vmcnt(%4)" : "+v"(a), "+v"(b), "+v"(c), "+v"(d) : "n"(N) : "memory"); }
template <int N> DI void landed8(u32x4 (&r)[8]) {
    asm volatile("s_waitcnt vmcnt(%8)" : "+v"(r[0]), "+v"(r[1]), "+v"(r[2]), "+v"(r[3]), "+v"(r[4]), "+v"(r[5]), "+v"(r[6]), "+v"(r[7]) : "n"(N) : "memory");
}
DI void mfma_v(f32x16& acc, u32x4 a, u32x4 b) { asm("v_mfma_f32_32x32x16_bf16 %0, %1, %2, %0" : "+v"(acc) : "v"(a), "v"(b)); }
DI void mfma_v0(f32x16& acc, u32x4 a, u32x4 b) { asm("s_nop 1\n\tv_mfma_f32_32x32x16_bf16 %0, %1, %2, 0" : "=&v"(acc) : "v"(a), "v"(b)); }
DI void settle(f32x16& x) { asm volatile("s_nop 11" : "+v"(x)); }
DI void barrier() { asm volatile("s_barrier" ::: "memory"); }
DI int lane_id() { int l; asm volatile("v_mbcnt_lo_u32_b32 %0, -1, 0\n\tv_mbcnt_hi_u32_b32 %0, -1, %0" : "=v"(l)); return l; }

#define STAMP(id) do {} while (0)
#define TSTAMP(id) do {} while (0)

DI void epoch_request(u64& v, const char* p) { asm volatile("s_load_dwordx2 %0, %1, 0x0" : "=s"(v) : "s"(p) : "memory"); }
DI void publish(char* ws, unsigned word, u64 nonce) {
    asm volatile("s_mov_b64 exec, 1\n\tglobal_store_dwordx2 %0, %1, %2 sc1\n\ts_mov_b64 exec, -1" ::"v"(word), "v"(nonce), "s"(ws) : "memory");
}
DI void peek(u32x2& v, const char* ws, unsigned word) { asm volatile("global_load_dwordx2 %0, %1, %2 sc1" : "=v"(v) : "v"(word), "s"(ws) : "memory"); }
DI void peek(u32x4& v, const char* ws, unsigned word) { asm volatile("global_load_dwordx4 %0, %1, %2 sc1" : "=v"(v) : "v"(word), "s"(ws) : "memory"); }
DI bool differs(const u32x2& v, u64 n) { return __builtin_amdgcn_ballot_w64(__builtin_bit_cast(u64, v) != n) != 0; }
DI bool differs(const u32x4& v, u64 n) {
    return (__builtin_amdgcn_ballot_w64(__builtin_bit_cast(u64, u32x2{v[0], v[1]}) != n) | __builtin_amdgcn_ballot_w64(__builtin_bit_cast(u64, u32x2{v[2], v[3]}) != n)) != 0;
}
template <int N, class V> DI void seen(V& v, const char* ws, unsigned word, u64 nonce) {
    asm volatile("s_waitcnt vmcnt(%1)" : "+v"(v) : "n"(N) : "memory");
    if (__builtin_expect(differs(v, nonce), 0)) {
        unsigned spins = 0;
#pragma unroll 1
        do {
            __builtin_amdgcn_s_sleep(PSLEEP);
            peek(v, ws, word);
            asm volatile("s_waitcnt vmcnt(0)" : "+v"(v) :: "memory");
        } while (differs(v, nonce) && ++spins < SPIN_CAP);
    }
}

DI void mfma_pair(int isv, u32x4 xf, u32x4 w0, u32x4 w1, f32x16& acc0, f32x16& acc1) {
    asm("s_cmp_lg_u32 %5, 0\n\ts_cbranch_scc1 1f\n\t"
        "v_mfma_f32_32x32x16_bf16 %0, %3, %2, %0\n\tv_mfma_f32_32x32x16_bf16 %1, %4, %2, %1\n\ts_branch 2f\n"
        "1:\n\tv_mfma_f32_32x32x16_bf16 %0, %2, %3, %0\n\tv_mfma_f32_32x32x16_bf16 %1, %2, %4, %1\n2:"
        : "+v"(acc0), "+v"(acc1) : "v"(xf), "v"(w0), "v"(w1), "s"(isv) : "scc");
}
DI void pin(u32x4& a, u32x4& b, u32x4& c, u32x4& d) { asm volatile("" : "+v"(a), "+v"(b), "+v"(c), "+v"(d) :: "memory"); }

DI void set_reference(u32x4& q1, float nm, int hi) {
    const unsigned a = __float_as_uint(nm) & 0xFFFF0000u;
    const float r1 = nm - __uint_as_float(a);
    const unsigned b = __float_as_uint(r1) & 0xFFFF0000u;
    const float r2 = r1 - __uint_as_float(b);
    q1[2] = hi ? (a >> 16) | b : q1[2]; q1[3] = hi ? __float_as_uint(r2) >> 16 : q1[3];
}

struct KV { u32x4 k0, k1, v0, v1; };

DI void ref_post(int wid, int lane, const float (&m)[4]) {
#pragma unroll
    for (int t = 0; t < 4; ++t) *(LAS float*)(size_t)(L_REF + ((wid + t) & 3) * 512 + (lane & 31) * 16 + wid * 4) = both_max(m[t]);
}
DI void ref_take(int wid, int lane, u32x4 (&q)[4][2], float offset) {
#pragma unroll
    for (int t = 0; t < 4; ++t) {
        const f32x4 m = __builtin_bit_cast(f32x4, lds128(L_REF + ((wid + t) & 3) * 512 + (lane & 31) * 16));
        set_reference(q[t][1], -(max3(max3(m[0], m[1], m[2]), m[3], m[3]) + offset), lane >> 5);
    }
}

DI void combine_post(int wid, int lane, const f32x16& o1, const f32x16& o2, const f32x16& o3) {
    const f32x16* ot[3] = {&o1, &o2, &o3};
#pragma unroll
    for (int t = 0; t < 3; ++t) {
        const unsigned at = L_COMB + (((wid + t + 1) & 3) * 3 + t) * 4096 + lane * 16;
#pragma unroll
        for (int g = 0; g < 4; ++g) sts128(at + g * 1024, quad(*ot[t], g));
    }
}
DI bool combine_take(int wid, int lane, f32x16 o, u32x4& f0, u32x4& f1) {
#pragma unroll
    for (int v = 0; v < 12; ++v) {
        const u32x4 p = lds128(L_COMB + wid * 12288 + v * 1024 + lane * 16);
#pragma unroll
        for (int i = 0; i < 4; ++i) o[4 * (v & 3) + i] += __uint_as_float(p[i]);
    }
    const float l = upper_val(o[12]);
    o *= __builtin_amdgcn_rcpf(l);
    float chk = 0.f;
#pragma unroll
    for (int i = 0; i < 16; ++i) chk = fmaf(o[i], 0.f, chk);
    f0 = pack_lo(o); f1 = pack_hi(o);
    if (lane >> 5) { f1[2] = 0; f1[3] = 0; }
    return !(l < L_LIMIT) || !(chk == 0.f);
}

DI void stage_out(unsigned img, int wid, int lane, const f32x16& r) {
    const u32x4 lo = pack_lo(r), up = pack_hi(r);
    const unsigned wr = img + (lane & 31) * 272 + wid * 64 + (lane >> 5) * 8;
    sts64(wr, u32x2{lo[0], lo[1]}); sts64(wr + 16, u32x2{lo[2], lo[3]}); sts64(wr + 32, u32x2{up[0], up[1]}); sts64(wr + 48, u32x2{up[2], up[3]});
}
DI void flush_out(unsigned img, int wid, int lane, int h, int qt, int br, u16* out) {
    const unsigned width = h >= 12 ? 128 : 256, colb = (lane & 15) * 16;
    const rsrc_t rd = make_rsrc((char*)out + (size_t)qt * 32 * (2 * XROW) + br * XROW + (h >> 2) * 256, 31 * 2 * XROW + width);
#pragma unroll
    for (int i = 0; i < 2; ++i) {
        const unsigned row = wid * 8 + i * 4 + (lane >> 4);
        st128_async(lds128(img + row * 272 + colb), rd, colb < width ? row * (2 * XROW) + colb : OOB, 0);
    }
}

__global__ __launch_bounds__(256, 1) void fused(const u16*, const u16*, const u16*, const u16*, const u16*, const u16*, const u16*, u16*, char*) {
    u64x8 args;
    u64 arg_ws;
    const auto arg_base = __builtin_amdgcn_kernarg_segment_ptr();
    asm volatile("s_load_dwordx16 %0, %2, 0x0\n\ts_load_dwordx2 %1, %2, 0x40" : "=&s"(args), "=&s"(arg_ws) : "s"(arg_base) : "memory");
    SBAR();
    STAMP(0);
    const int wid = __builtin_amdgcn_readfirstlane(threadIdx.x >> 6);
    const int blk = blockIdx.x, xcd = blk & 7, bi = blk >> 3;
    const int h = (xcd & 1) * 8 + (bi & 7), rb = (xcd >> 1) * 4 + (bi >> 3), tile = rb * 4 + wid;
    const int rot = ((bi >> 3) + (bi & 7)) % 3;
    const int rotb = (2 * (bi >> 3) + (bi & 7)) % 7;
    auto chunk_b = [&](int p) { const int v = p + rotb; return v >= 7 ? v - 7 : v; };
    auto chunk_at = [&](int p) { const int v = p % 3 + rot; return p == 6 ? 6 : p - p % 3 + (v >= 3 ? v - 3 : v); };
    u64 epoch, code_pc;
    unsigned code_probe;
    u64 nonce;
    const f32x16 zero = {};
    u32x4 q[4][2], eq[4];
    KV a, b, c, d;
    char* ws;
    rsrc_t rws;
    const unsigned kv_at = WS_KVA + (h * 64 + wid * 16) * 4096;
#define LDKV(X, j) do { const unsigned s_ = kv_at + (j) * 4096; ld128_async_dev<3072>(X.v1, rws, voff, s_); ld128_async_dev<2048>(X.v0, rws, voff, s_); \
                        ld128_async_dev<1024>(X.k1, rws, voff, s_); ld128_async_dev(X.k0, rws, voff, s_); } while (0)
    {
        const int lane = lane_id(), hi = lane >> 5, r32 = lane & 31, q4 = (lane & 15) >> 2;
        const unsigned voff = lane * 16, col = 32 * ((lane >> 4) & 1) + 8 * (lane & 3);
        const int isv = h & 1, row8 = lane >> 3;
        u32x4 xf[28];
        f32x16 acc0 = {}, acc1 = {};
        u32x4 pw;
        {
            const unsigned piece = (lane & 7) ^ (((row8 >> 1) & 1) << 2);
            unsigned vw = (16 * wid + row8) * WROW + (1 + isv) * XROW + (h >> 1) * 112 + ((piece < 7 ? piece : 6) << 4);
            unsigned vx0 = (tile * 32 + row8) * XROW + (((lane & 7) ^ (lane >> 4)) << 4), vx1 = (tile * 32 + 8 + row8) * XROW + (((lane & 7) ^ (4 + (lane >> 4))) << 4);
            int rot0 = rot;
            asm volatile("s_waitcnt lgkmcnt(0)" : "+s"(args), "+s"(arg_ws), "+v"(vw), "+v"(vx0), "+v"(vx1), "+s"(rot0) : "s"(arg_base) : "memory");
            const rsrc_t rxa = make_rsrc((const void*)args[0], X_BYTES), rwa = make_rsrc((const void*)args[1], W_BYTES);
            auto req_x = [&](rsrc_t rx, int slot, unsigned k, int i) { dma16(rx, L_X + slot * 16384 + wid * 4096 + i * 1024, (i & 1) ? vx1 : vx0, k * 128 + (i >> 1) * 16 * XROW); };
            auto req_w = [&](int ch, int i) { dma16(rwa, L_WKV + ch * 8192 + wid * 2048 + i * 1024, vw, (chunk_at(ch) * 64 + 8 * i) * WROW); };
            auto issue = [&](int ch) {
                req_w(ch, 0); req_w(ch, 1);
#pragma unroll
                for (int i = 0; i < 4; ++i) req_x(rxa, ch & 3, chunk_at(ch), i);
            };
            issue(0);
            SBAR();
            ws = (char*)arg_ws;
            rws = make_rsrc(ws, WS_END);
            const rsrc_t rxb = make_rsrc((const void*)args[3], X_BYTES);
            epoch_request(epoch, ws + WS_EPOCH);
            {
                unsigned code_len;
                asm volatile("s_getpc_b64 %0\n.Lfused_code_begin:\n\ts_mov_b32 %1, .Lfused_code_end-.Lfused_code_begin" : "=s"(code_pc), "=s"(code_len));
                const unsigned at = (bi * 16 + wid * 4 + (threadIdx.x & 3)) * 128;
                asm volatile("global_load_dword %0, %1, %2" : "=v"(code_probe) : "v"(at < code_len ? at : 0), "s"(code_pc) : "memory");
            }
            int ub = blk, uw = wid;
            asm volatile("" : "+s"(ub), "+s"(uw));
            const int ug = ub & 31, ucg = ub >> 5, ubr = (ub >> 5) & 1, uh = (ub >> 1) & 15, uhalf = ub & 1;
            const unsigned mask_o = (unsigned)((191 - ub) >> 31), mask_w = ~mask_o & (unsigned)((ug - 28) >> 31), mask_q = mask_w & (unsigned)((ucg - 2) >> 31);
            {
                const u64 wo_p = ubr ? args[6] : args[2];
                const rsrc_t rr = make_rsrc((const void*)(mask_o ? wo_p : args[4]), mask_o ? WO_BYTES : W_BYTES);
                const rsrc_t rr2 = make_rsrc((const void*)(mask_o ? wo_p : args[1]), mask_o ? WO_BYTES : W_BYTES);
                const unsigned i0 = 4 * uw + hi;
                const unsigned vw0 = (64 * (ug >> 2) + 8 * (ug & 3) + (i0 & 7) + 32 * (i0 >> 3)) * WROW + ucg * 448 + (r32 < 29 ? r32 : 28) * 16;
                const unsigned vo0 = (28 * uh + 2 * uw + hi) * XROW + uhalf * 448 + (r32 < 28 ? r32 : 27) * 16;
#pragma unroll
                for (int j = 0; j < 4; ++j) {
                    const unsigned mw = j < 2 ? mask_w : mask_q, mo = mask_o & (unsigned)((uw + 4 * j - 14) >> 31);
                    dma16(j < 2 ? rr : rr2, L_RET + ((mw & ((j >> 1) * 8192 + (2 * uw + (j & 1)) * 1024)) | (mo & ((uw + 4 * j) * 1024)) | (~(mw | mo) & (16384 + uw * 1024))),
                          (mw & (vw0 + 2 * (j & 1) * WROW)) | (mo & (vo0 + 8 * j * XROW)) | (~(mw | mo) & OOB), 0);
                }
            }
            issue(1);
            unsigned rt_at[4], rt_2nd[4], rt_dst[4], rt_v[4];
            {
                const unsigned rd = L_RET + q4 * 512 + col + hi * (2048 + (mask_w & 2048));
                const unsigned mq = (unsigned)((ucg - 2) >> 31);
                const unsigned w_first = (ucg & 1) * 672 + ((mq & (56 + ug)) | (~mq & (2 * ug + (ucg >> 2)))), o_first = (ubr * 14 + uhalf * 7) * 32 + uh * 2;
#pragma unroll
                for (int i = 0; i < 4; ++i) {
                    const int f = uw + 4 * i, s = f & 1, t = f >> 1, hl = 2 * uw + (i & 1);
                    const unsigned mw = i < 2 ? mask_w : mask_q, mo = mask_o & (unsigned)((f - 14) >> 31);
                    rt_at[i] = rd + ((mw & ((i >> 1) * 8192 + hl * 56)) | (mo & (s * 8192 + t * 64)));
                    rt_2nd[i] = rt_at[i] + 4096 - ((mask_w | -(unsigned)(s & hi)) & 2048);
                    rt_dst[i] = (mw & (i < 2 ? WS_WB + (w_first + hl * 84) * 1024 : WS_WQ + (((ucg & 1) * 8 + hl) * 28 + ug) * 1024)) | (mo & (WS_WO + (o_first + t * 32 + s) * 1024));
                    rt_v[i] = voff | (~(mw | mo) & OOB);
                    asm volatile("" : "+v"(rt_at[i]), "+v"(rt_2nd[i]), "+s"(rt_dst[i]), "+v"(rt_v[i]));
                }
            }
            const unsigned m0w = (unsigned)((uw - 1) >> 31);
            unsigned rt_word = (m0w & mask_w & (WS_FW + (((ucg & 1) * 3 + (ucg >> 1)) * 28 + ug) * 8)) | (m0w & mask_o & (WS_FWO + ((ubr * 2 + uhalf) * 16 + uh) * 8))
                             | (~(m0w & (mask_w | mask_o)) & (WS_VOID + (ub * 4 + uw) * 8));
            unsigned w0 = L_WKV + (32 * hi + q4) * 128 + (col ^ ((q4 >> 1) << 6)), w1 = L_WKV + (32 * hi + q4) * 128 + ((col + 56) ^ ((q4 >> 1) << 6));
            unsigned xo[4];
#pragma unroll
            for (int s = 0; s < 4; ++s) xo[s] = L_X + wid * 4096 + r32 * 128 + (((4 * hi + s) ^ ((r32 >> 1) & 7)) << 4);
            const unsigned pw_word = WS_FW + (h >> 3) * 672 + (lane < 42 ? lane : 41) * 16;
            asm volatile("" : "+s"(rt_word), "+v"(w0), "+v"(w1), "+v"(xo[0]), "+v"(xo[1]), "+v"(xo[2]), "+v"(xo[3]), "+v"(acc0), "+v"(acc1));
            issue(2);
            asm volatile("s_waitcnt lgkmcnt(0)" : "+s"(epoch) :: "memory");
            nonce = epoch + 1;
            STAMP(1);
            const unsigned wb_at = WS_WB + h * 86016;
            auto req_kvb = [&](int ch, int j) { dma16_dev(rws, l_kvb(ch) + wid * 2048 + j * 1024, voff, wb_at + chunk_b(ch) * 8192 + wid * 2048 + j * 1024); };
            auto req_qb = [&](int ch) { dma16_dev(rws, L_QB + ch * 4096 + wid * 1024, voff, wb_at + 57344 + chunk_b(ch) * 4096 + wid * 1024); };
            auto req_wq = [&](int j) { dma16_dev(rws, l_wq(j) + wid * 1024, voff, WS_WQ + h * 28672 + chunk_at(j) * 4096 + wid * 1024); };
            auto xb_slot = [](int ch) { return (ch + 3) & 3; };
            u32x4 wf[2][8];
#pragma unroll
            for (int p = 0; p <= 7; ++p) {
                if (p < 7) {
                    if (p == 0) wait_vm<12>(); else if (p == 1) wait_vm<22>(); else if (p == 2) wait_vm<18>(); else if (p == 3) wait_vm<19>();
                    else if (p == 4) wait_vm<17>(); else if (p == 5) wait_vm<15>(); else wait_vm<14>();
                    barrier();
                    if (p == 0) asm volatile("" :: "v"(code_probe), "s"(code_pc));
                    if (p == 0) { STAMP(2); }
                    if (p == 6) { STAMP(3); }
#pragma unroll
                    for (int s = 0; s < 4; ++s) xf[4 * p + s] = lds128(xo[s] + (p & 3) * 16384);
                }
#pragma unroll
                for (int s = 0; s < 4; ++s) {
                    if (p < 7) {
                        const unsigned o = p * 8192 + s * 1024;
                        wf[p & 1][2 * s] = tr_frag(w0 + o, w0 + o + 512); wf[p & 1][2 * s + 1] = tr_frag(w1 + o, w1 + o + 512);
                    }
                    SBAR();
                    if (p > 0) mfma_pair(isv, xf[4 * p - 4 + s], wf[(p - 1) & 1][2 * s], wf[(p - 1) & 1][2 * s + 1], acc0, acc1);
                    SBAR();
                    if (p == 0 && s == 0) {
#pragma unroll
                        for (int i = 0; i < 4; ++i) st128_async(tr_frag(rt_at[i], rt_2nd[i]), rws, rt_v[i], rt_dst[i]);
                        issue(3);
                    }
                    if (p == 2 && s == 0) publish(ws, rt_word, nonce);
                    if (p < 7 && s == 1) pin(xf[4 * p], xf[4 * p + 1], xf[4 * p + 2], xf[4 * p + 3]);
                    if (p <= 2) {
                        if (s == 0) { req_w(p + 4, 0); req_w(p + 4, 1); }
                        if (s == 1 || s == 2) { req_x(rxa, p & 3, chunk_at(p + 4), 2 * s - 2); req_x(rxa, p & 3, chunk_at(p + 4), 2 * s - 1); }
                    }
                    if (p == 6 && s == 0) peek(pw, ws, pw_word);
                    if (p >= 3 && p <= 6 && s >= 2) { req_x(rxb, xb_slot(p - 3), chunk_b(p - 3), 2 * s - 4); req_x(rxb, xb_slot(p - 3), chunk_b(p - 3), 2 * s - 3); }
                    if (p == 5 && s == 3) {
                        const rsrc_t rp = make_rsrc((const char*)args[5] + h * 1568, 1568);
                        dma16(rp, L_WPHI, voff, 0); dma16(rp, L_WPHI + 1024, voff, 1024);
                    }
                    if (p == 7) {
                        if (s == 0) { seen<4>(pw, ws, pw_word, nonce); lds_bar(); }
                        req_kvb(s, 0); req_kvb(s, 1); req_qb(s);
                    }
                    SBAR();
                }
            }
            asm volatile("s_nop 3" : "+v"(acc0), "+v"(acc1));
            {
                u32x4 f[4] = {pack_lo(acc0), pack_hi(acc0), pack_lo(acc1), pack_hi(acc1)};
                const bool all = isv && r32 >= 28, tail = isv ? all : (bool)hi;
                const unsigned va = r32 == 28 ? 0x3F803F80u : 0u, v2 = isv ? va : 0x3F803F80u, v3 = isv ? va : 0x00003F80u;
#pragma unroll
                for (int i = 0; i < 4; ++i) {
                    f[i][0] = all ? va : f[i][0]; f[i][1] = all ? va : f[i][1];
                    if (i & 1) { f[i][2] = tail ? v2 : f[i][2]; f[i][3] = tail ? v3 : f[i][3]; }
                    else       { f[i][2] = all ? va : f[i][2]; f[i][3] = all ? va : f[i][3]; }
                }
                const unsigned at = WS_KVA + ((h & 14) * 64 + tile) * 4096 + isv * 2048;
                st128_async(f[0], rws, voff, at); st128_async<1024>(f[1], rws, voff, at); st128_async(f[2], rws, voff, at + 64 * 4096); st128_async<1024>(f[3], rws, voff, at + 64 * 4096);
            }
            STAMP(4);
            if (wid == 0 && lane == 0) *(LAS unsigned*)(size_t)L_FLAG = 0;
            u32x2 pk_;
            const unsigned pk_word = WS_FK + ((h >> 1) * 64 + wid * 16) * 16 + r32 * 8;
            f32x16 aq = {}, ak = {}, av = {};
            unsigned qba = L_QB + voff;
            asm volatile("s_nop 1" : "+v"(aq), "+v"(ak), "+v"(av), "+v"(qba));
            {
                u32x4 wg[28][3], xg[28];
#pragma unroll
                for (int i = -3; i < 28; ++i) {
                    const int n = i + 3, p = n >> 2, s = n & 3;
                    if (n < 28) {
                        if (s == 0) {
                            if (p == 0) wait_vm<13>(); else if (p == 1) wait_vm<17>(); else if (p == 2) wait_vm<21>(); else if (p == 3) wait_vm<25>();
                            else if (p == 4) wait_vm<14>(); else if (p == 5) wait_vm<8>(); else wait_vm<8>();
                            barrier();
                            if (p == 4) { publish(ws, WS_FK + (((h >> 1) * 64 + tile) * 2 + isv) * 8, nonce); STAMP(5); }
                        }
                    }
                    const unsigned at = l_kvb(p < 7 ? p : 0) + s * 2048 + voff;
                    SBAR();
                    if (i >= 0) mfma_v(ak, wg[i][0], xg[i]);
                    SBAR();
                    if (n < 28) { wg[n][0] = lds128(at); if (s < 2) xg[4 * p + 2 * s] = lds128(xo[2 * s] + xb_slot(p) * 16384); }
                    if (p <= 2 && s == 0) req_kvb(p + 4, 0);
                    if (p <= 2 && s == 3) req_x(rxb, xb_slot(p), chunk_b(p + 4), 1);
                    SBAR();
                    if (i >= 0) mfma_v(av, xg[i], wg[i][1]);
                    SBAR();
                    if (n < 28) { wg[n][1] = lds128(at + 1024); if (s < 2) xg[4 * p + 2 * s + 1] = lds128(xo[2 * s + 1] + xb_slot(p) * 16384); }
                    if (p <= 2 && s == 0) req_kvb(p + 4, 1);
                    if (p <= 2 && s == 3) req_x(rxb, xb_slot(p), chunk_b(p + 4), 2);
                    if (p == 5) req_wq(2 * s);
                    SBAR();
                    if (i >= 0) mfma_v(aq, wg[i][2], xg[i]);
                    SBAR();
                    if (n < 28) wg[n][2] = lds128(qba + n * 1024);
                    if (p <= 2 && s == 1) req_qb(p + 4);
                    if (p <= 2 && s == 2) { pin(xg[4 * p], xg[4 * p + 1], xg[4 * p + 2], xg[4 * p + 3]); req_x(rxb, xb_slot(p), chunk_b(p + 4), 0); }
                    if (p <= 2 && s == 3) req_x(rxb, xb_slot(p), chunk_b(p + 4), 3);
                    if (p == 5 && s < 3) req_wq(2 * s + 1);
                    SBAR();
                }
            }
            STAMP(6);
            u32x4 wp[2];
            {
                const unsigned fc = 16 * ((lane >> 4) & 1) + 4 * (lane & 3);
                const unsigned a0 = L_WPHI + (4 * hi + q4) * 56 + (fc < 28 ? fc : 24) * 2;
                wp[0] = tr_frag(a0, a0 + 8 * 56); wp[1] = tr_frag(a0 + 16 * 56, a0 + (hi ? 20 : 24) * 56);
            }
            wait_vm<0>();
            barrier();
            u32x4 wqf[28];
#pragma unroll
            for (int i = 0; i < 6; ++i) wqf[i] = lds128(l_wq(i >> 2) + (i & 3) * 1024 + voff);
            asm volatile("s_nop 1" : "+v"(aq), "+v"(ak), "+v"(av));
            STAMP(7);
            f32x16 pkk, pqq, qa;
            {
                u32x4 f0 = pack_lo(ak), f1 = pack_hi(ak);
                if (hi) { f1[2] = 0; f1[3] = 0; }
                asm volatile("" : "+v"(f0), "+v"(f1));
                mfma_v0(pkk, wp[0], f0); mfma_v(pkk, wp[1], f1);
            }
            u32x4 g0 = pack_lo(aq), g1 = pack_hi(aq);
            if (hi) { g1[2] = 0; g1[3] = 0; }
            asm volatile("" : "+v"(g0), "+v"(g1));
#define QSTEP(i) do { SBAR(); if (((i) & 3) == 0) pin(wqf[i], wqf[(i) + 1], wqf[(i) + 2], wqf[(i) + 3]); \
                      if ((i) == 0) mfma_v0(qa, wqf[0], xf[0]); else mfma_v(qa, wqf[i], xf[i]); \
                      SBAR(); if ((i) + 6 < 28) wqf[(i) + 6] = lds128(l_wq(((i) + 6) >> 2) + (((i) + 6) & 3) * 1024 + voff); SBAR(); } while (0)
            QSTEP(0);
            u32x4 v0 = pack_lo(av);
            QSTEP(1);
            u32x4 v1 = pack_hi(av);
            if (r32 >= 28) { const unsigned v = r32 == 28 ? 0x3F803F80u : 0u; v0 = u32x4{v, v, v, v}; v1 = v0; }
            asm volatile("" : "+v"(pkk), "+v"(v0), "+v"(v1));
            QSTEP(2);
            const unsigned img = L_PHI + wid * 4096;
            {
                f32x16 ep, en;
                const float hx = max16(pkk), hn = min16(pkk);
                QSTEP(3);
                const float mx = both_max(hx) * LOG2E, mn = both_min(hn) * LOG2E;
                const float live = hi ? 0.f : 1.f;
                float sp = 0.f, sn = 0.f;
                float ap = fmaf(pkk[15], LOG2E, -mx), an = fmaf(pkk[15], -LOG2E, mn);
#pragma unroll
                for (int i = 0; i < 16; ++i) {
                    QSTEP(4 + i);
                    ep[15 - i] = __builtin_amdgcn_exp2f(ap); en[15 - i] = __builtin_amdgcn_exp2f(an);
                    SBAR();
                    if (i < 15) { ap = fmaf(pkk[14 - i], LOG2E, -mx); an = fmaf(pkk[14 - i], -LOG2E, mn); }
                    if (i == 1) { sp = ep[15]; sn = en[15]; }
                    if (i > 1) { sp += ep[16 - i]; sn += en[16 - i]; }
                    if (i == 4) { sp *= live; sn *= live; }
                    if (i == KP_PEEK) peek(pk_, ws, pk_word);
                    if (i == KP_SEEN) { seen<0>(pk_, ws, pk_word, nonce); STAMP(8); LDKV(a, 0); }
                }
                QSTEP(20);
                const float rp = __builtin_amdgcn_rcpf(both_sum(sp + ep[0])), rn = __builtin_amdgcn_rcpf(both_sum(sn + en[0]));
                const float rp3 = rp * live, rn3 = rn * live;
                const unsigned wr = img + (r32 & 15) * 136 + (r32 >> 4) * 16384 + hi * 8;
#pragma unroll
                for (int g = 0; g < 4; ++g) {
                    const float u = g == 3 ? rp3 : rp, v = g == 3 ? rn3 : rn;
                    QSTEP(21 + 2 * g);
                    sts64(wr + g * 16, u32x2{pk(ep[4 * g] * u, ep[4 * g + 1] * u), pk(ep[4 * g + 2] * u, ep[4 * g + 3] * u)});
                    if (g < 3) QSTEP(22 + 2 * g);
                    sts64(wr + 64 + g * 16, u32x2{pk(en[4 * g] * v, en[4 * g + 1] * v), pk(en[4 * g + 2] * v, en[4 * g + 3] * v)});
                }
            }
#undef QSTEP
            u32x4 pf[4];
            {
                const unsigned rd = img + (4 * hi + q4) * 136 + col;
#pragma unroll
                for (int i = 0; i < 4; ++i) pf[i] = tr_frag(rd + (i >> 1) * 64 + (i & 1) * 16384, rd + (i >> 1) * 64 + (i & 1) * 16384 + 8 * 136);
            }
            SBAR(); mfma_v0(pqq, wp[0], g0); SBAR(); LDKV(b, 1); SBAR(); mfma_v(pqq, wp[1], g1); SBAR(); LDKV(c, 2);
            asm volatile("" : "+v"(qa));
            {
                qa *= QSCALE;
                u32x4 q0 = pack_lo(qa), q1 = pack_hi(qa);
                if (hi) { q1[2] = 0; q1[3] = 0; }
                sts128(L_QX + wid * 2048 + voff, q0); sts128(L_QX + wid * 2048 + 1024 + voff, q1);
            }
            f32x16 kv0, kv1, sc[4];
            SBAR(); mfma_v0(kv0, pf[0], v0); SBAR();
            asm volatile("" : "+v"(pqq));
            const float qhx = max16(pqq);
            SBAR(); mfma_v(kv0, pf[1], v1); SBAR();
            lds_bar();
#pragma unroll
            for (int t = 0; t < 4; ++t) { const unsigned at = L_QX + ((wid + t) & 3) * 2048 + voff; q[t][0] = lds128(at); q[t][1] = lds128(at + 1024); }
            const float qhn = min16(pqq);
            SBAR(); mfma_v0(kv1, pf[2], v0); SBAR();
            const float qmx = both_max(qhx) * LOG2E, qmn = both_min(qhn) * LOG2E;
            SBAR(); mfma_v(kv1, pf[3], v1); SBAR();
            landed<8>(a.k0, a.k1, a.v0, a.v1);
            f32x16 eqp, eqn;
#pragma unroll
            for (int i = 0; i < 8; ++i) {
                const float a0 = fmaf(pqq[2 * i], LOG2E, -qmx), a1 = fmaf(pqq[2 * i + 1], LOG2E, -qmx), n0 = fmaf(pqq[2 * i], -LOG2E, qmn), n1 = fmaf(pqq[2 * i + 1], -LOG2E, qmn);
                SBAR();
                if (i & 1) mfma_v(sc[i >> 1], a.k1, q[i >> 1][1]); else mfma_v0(sc[i >> 1], a.k0, q[i >> 1][0]);
                SBAR();
                eqp[2 * i] = __builtin_amdgcn_exp2f(a0); eqp[2 * i + 1] = __builtin_amdgcn_exp2f(a1); eqn[2 * i] = __builtin_amdgcn_exp2f(n0); eqn[2 * i + 1] = __builtin_amdgcn_exp2f(n1);
                if (i == 0) asm volatile("" : "+v"(kv0));
                if (i == 4) asm volatile("" : "+v"(kv1));
                sts128(L_RED + wid * 8192 + i * 1024 + voff, quad(i < 4 ? kv0 : kv1, i & 3));
                SBAR();
            }
            eq[0] = pack_lo(eqp); eq[1] = pack_hi(eqp); eq[2] = pack_lo(eqn); eq[3] = pack_hi(eqn);
            asm volatile("" : "+v"(eq[0]), "+v"(eq[1]), "+v"(eq[2]), "+v"(eq[3]), "+v"(sc[0]), "+v"(sc[1]), "+v"(sc[2]), "+v"(sc[3]));
            {
                const float m[4] = {max16(sc[0]), max16(sc[1]), max16(sc[2]), max16(sc[3])};
                ref_post(wid, lane, m);
            }
            lds_bar();
            {
                ref_take(wid, lane, q, REF_OFFSET);
#pragma unroll
                for (int i = 0; i < 2; ++i) {
                    const unsigned off = (2 * wid + i) * 1024 + voff;
                    f32x4 u = __builtin_bit_cast(f32x4, lds128(L_RED + off));
#pragma unroll
                    for (int v = 1; v < 4; ++v) u += __builtin_bit_cast(f32x4, lds128(L_RED + v * 8192 + off));
                    st128_async(__builtin_bit_cast(u32x4, u), rws, off, WS_KVP + (h * 16 + rb) * 8192);
                }
                STAMP(9);
            }
        }
    }
    const int qt = rb * 4 + (h & 3), nt = (h & 12) + wid;
    const bool live = nt < 14;
    u32x4 wob_f[32], woa_f[32], f0, f1;
    const unsigned wo_at = WS_WO + (live ? nt : 0) * 32768;
    const unsigned o_mine = (tile * 16 + h) * 2048, o_need = (qt * 16 + wid * 4) * 2048;
    {
        const int lane = lane_id(), hi = lane >> 5, r32 = lane & 31;
        const unsigned voff = lane * 16;
        STAMP(10);
        f32x16 o0 = {}, o1 = {}, o2 = {}, o3 = {};
        f32x16 sa, sb; u32x4 pa, pb = {};
        float e12 = 0.f, e13 = 0.f, e14 = 0.f, e15 = 0.f;
        d.v1 = u32x4{};
#define QK0(S, K, Q) asm volatile("v_mfma_f32_32x32x16_bf16 %0, %1, %2, 0" : "=&v"(S) : "v"(K), "v"(Q))
#define QK1(S, K, Q) asm volatile("v_mfma_f32_32x32x16_bf16 %0, %1, %2, %0" : "+v"(S) : "v"(K), "v"(Q))
        asm volatile("s_nop 1" : "+v"(q[0][1]), "+v"(q[1][1]), "+v"(q[2][1]), "+v"(q[3][1]));
        QK0(sa, a.k0, q[0][0]); QK1(sa, a.k1, q[0][1]);
        asm volatile("s_nop 11" : "+v"(sa));
#define EX(S, i) S[i] = __builtin_amdgcn_exp2f(S[i])
#define STEP(NEXT, SC, SN, KN0, KN1, QN0, QN1, OP, VP1, OC, VC0) do { \
    if (NEXT) QK0(SN, KN0, QN0); SBAR(); \
    pb[2] = pk(e12, e13); SBAR(); EX(SC, 0); SBAR(); pb[3] = pk(e14, e15); SBAR(); EX(SC, 1); EX(SC, 2); EX(SC, 3); SBAR(); \
    OP = mfma(VP1, pb, OP); SBAR(); \
    EX(SC, 4); EX(SC, 5); SBAR(); pa[0] = pk(SC[0], SC[1]); SBAR(); EX(SC, 6); SBAR(); pa[1] = pk(SC[2], SC[3]); SBAR(); EX(SC, 7); SBAR(); \
    if (NEXT) QK1(SN, KN1, QN1); SBAR(); \
    EX(SC, 8); SBAR(); pa[2] = pk(SC[4], SC[5]); SBAR(); EX(SC, 9); SBAR(); pa[3] = pk(SC[6], SC[7]); SBAR(); EX(SC, 10); EX(SC, 11); SBAR(); \
    OC = mfma(VC0, pa, OC); SBAR(); \
    e12 = __builtin_amdgcn_exp2f(SC[12]); SBAR(); pb[0] = pk(SC[8], SC[9]); SBAR(); e13 = __builtin_amdgcn_exp2f(SC[13]); SBAR(); pb[1] = pk(SC[10], SC[11]); SBAR(); \
    e14 = __builtin_amdgcn_exp2f(SC[14]); e15 = __builtin_amdgcn_exp2f(SC[15]); SBAR(); } while (0)
#define TILE(J, CUR, PREV, NXT, NW, SEAM, MORE) do { \
    STEP(true, sa, sb, CUR.k0, CUR.k1, q[1][0], q[1][1], o3, PREV.v1, o0, CUR.v0); \
    if (J + 3 <= 15) LDKV(PREV, J + 3); \
    SEAM; SBAR(); \
    STEP(true, sb, sa, CUR.k0, CUR.k1, q[2][0], q[2][1], o0, CUR.v1, o1, CUR.v0); \
    MORE(0, 3); SBAR(); \
    STEP(true, sa, sb, CUR.k0, CUR.k1, q[3][0], q[3][1], o1, CUR.v1, o2, CUR.v0); \
    MORE(3, 6); \
    if (J < 15) landed<NW>(NXT.k0, NXT.k1, NXT.v0, NXT.v1); \
    SBAR(); \
    STEP(J < 15, sb, sa, NXT.k0, NXT.k1, q[0][0], q[0][1], o2, CUR.v1, o3, CUR.v0); \
    MORE(6, 8); SBAR(); } while (0)
        u32x4 pl[2], sf[4];
        u32x2 pp;
        const unsigned pp_word = lane < 16 ? WS_FP + (h * 4 + (rb >> 2)) * 128 + lane * 8 : WS_FWO + (((lane >> 5) & 1) * 2 + (live ? nt / 7 : 1)) * 128 + (lane & 15) * 8;
        const unsigned ps_word = WS_FS + h * 512 + lane * 8;
        auto slice_load = [&]() {
            const unsigned at = WS_KVP + h * 16 * 8192 + tile * 128, v = (lane >> 3) * 8192 + (lane & 7) * 16;
            ld128_async_dev(pl[0], rws, v, at); ld128_async_dev(pl[1], rws, v, at + 8 * 8192);
        };
        auto slice_sum = [&]() {
            asm volatile("s_waitcnt vmcnt(4)" : "+v"(pl[0]), "+v"(pl[1]) :: "memory");
            float u[4];
#pragma unroll
            for (int e = 0; e < 4; ++e) {
                u[e] = both_sum(__uint_as_float(pl[0][e]) + __uint_as_float(pl[1][e]));
                auto r = __builtin_amdgcn_permlane16_swap(__float_as_uint(u[e]), __float_as_uint(u[e]), false, false);
                u[e] = __uint_as_float(r[0]) + __uint_as_float(r[1]);
                u[e] += __uint_as_float(__builtin_amdgcn_update_dpp(0u, __float_as_uint(u[e]), 0x128, 0xf, 0xf, false));
            }
            const u32x2 v = {pk(u[0], u[1]), pk(u[2], u[3])};
            const unsigned at = WS_ST + h * 4096 + (rb >> 2) * 1024 + ((rb & 1) * 32 + wid * 8) * 16 + ((rb >> 1) & 1) * 8;
            asm volatile("buffer_store_dwordx2 %0, %1, %2, %3 offen sc1" ::"v"(v), "v"(lane < 8 ? voff : OOB), "s"(rws), "s"(at) : "memory");
        };
        auto state_load = [&]() {
#pragma unroll
            for (int i = 0; i < 4; ++i) ld_frag<false>(sf[i], rws, voff, WS_ST + h * 4096, i);
        };
        auto wo_load = [&](int n, int first, int last) {
#pragma unroll
            for (int i = 0; i < 8; ++i) if (i >= first && i < last) ld_frag<true>(wob_f[8 * n + i], rws, voff, wo_at + 14 * 32768, 8 * n + i);
        };
        auto wa_load = [&](int n, int first, int last) {
#pragma unroll
            for (int i = 0; i < 8; ++i) if (i >= first && i < last) {
                if (n == 3) ld_frag<true>(woa_f[8 * n + i], rws, voff, wo_at, 8 * n + i); else ld_frag<false>(woa_f[8 * n + i], rws, voff, wo_at, 8 * n + i);
            }
        };
        f32x16 np, nn;
        auto start_b = [&]() {
            landed<4>(sf[0], sf[1], sf[2], sf[3]);
            if (r32 == 29) {
                const unsigned tail = hi ? 0u : 0x3F803F80u;
                sf[0] = u32x4{0x3F803F80u, 0x3F803F80u, 0x3F803F80u, 0x3F803F80u}; sf[2] = sf[0];
                sf[1] = u32x4{0x3F803F80u, 0x3F803F80u, tail, tail}; sf[3] = sf[1];
            }
            asm volatile("" : "+v"(sf[0]), "+v"(sf[1]), "+v"(sf[2]), "+v"(sf[3]));
            mfma_v0(np, sf[0], eq[0]); mfma_v(np, sf[1], eq[1]); mfma_v0(nn, sf[2], eq[2]); mfma_v(nn, sf[3], eq[3]);
        };
        auto finish_b = [&](int first, int last) {
            if (first != 0) return;
            asm volatile("" : "+v"(np), "+v"(nn));
            const float rp_ = __builtin_amdgcn_rcpf(np[13]), rn_ = __builtin_amdgcn_rcpf(nn[13]);
            const float inv = __builtin_amdgcn_rcpf(fmaf(np[12], rp_, fmaf(nn[12], rn_, 1e-6f)));
            const float ap = upper_val(rp_ * inv), an = upper_val(rn_ * inv);
            f32x16 o;
#pragma unroll
            for (int i = 0; i < 16; ++i) o[i] = fmaf(np[i], ap, nn[i] * an);
            u32x4 g0 = pack_lo(o), g1 = pack_hi(o);
            if (hi) { g1[2] = 0; g1[3] = 0; }
            st128_async(g0, rws, voff, WS_OB + o_mine); st128_async<1024>(g1, rws, voff, WS_OB + o_mine);
        };
#define NONE(x, y) (void)0
#define WB0(x, y) wo_load(0, x, y)
#define WB1(x, y) wo_load(1, x, y)
#define WB2(x, y) wo_load(2, x, y)
#define WB3(x, y) wo_load(3, x, y)
#define WA0(x, y) wa_load(0, x, y)
#define WA1(x, y) wa_load(1, x, y)
#define WA2(x, y) wa_load(2, x, y)
#define WA3(x, y) wa_load(3, x, y)
        TILE(0, a, d, b, 10, (void)0, NONE);
        TILE(1, b, a, c, 11, (wait_vm<8>(), publish(ws, WS_FP + ((h * 4 + wid) * 16 + rb) * 8, nonce)), NONE);
        TSTAMP(19); TILE(2, c, b, d, 9, (void)0, NONE);
        TILE(3, d, c, a, 9, (void)0, NONE);
        TILE(4, a, d, b, 9, peek(pp, ws, pp_word), NONE);
        TSTAMP(20); TILE(5, b, a, c, 11, (seen<4>(pp, ws, pp_word, nonce), slice_load()), NONE);
        TSTAMP(21); TILE(6, c, b, d, 18, slice_sum(), WB0);
        TSTAMP(22); TILE(7, d, c, a, 25, (void)0, WB1);
        TILE(8, a, d, b, 32, (wait_vm<24>(), publish(ws, WS_FS + (h * 64 + tile) * 8, nonce)), WB2);
        TILE(9, b, a, c, 31, (void)0, WB3);
        TILE(10, c, b, d, 32, peek(pp, ws, ps_word), WA3);
        TSTAMP(23); TILE(11, d, c, a, 29, (seen<12>(pp, ws, ps_word, nonce), state_load()), NONE);
        TSTAMP(24); TILE(12, a, d, b, 23, start_b(), finish_b);
        TSTAMP(25); TILE(13, b, a, c, 16, (void)0, WA0);
        TILE(14, c, b, d, 17, (wait_vm<8>(), publish(ws, WS_FOB + (tile * 16 + h) * 8, nonce)), WA1);
        TSTAMP(26); TILE(15, d, c, a, 0, (void)0, WA2);
        pb[2] = pk(e12, e13); pb[3] = pk(e14, e15);
        o3 = mfma(d.v1, pb, o3);
#undef TILE
#undef STEP
#undef EX
#undef QK0
#undef QK1
#undef NONE
#undef WB0
#undef WB1
#undef WB2
#undef WB3
#undef WA0
#undef WA1
#undef WA2
#undef WA3
        STAMP(11);
        combine_post(wid, lane, o1, o2, o3);
        u32x2 pob;
        const unsigned pob_word = WS_FOB + (qt * 16 + wid * 4 + (lane & 3)) * 8;
        peek(pob, ws, pob_word);
        lds_bar();
        const bool bad = combine_take(wid, lane, o0, f0, f1);
        st128_async(f0, rws, voff, WS_OA + o_mine); st128_async<1024>(f1, rws, voff, WS_OA + o_mine);
        STAMP(12);
        if (__any(bad) && lane == 0) *(LAS unsigned*)(size_t)L_FLAG = 1;
        seen<2>(pob, ws, pob_word, nonce);
#pragma unroll
        for (int i = 0; i < 8; ++i) dma16_dev(rws, L_OIMG + (wid * 8 + i) * 1024, voff, WS_OB + o_need + i * 1024);
        wait_bar<8>();
        if (__builtin_amdgcn_readfirstlane(*(const LAS unsigned*)(size_t)L_FLAG)) {
            float ref[4];
#pragma unroll
            for (int t = 0; t < 4; ++t) { if (hi) { q[t][1][2] = 0; q[t][1][3] = 0; } ref[t] = -__builtin_inff(); }
#pragma unroll 1
            for (int j = 0; j < 16; ++j) {
                const u32x4 k0 = ld128_dev(rws, voff, kv_at + j * 4096), k1 = ld128_dev(rws, voff + 1024, kv_at + j * 4096);
#pragma unroll
                for (int t = 0; t < 4; ++t) { f32x16 s = mfma(k0, q[t][0], zero); s = mfma(k1, q[t][1], s); ref[t] = fmaxf(ref[t], max16(s)); }
            }
            ref_post(wid, lane, ref);
            lds_bar();
            ref_take(wid, lane, q, 0.f);
            lds_bar();
            f32x16 x[4] = {};
#pragma unroll 1
            for (int j = 0; j < 16; ++j) {
                const u32x4 k0 = ld128_dev(rws, voff, kv_at + j * 4096), k1 = ld128_dev(rws, voff + 1024, kv_at + j * 4096);
                const u32x4 v0 = ld128_dev(rws, voff + 2048, kv_at + j * 4096), v1 = ld128_dev(rws, voff + 3072, kv_at + j * 4096);
#pragma unroll
                for (int t = 0; t < 4; ++t) {
                    f32x16 s = mfma(k0, q[t][0], zero); s = mfma(k1, q[t][1], s);
#pragma unroll
                    for (int i = 0; i < 16; ++i) s[i] = __builtin_amdgcn_exp2f(s[i]);
                    x[t] = mfma(v0, pack_lo(s), x[t]); x[t] = mfma(v1, pack_hi(s), x[t]);
                }
            }
            combine_post(wid, lane, x[1], x[2], x[3]);
            lds_bar();
            combine_take(wid, lane, x[0], f0, f1);
            st128_async(f0, rws, voff, WS_OA + o_mine); st128_async<1024>(f1, rws, voff, WS_OA + o_mine);
            wait_vm<0>();
        }
        publish(ws, WS_FOA + (tile * 16 + h) * 8, nonce);
        STAMP(13);
    }
#undef LDKV
    {
        const int lane = lane_id();
        const unsigned voff = lane * 16;
#pragma unroll
        for (int i = 0; i < 32; i += 8)
            asm volatile("" : "+a"(wob_f[i]), "+a"(wob_f[i + 1]), "+a"(wob_f[i + 2]), "+a"(wob_f[i + 3]), "+a"(wob_f[i + 4]), "+a"(wob_f[i + 5]), "+a"(wob_f[i + 6]), "+a"(wob_f[i + 7]));
        f32x16 r = {};
        asm volatile("s_nop 1" : "+v"(r));
        u32x4 of[32];
        u32x2 poa;
        const unsigned poa_word = WS_FOA + (qt * 16 + wid * 4 + (lane & 3)) * 8;
        wait_vm<1>();
        barrier();
#pragma unroll
        for (int i = 0; i < 6; ++i) of[i] = lds128(L_OIMG + i * 1024 + voff);
#pragma unroll
        for (int i = 0; i < 32; ++i) {
            if (i + 6 < 32) of[i + 6] = lds128(L_OIMG + (i + 6) * 1024 + voff);
            SBAR();
            asm volatile("v_mfma_f32_32x32x16_bf16 %0, %1, %2, %0" : "+v"(r) : "a"(wob_f[i]), "v"(of[i]));
            if (i == PEEK_AT) peek(poa, ws, poa_word);
            if (i == SEEN_AT) { seen<0>(poa, ws, poa_word, nonce); STAMP(15); }
            if (i >= SEEN_AT && i < SEEN_AT + 8) dma16_dev(rws, L_OIMG2 + (wid * 8 + i - SEEN_AT) * 1024, voff, WS_OA + o_need + (i - SEEN_AT) * 1024);
            SBAR();
        }
        settle(r);
        stage_out(L_OUT, wid, lane, r);
        STAMP(14);
        wait_bar<0>();
        if (blk == 0 && wid == 0) publish(ws, WS_EPOCH, nonce);
#pragma unroll
        for (int i = 0; i < 24; i += 8)
            asm volatile("" : "+v"(woa_f[i]), "+v"(woa_f[i + 1]), "+v"(woa_f[i + 2]), "+v"(woa_f[i + 3]), "+v"(woa_f[i + 4]), "+v"(woa_f[i + 5]), "+v"(woa_f[i + 6]), "+v"(woa_f[i + 7]));
        asm volatile("" : "+a"(woa_f[24]), "+a"(woa_f[25]), "+a"(woa_f[26]), "+a"(woa_f[27]), "+a"(woa_f[28]), "+a"(woa_f[29]), "+a"(woa_f[30]), "+a"(woa_f[31]));
        f32x16 s;
#pragma unroll
        for (int i = 0; i < 6; ++i) of[i] = lds128(L_OIMG2 + i * 1024 + voff);
#pragma unroll
        for (int i = 0; i < 32; ++i) {
            if (i + 6 < 32) of[i + 6] = lds128(L_OIMG2 + (i + 6) * 1024 + voff);
            SBAR();
            if (i == 0) mfma_v0(s, woa_f[0], of[0]); else if (i < 24) mfma_v(s, woa_f[i], of[i]);
            else asm volatile("v_mfma_f32_32x32x16_bf16 %0, %1, %2, %0" : "+v"(s) : "a"(woa_f[i]), "v"(of[i]));
            if (i == 4) flush_out(L_OUT, wid, lane, h, qt, 1, (u16*)args[7]);
            SBAR();
        }
        settle(s);
        STAMP(16);
        stage_out(L_OUT + 16384, wid, lane, s);
        lds_bar();
        asm volatile(".Lfused_code_end:");
        flush_out(L_OUT + 16384, wid, lane, h, qt, 0, (u16*)args[7]);
    }
}

}

extern "C" void kernel_launch(void* const* d_in, const int* in_sizes, int n_in, void* d_out, int out_size, void* d_ws, size_t ws_size, hipStream_t stream) {
    (void)hipFuncSetAttribute((const void*)fused, hipFuncAttributeMaxDynamicSharedMemorySize, LDS_BYTES);
    fused<<<256, 256, LDS_BYTES, stream>>>((const u16*)d_in[0], (const u16*)d_in[1], (const u16*)d_in[2], (const u16*)d_in[3], (const u16*)d_in[4], (const u16*)d_in[5],
                                           (const u16*)d_in[6], (u16*)d_out, (char*)d_ws);
}
```

```cpp
#define DIAG 2
#include <hip/hip_runtime.h>
#include <stdint.h>

namespace {

typedef unsigned short u16;
typedef unsigned long long u64;
typedef __attribute__((ext_vector_type(16))) float f32x16;
typedef __attribute__((ext_vector_type(4))) float f32x4;
typedef __attribute__((ext_vector_type(2))) float f32x2;
typedef __attribute__((ext_vector_type(8))) __bf16 bf16x8;
typedef __attribute__((ext_vector_type(2))) __bf16 bf16x2;
typedef __attribute__((ext_vector_type(4))) unsigned u32x4;
typedef __attribute__((ext_vector_type(2))) unsigned u32x2;
typedef __attribute__((ext_vector_type(4))) short s16x4;
typedef __attribute__((ext_vector_type(8))) unsigned long long u64x8;
typedef __amdgpu_buffer_rsrc_t rsrc_t;
#define LAS __attribute__((address_space(3)))
#define GAS __attribute__((address_space(1)))
#define DI __device__ __forceinline__
#define SBAR() __builtin_amdgcn_sched_barrier(0)

constexpr int SEQ = 2048, DM = 448;
constexpr unsigned XROW = DM * 2;
constexpr unsigned WROW = 3 * DM * 2;
constexpr unsigned X_BYTES = SEQ * XROW, W_BYTES = DM * WROW, WO_BYTES = DM * XROW;
constexpr unsigned WS_EPOCH = 0;
constexpr unsigned WS_FK = 4096;
constexpr unsigned WS_FP = WS_FK + 8192;
constexpr unsigned WS_FOA = WS_FP + 8192;
constexpr unsigned WS_FOB = WS_FOA + 8192;
constexpr unsigned WS_FW = WS_FOB + 8192;
constexpr unsigned WS_FWO = WS_FW + 8192;
constexpr unsigned WS_FS = WS_FWO + 8192;
constexpr unsigned WS_VOID = WS_FS + 8192;
constexpr unsigned WS_KVA = 131072;
constexpr unsigned WS_OA = WS_KVA + (4u << 20);
constexpr unsigned WS_OB = WS_OA + (2u << 20);
constexpr unsigned WS_KVP = WS_OB + (2u << 20);
constexpr unsigned WS_WB = WS_KVP + (2u << 20);
constexpr unsigned WS_WO = WS_WB + (2u << 20);
constexpr unsigned WS_ST = WS_WO + (1u << 20);
constexpr unsigned WS_END = WS_ST + 65536;
constexpr int LDS_BYTES = 160 * 1024;
constexpr unsigned L_WKV = 0, L_RET = 57344, L_QB = 57344;
constexpr unsigned L_X = 86016;
constexpr unsigned l_kvb(int ch) { return ch == 0 ? 151552 : (ch - 1) * 8192; }
constexpr unsigned l_wq(int ch) { return ch < 6 ? ch * 4096 : 49152; }
constexpr unsigned L_QX = 151552, L_REF = 40960, L_WPHI = 159744, L_FLAG = 161792;
constexpr unsigned L_PHI = 86016, L_RED = 118784;
constexpr unsigned L_COMB = 0, L_OIMG = 65536, L_OIMG2 = 0, L_OUT = 98304;
constexpr float LOG2E = 1.4426950408889634f;
constexpr float QSCALE = 0.18898223650461362f * LOG2E;
#define REF_OFF 100.f
constexpr float REF_OFFSET = REF_OFF;
constexpr float L_LIMIT = 18446744073709551616.f;
constexpr int SC1 = 16;
constexpr unsigned OOB = 0x80000000u;
#define SPINLOG 16
constexpr unsigned SPIN_CAP = 1u << SPINLOG;
#define PEEK_AT 4
#define SEEN_AT 18
#define KP_PEEK 4
#define KP_SEEN 15
#define PSLEEP 4

DI f32x16 mfma(u32x4 a, u32x4 b, f32x16 c) {
    return __builtin_amdgcn_mfma_f32_32x32x16_bf16(__builtin_bit_cast(bf16x8, a), __builtin_bit_cast(bf16x8, b), c, 0, 0, 0);
}
DI unsigned pk(float lo, float hi) { f32x2 v = {lo, hi}; return __builtin_bit_cast(unsigned, __builtin_convertvector(v, bf16x2)); }
DI u32x4 pack_lo(const f32x16& x) { return u32x4{pk(x[0], x[1]), pk(x[2], x[3]), pk(x[4], x[5]), pk(x[6], x[7])}; }
DI u32x4 pack_hi(const f32x16& x) { return u32x4{pk(x[8], x[9]), pk(x[10], x[11]), pk(x[12], x[13]), pk(x[14], x[15])}; }
DI u32x4 quad(const f32x16& x, int g) { return u32x4{__float_as_uint(x[4 * g]), __float_as_uint(x[4 * g + 1]), __float_as_uint(x[4 * g + 2]), __float_as_uint(x[4 * g + 3])}; }
DI rsrc_t make_rsrc(const void* p, unsigned bytes) { return __builtin_amdgcn_make_buffer_rsrc((void*)p, 0, bytes, 0x00020000); }
DI u32x4 ld128(rsrc_t rs, unsigned voff, unsigned soff) { return __builtin_amdgcn_raw_buffer_load_b128(rs, voff, soff, 0); }
DI u32x4 ld128_dev(rsrc_t rs, unsigned voff, unsigned soff) { return __builtin_amdgcn_raw_buffer_load_b128(rs, voff, soff, SC1); }
DI void st128_dev(u32x4 v, rsrc_t rs, unsigned voff, unsigned soff) { __builtin_amdgcn_raw_buffer_store_b128(v, rs, voff, soff, SC1); }
DI void dma16(rsrc_t rs, unsigned lds_addr, unsigned voff, unsigned soff) {
    asm volatile("s_mov_b32 m0, %0\n\ts_nop 0\n\tbuffer_load_dwordx4 %1, %2, %3 offen lds" ::"s"(lds_addr), "v"(voff), "s"(rs), "s"(soff) : "memory");
}
DI void ld128_async(u32x4& d, rsrc_t rs, unsigned voff, unsigned soff) {
    asm volatile("buffer_load_dwordx4 %0, %1, %2, %3 offen" : "=v"(d) : "v"(voff), "s"(rs), "s"(soff) : "memory");
}
DI void dma16_dev(rsrc_t rs, unsigned lds_addr, unsigned voff, unsigned soff) {
    asm volatile("s_mov_b32 m0, %0\n\ts_nop 0\n\tbuffer_load_dwordx4 %1, %2, %3 offen sc1 lds" ::"s"(lds_addr), "v"(voff), "s"(rs), "s"(soff) : "memory");
}
template <int OFF> DI void dma_next(rsrc_t rs, unsigned voff, unsigned soff) {
    asm volatile("buffer_load_dwordx4 %0, %1, %2 offen offset:%3 lds" ::"v"(voff), "s"(rs), "s"(soff), "n"(OFF) : "memory");
}
template <int OFF> DI void dma_next_dev(rsrc_t rs, unsigned voff, unsigned soff) {
    asm volatile("buffer_load_dwordx4 %0, %1, %2 offen offset:%3 sc1 lds" ::"v"(voff), "s"(rs), "s"(soff), "n"(OFF) : "memory");
}
DI void dma_run_dev(rsrc_t rs, unsigned lds, unsigned voff, unsigned base, int i) {
    const unsigned s = base + (i >> 2) * 4096;
    if ((i & 3) == 0) dma16_dev(rs, lds + i * 1024, voff, s); else if ((i & 3) == 1) dma_next_dev<1024>(rs, voff, s);
    else if ((i & 3) == 2) dma_next_dev<2048>(rs, voff, s); else dma_next_dev<3072>(rs, voff, s);
}
template <int N> DI void wait_vm() { asm volatile("s_waitcnt vmcnt(%0)" ::"n"(N) : "memory"); }
template <int N> DI void wait_bar() { asm volatile("s_waitcnt vmcnt(%0) lgkmcnt(0)\n\ts_barrier" ::"n"(N) : "memory"); }
DI void lds_bar() { asm volatile("s_waitcnt lgkmcnt(0)\n\ts_barrier" ::: "memory"); }
DI u32x4 lds128(unsigned a) { return *(const LAS u32x4*)(size_t)a; }
DI void sts128(unsigned a, u32x4 v) { *(LAS u32x4*)(size_t)a = v; }
DI void sts64(unsigned a, u32x2 v) { *(LAS u32x2*)(size_t)a = v; }
DI u32x2 tr64(unsigned a) { return __builtin_bit_cast(u32x2, __builtin_amdgcn_ds_read_tr16_b64_v4i16((LAS s16x4*)(size_t)a)); }
DI u32x4 tr_frag(unsigned a0, unsigned a1) { u32x2 lo = tr64(a0), hi = tr64(a1); return u32x4{lo[0], lo[1], hi[0], hi[1]}; }
DI float max3(float a, float b, float c) { float r; asm("v_max3_f32 %0, %1, %2, %3" : "=v"(r) : "v"(a), "v"(b), "v"(c)); return r; }
DI float min3(float a, float b, float c) { float r; asm("v_min3_f32 %0, %1, %2, %3" : "=v"(r) : "v"(a), "v"(b), "v"(c)); return r; }
DI float both_max(float v) { auto r = __builtin_amdgcn_permlane32_swap(__float_as_uint(v), __float_as_uint(v), false, false); return max3(__uint_as_float(r[0]), __uint_as_float(r[1]), __uint_as_float(r[1])); }
DI float both_min(float v) { auto r = __builtin_amdgcn_permlane32_swap(__float_as_uint(v), __float_as_uint(v), false, false); return min3(__uint_as_float(r[0]), __uint_as_float(r[1]), __uint_as_float(r[1])); }
DI float both_sum(float v) { auto r = __builtin_amdgcn_permlane32_swap(__float_as_uint(v), __float_as_uint(v), false, false); return __uint_as_float(r[0]) + __uint_as_float(r[1]); }
DI float upper_val(float v) { auto r = __builtin_amdgcn_permlane32_swap(__float_as_uint(v), __float_as_uint(v), false, false); return __uint_as_float(r[1]); }
DI float max16(const f32x16& s) {
    const float a = max3(s[0], s[1], s[2]), b = max3(s[3], s[4], s[5]), c = max3(s[6], s[7], s[8]), d = max3(s[9], s[10], s[11]), e = max3(s[12], s[13], s[14]);
    return max3(max3(a, b, c), max3(d, e, s[15]), a);
}
DI float min16(const f32x16& s) {
    const float a = min3(s[0], s[1], s[2]), b = min3(s[3], s[4], s[5]), c = min3(s[6], s[7], s[8]), d = min3(s[9], s[10], s[11]), e = min3(s[12], s[13], s[14]);
    return min3(min3(a, b, c), min3(d, e, s[15]), a);
}

template <int OFF = 0> DI void ld128_async_dev(u32x4& d, rsrc_t rs, unsigned voff, unsigned soff) {
    asm volatile("buffer_load_dwordx4 %0, %1, %2, %3 offen offset:%4 sc1" : "=v"(d) : "v"(voff), "s"(rs), "s"(soff), "n"(OFF) : "memory");
}
template <int OFF = 0> DI void ld128_async_acc(u32x4& d, rsrc_t rs, unsigned voff, unsigned soff) {
    asm volatile("buffer_load_dwordx4 %0, %1, %2, %3 offen offset:%4 sc1" : "=a"(d) : "v"(voff), "s"(rs), "s"(soff), "n"(OFF) : "memory");
}
template <bool ACC> DI void ld_frag(u32x4& d, rsrc_t rs, unsigned voff, unsigned base, int i) {
    const unsigned s = base + (i >> 2) * 4096;
    if (ACC) { if ((i & 3) == 0) ld128_async_acc(d, rs, voff, s); if ((i & 3) == 1) ld128_async_acc<1024>(d, rs, voff, s); if ((i & 3) == 2) ld128_async_acc<2048>(d, rs, voff, s); if ((i & 3) == 3) ld128_async_acc<3072>(d, rs, voff, s); }
    else     { if ((i & 3) == 0) ld128_async_dev(d, rs, voff, s); if ((i & 3) == 1) ld128_async_dev<1024>(d, rs, voff, s); if ((i & 3) == 2) ld128_async_dev<2048>(d, rs, voff, s); if ((i & 3) == 3) ld128_async_dev<3072>(d, rs, voff, s); }
}
template <int OFF = 0> DI void st128_async(u32x4 v, rsrc_t rs, unsigned voff, unsigned soff) {
    asm volatile("buffer_store_dwordx4 %0, %1, %2, %3 offen offset:%4 sc1\n\ts_nop 1" ::"v"(v), "v"(voff), "s"(rs), "s"(soff), "n"(OFF) : "memory");
}
template <int N> DI void landed(u32x4& a, u32x4& b, u32x4& c, u32x4& d) { asm volatile("s_waitcnt vmcnt(%4)" : "+v"(a), "+v"(b), "+v"(c), "+v"(d) : "n"(N) : "memory"); }
template <int N> DI void landed8(u32x4 (&r)[8]) {
    asm volatile("s_waitcnt vmcnt(%8)" : "+v"(r[0]), "+v"(r[1]), "+v"(r[2]), "+v"(r[3]), "+v"(r[4]), "+v"(r[5]), "+v"(r[6]), "+v"(r[7]) : "n"(N) : "memory");
}
DI void mfma_v(f32x16& acc, u32x4 a, u32x4 b) { asm("v_mfma_f32_32x32x16_bf16 %0, %1, %2, %0" : "+v"(acc) : "v"(a), "v"(b)); }
DI void mfma_v0(f32x16& acc, u32x4 a, u32x4 b) { asm("s_nop 1\n\tv_mfma_f32_32x32x16_bf16 %0, %1, %2, 0" : "=&v"(acc) : "v"(a), "v"(b)); }
DI void settle(f32x16& x) { asm volatile("s_nop 11" : "+v"(x)); }
template <int V> struct Int { static constexpr int value = V; };
template <int I, int N, class F> DI void static_for(F&& f) { if constexpr (I < N) { f(Int<I>{}); static_for<I + 1, N>(f); } }
DI void barrier() { asm volatile("s_barrier" ::: "memory"); }
DI int lane_id() { int l; asm volatile("v_mbcnt_lo_u32_b32 %0, -1, 0\n\tv_mbcnt_hi_u32_b32 %0, -1, %0" : "=v"(l)); return l; }

#define STAMP(id) do { if (id == DIAG || id == 0) { u64 t_; asm volatile("s_memrealtime %0\n\ts_waitcnt lgkmcnt(0)" : "=s"(t_) :: "memory"); if (id == 0) dg0 = t_; else dg1 = t_; } } while (0)
#define TSTAMP(id) do {} while (0)

DI void epoch_request(u64& v, const char* p) { asm volatile("s_load_dwordx2 %0, %1, 0x0" : "=s"(v) : "s"(p) : "memory"); }
DI void publish(char* ws, unsigned word, u64 nonce) {
    asm volatile("s_mov_b64 exec, 1\n\tglobal_store_dwordx2 %0, %1, %2 sc1\n\ts_mov_b64 exec, -1" ::"v"(word), "v"(nonce), "s"(ws) : "memory");
}
DI void peek(u32x2& v, const char* ws, unsigned word) { asm volatile("global_load_dwordx2 %0, %1, %2 sc1" : "=v"(v) : "v"(word), "s"(ws) : "memory"); }
DI void peek(u32x4& v, const char* ws, unsigned word) { asm volatile("global_load_dwordx4 %0, %1, %2 sc1" : "=v"(v) : "v"(word), "s"(ws) : "memory"); }
DI bool differs(const u32x2& v, u64 n) { return __builtin_amdgcn_ballot_w64(__builtin_bit_cast(u64, v) != n) != 0; }
DI bool differs(const u32x4& v, u64 n) {
    return (__builtin_amdgcn_ballot_w64(__builtin_bit_cast(u64, u32x2{v[0], v[1]}) != n) | __builtin_amdgcn_ballot_w64(__builtin_bit_cast(u64, u32x2{v[2], v[3]}) != n)) != 0;
}
template <int N, class V> DI void seen(V& v, const char* ws, unsigned word, u64 nonce) {
    asm volatile("s_waitcnt vmcnt(%1)" : "+v"(v) : "n"(N) : "memory");
    if (__builtin_expect(differs(v, nonce), 0)) {
        unsigned spins = 0;
#pragma unroll 1
        do {
            __builtin_amdgcn_s_sleep(PSLEEP);
            peek(v, ws, word);
            asm volatile("s_waitcnt vmcnt(0)" : "+v"(v) :: "memory");
        } while (differs(v, nonce) && ++spins < SPIN_CAP);
    }
}

DI void mfma_pair(int isv, u32x4 xf, u32x4 w0, u32x4 w1, f32x16& acc0, f32x16& acc1) {
    asm("s_cmp_lg_u32 %5, 0\n\ts_cbranch_scc1 1f\n\t"
        "v_mfma_f32_32x32x16_bf16 %0, %3, %2, %0\n\tv_mfma_f32_32x32x16_bf16 %1, %4, %2, %1\n\ts_branch 2f\n"
        "1:\n\tv_mfma_f32_32x32x16_bf16 %0, %2, %3, %0\n\tv_mfma_f32_32x32x16_bf16 %1, %2, %4, %1\n2:"
        : "+v"(acc0), "+v"(acc1) : "v"(xf), "v"(w0), "v"(w1), "s"(isv) : "scc");
}
DI void pin(u32x4& a, u32x4& b, u32x4& c, u32x4& d) { asm volatile("" : "+v"(a), "+v"(b), "+v"(c), "+v"(d) :: "memory"); }

DI void set_reference(u32x4& q1, float nm, int hi) {
    const unsigned a = __float_as_uint(nm) & 0xFFFF0000u;
    const float r1 = nm - __uint_as_float(a);
    const unsigned b = __float_as_uint(r1) & 0xFFFF0000u;
    const float r2 = r1 - __uint_as_float(b);
    q1[2] = hi ? (a >> 16) | b : q1[2]; q1[3] = hi ? __float_as_uint(r2) >> 16 : q1[3];
}

struct KV { u32x4 k0, k1, v0, v1; };

DI void ref_post(int wid, int lane, const float (&m)[4]) {
#pragma unroll
    for (int t = 0; t < 4; ++t) *(LAS float*)(size_t)(L_REF + ((wid + t) & 3) * 512 + (lane & 31) * 16 + wid * 4) = both_max(m[t]);
}
DI void ref_take(int wid, int lane, u32x4 (&q)[4][2], float offset) {
#pragma unroll
    for (int t = 0; t < 4; ++t) {
        const f32x4 m = __builtin_bit_cast(f32x4, lds128(L_REF + ((wid + t) & 3) * 512 + (lane & 31) * 16));
        set_reference(q[t][1], -(max3(max3(m[0], m[1], m[2]), m[3], m[3]) + offset), lane >> 5);
    }
}

DI void combine_post(int wid, int lane, const f32x16& o1, const f32x16& o2, const f32x16& o3) {
    const f32x16* ot[3] = {&o1, &o2, &o3};
#pragma unroll
    for (int t = 0; t < 3; ++t) {
        const unsigned at = L_COMB + (((wid + t + 1) & 3) * 3 + t) * 4096 + lane * 16;
#pragma unroll
        for (int g = 0; g < 4; ++g) sts128(at + g * 1024, quad(*ot[t], g));
    }
}
DI bool combine_take(int wid, int lane, f32x16 o, u32x4& f0, u32x4& f1) {
#pragma unroll
    for (int v = 0; v < 12; ++v) {
        const u32x4 p = lds128(L_COMB + wid * 12288 + v * 1024 + lane * 16);
#pragma unroll
        for (int i = 0; i < 4; ++i) o[4 * (v & 3) + i] += __uint_as_float(p[i]);
    }
    const float l = upper_val(o[12]);
    o *= __builtin_amdgcn_rcpf(l);
    float chk = 0.f;
#pragma unroll
    for (int i = 0; i < 16; ++i) chk = fmaf(o[i], 0.f, chk);
    f0 = pack_lo(o); f1 = pack_hi(o);
    if (lane >> 5) { f1[2] = 0; f1[3] = 0; }
    return !(l < L_LIMIT) || !(chk == 0.f);
}

DI void stage_out(unsigned img, int wid, int lane, const f32x16& r) {
    const u32x4 lo = pack_lo(r), up = pack_hi(r);
    const unsigned wr = img + (lane & 31) * 272 + wid * 64 + (lane >> 5) * 8;
    sts64(wr, u32x2{lo[0], lo[1]}); sts64(wr + 16, u32x2{lo[2], lo[3]}); sts64(wr + 32, u32x2{up[0], up[1]}); sts64(wr + 48, u32x2{up[2], up[3]});
}
DI void flush_out(unsigned img, int wid, int lane, int h, int qt, int br, u16* out) {
    const unsigned width = h >= 12 ? 128 : 256, colb = (lane & 15) * 16;
    const rsrc_t rd = make_rsrc((char*)out + (size_t)qt * 32 * (2 * XROW) + br * XROW + (h >> 2) * 256, 31 * 2 * XROW + width);
#pragma unroll
    for (int i = 0; i < 2; ++i) {
        const unsigned row = wid * 8 + i * 4 + (lane >> 4);
        st128_async(lds128(img + row * 272 + colb), rd, colb < width ? row * (2 * XROW) + colb : OOB, 0);
    }
}

__global__ __launch_bounds__(256, 1) void fused(const u16*, const u16*, const u16*, const u16*, const u16*, const u16*, const u16*, u16*, char*) {
    u64x8 args;
    u64 arg_ws;
    const auto arg_base = __builtin_amdgcn_kernarg_segment_ptr();
    asm volatile("s_load_dwordx16 %0, %2, 0x0\n\ts_load_dwordx2 %1, %2, 0x40" : "=&s"(args), "=&s"(arg_ws) : "s"(arg_base) : "memory");
    SBAR();
    u64 dg0 = 0, dg1 = 0;
    STAMP(0);
    const int wid = __builtin_amdgcn_readfirstlane(threadIdx.x >> 6);
    const int blk = blockIdx.x, xcd = blk & 7, bi = blk >> 3;
    const int h = (xcd & 1) * 8 + (bi & 7), rb = (xcd >> 1) * 4 + (bi >> 3), tile = rb * 4 + wid;
    const int rot = ((bi >> 3) + (bi & 7)) % 3;
    const int rotb = (2 * (bi >> 3) + (bi & 7)) % 7;
    auto chunk_b = [&](int p) { const int v = p + rotb; return v >= 7 ? v - 7 : v; };
    auto chunk_at = [&](int p) { const int v = p % 3 + rot; return p == 6 ? 6 : p - p % 3 + (v >= 3 ? v - 3 : v); };
    u64 epoch, code_pc;
    unsigned code_probe;
    u64 nonce;
    const f32x16 zero = {};
    u32x4 q[4][2], eq[4];
    KV a, b, c, d;
    char* ws;
    rsrc_t rws;
    const unsigned kv_at = WS_KVA + (h * 64 + wid * 16) * 4096;
#define LDKV(X, j) do { const unsigned s_ = kv_at + (j) * 4096; ld128_async_dev<3072>(X.v1, rws, voff, s_); ld128_async_dev<2048>(X.v0, rws, voff, s_); \
                        ld128_async_dev<1024>(X.k1, rws, voff, s_); ld128_async_dev(X.k0, rws, voff, s_); } while (0)
    {
        const int lane = lane_id(), hi = lane >> 5, r32 = lane & 31, q4 = (lane & 15) >> 2;
        const unsigned voff = lane * 16, col = 32 * ((lane >> 4) & 1) + 8 * (lane & 3);
        const int isv = h & 1, row8 = lane >> 3;
        u32x4 xf[28];
        f32x16 acc0 = {}, acc1 = {};
        u32x2 pw;
        {
            const unsigned piece = (lane & 7) ^ (((row8 >> 1) & 1) << 2);
            unsigned vw0 = (16 * wid + row8) * WROW + (1 + isv) * XROW + (h >> 1) * 112 + ((piece < 7 ? piece : 6) << 4), vw1 = vw0 + 8 * WROW - 1024;
            unsigned vx[4];
#pragma unroll
            for (int i = 0; i < 4; ++i) vx[i] = (tile * 32 + row8) * XROW + i * (8 * XROW - 1024) + (((lane & 7) ^ (4 * (i & 1) + (lane >> 4))) << 4);
            int rot0 = rot;
            asm volatile("s_waitcnt lgkmcnt(0)" : "+s"(args), "+s"(arg_ws), "+v"(vw0), "+v"(vw1), "+v"(vx[0]), "+v"(vx[1]), "+v"(vx[2]), "+v"(vx[3]), "+s"(rot0) : "s"(arg_base) : "memory");
            const rsrc_t rxa = make_rsrc((const void*)args[0], X_BYTES), rwa = make_rsrc((const void*)args[1], W_BYTES);
            auto req_x = [&](rsrc_t rx, int slot, unsigned k, int i) {
                if (i == 0) dma16(rx, L_X + slot * 16384 + wid * 4096, vx[0], k * 128); else if (i == 1) dma_next<1024>(rx, vx[1], k * 128);
                else if (i == 2) dma_next<2048>(rx, vx[2], k * 128); else dma_next<3072>(rx, vx[3], k * 128);
            };
            auto req_w = [&](int ch, int i) {
                if (i == 0) dma16(rwa, L_WKV + ch * 8192 + wid * 2048, vw0, chunk_at(ch) * 64 * WROW); else dma_next<1024>(rwa, vw1, chunk_at(ch) * 64 * WROW);
            };
            auto issue = [&](int ch) {
                req_w(ch, 0); req_w(ch, 1);
#pragma unroll
                for (int i = 0; i < 4; ++i) req_x(rxa, ch & 3, chunk_at(ch), i);
            };
            issue(0);
            SBAR();
            ws = (char*)arg_ws;
            rws = make_rsrc(ws, WS_END);
            const rsrc_t rxb = make_rsrc((const void*)args[3], X_BYTES);
            epoch_request(epoch, ws + WS_EPOCH);
            {
                unsigned code_len;
                asm volatile("s_getpc_b64 %0\n.Lfused_code_begin:\n\ts_mov_b32 %1, .Lfused_code_end-.Lfused_code_begin" : "=s"(code_pc), "=s"(code_len));
                const unsigned at = (bi * 16 + wid * 4 + (threadIdx.x & 3)) * 128;
                asm volatile("global_load_dword %0, %1, %2" : "=v"(code_probe) : "v"(at < code_len ? at : 0), "s"(code_pc) : "memory");
            }
            int gw = blk * 4 + wid;
            asm volatile("" : "+s"(gw));
            const unsigned idle = gw < 896 ? 0u : OOB;
            {
                const int uh = gw / 56, uj = gw - uh * 56, i = lane >> 2;
                const bool kvp = uj < 28;
                const int ks = kvp ? uj : 2 * (uj < 42 ? uj - 28 : uj - 42);
                const rsrc_t rr = make_rsrc((const void*)(uj < 42 ? args[4] : args[1]), W_BYTES);
                const unsigned v = (((i & 7) + 32 * (i >> 3)) * WROW + (lane & 3) * 16) | idle;
                const unsigned s0 = (64 * (ks >> 2) + 8 * (ks & 3)) * WROW + (kvp ? XROW : 0) + uh * 56;
                dma16(rr, L_RET + wid * 2048, v, s0); dma16(rr, L_RET + wid * 2048 + 1024, v, s0 + (kvp ? XROW : 8 * WROW));
            }
            issue(1);
            u64 ro_ptr = gw >= 448 ? args[6] : args[2];
            unsigned ro_at = (28 * ((gw & 31) >> 1) + 16 * (gw & 1)) * XROW + ((gw >= 448 ? gw - 448 : gw) >> 5) * 64;
            asm volatile("" : "+s"(ro_ptr), "+s"(ro_at));
            unsigned rt_at = L_RET + wid * 2048 + (8 * hi + q4) * 64 + col, rt_v = voff | idle;
            unsigned rt_word = gw < 896 ? WS_FW + (gw + (gw / 56) * 8) * 8 : WS_VOID + gw * 8;
            unsigned w0 = L_WKV + (32 * hi + q4) * 128 + (col ^ ((q4 >> 1) << 6)), w1 = L_WKV + (32 * hi + q4) * 128 + ((col + 56) ^ ((q4 >> 1) << 6));
            unsigned xo[4];
#pragma unroll
            for (int s = 0; s < 4; ++s) xo[s] = L_X + wid * 4096 + r32 * 128 + (((4 * hi + s) ^ ((r32 >> 1) & 7)) << 4);
            const unsigned pw_word = WS_FW + h * 512 + (lane < 56 ? lane : 55) * 8;
            asm volatile("" : "+s"(rt_word), "+v"(rt_at), "+v"(rt_v), "+v"(w0), "+v"(w1), "+v"(xo[0]), "+v"(xo[1]), "+v"(xo[2]), "+v"(xo[3]), "+v"(acc0), "+v"(acc1));
            asm volatile("s_waitcnt lgkmcnt(0)" : "+s"(epoch) :: "memory");
            nonce = epoch + 1;
            STAMP(1);
            const unsigned wb_at = WS_WB + h * 114688;
            auto req_kvb = [&](int ch, int j) {
                const unsigned s = wb_at + chunk_b(ch) * 8192 + wid * 2048;
                if (j == 0) dma16_dev(rws, l_kvb(ch) + wid * 2048, voff, s); else dma_next_dev<1024>(rws, voff, s);
            };
            auto req_qb = [&](int ch) { dma16_dev(rws, L_QB + ch * 4096 + wid * 1024, voff, wb_at + 57344 + chunk_b(ch) * 4096 + wid * 1024); };
            auto req_wq = [&](int j) { dma16_dev(rws, l_wq(j) + wid * 1024, voff, wb_at + 86016 + chunk_at(j) * 4096 + wid * 1024); };
            auto xb_slot = [](int ch) { return (ch + 3) & 3; };
            u32x4 wf[2][8];
#pragma unroll
            for (int p = 0; p <= 7; ++p) {
                if (p < 7) {
                    if (p == 1 || p == 2) wait_vm<8>(); else if (p == 4) wait_vm<7>(); else wait_vm<6>();
                    if (p == 3) publish(ws, rt_word, nonce);
                    barrier();
                    if (p == 0) asm volatile("" :: "v"(code_probe), "s"(code_pc));
                    if (p == 0) { STAMP(2); }
                    if (p == 6) { STAMP(3); }
                    if (p == 6) peek(pw, ws, pw_word);
                } else {
                    seen<4>(pw, ws, pw_word, nonce);
                    lds_bar();
                }
#pragma unroll
                for (int s = 0; s < 4; ++s) {
                    SBAR();
                    if (p > 0) mfma_pair(isv, xf[4 * p - 4 + s], wf[(p - 1) & 1][2 * s], wf[(p - 1) & 1][2 * s + 1], acc0, acc1);
                    SBAR();
                    if (p < 7) {
                        const unsigned o = p * 8192 + s * 1024;
                        xf[4 * p + s] = lds128(xo[s] + (p & 3) * 16384);
                        wf[p & 1][2 * s] = tr_frag(w0 + o, w0 + o + 512); wf[p & 1][2 * s + 1] = tr_frag(w1 + o, w1 + o + 512);
                    }
                    if (p <= 4) {
                        if (s == 0) { req_w(p + 2, 0); req_w(p + 2, 1); }
                        if (s == 1) { req_x(rxa, (p + 2) & 3, chunk_at(p + 2), 0); req_x(rxa, (p + 2) & 3, chunk_at(p + 2), 1); }
                        if (s >= 2) req_x(rxa, (p + 2) & 3, chunk_at(p + 2), s);
                    }
                    if (p == 0 && s == 3) {
                        st128_async(tr_frag(rt_at, rt_at + 256), rws, rt_v, WS_WB + gw * 2048);
                        st128_async<1024>(tr_frag(rt_at + 1024, rt_at + 1280), rws, rt_v, WS_WB + gw * 2048);
                    }
                    if (p == 5 || p == 6) req_x(rxb, xb_slot(p - 5), chunk_b(p - 5), s);
                    if (p == 5 && s == 3) {
                        const rsrc_t rp = make_rsrc((const char*)args[5] + h * 1568, 1568);
                        dma16(rp, L_WPHI, voff, 0); dma_next<1024>(rp, voff, 0);
                    }
                    if (p == 7) {
                        if (s < 2) { req_kvb(s, 0); req_kvb(s, 1); req_qb(s); }
                        else { req_x(rxb, xb_slot(2), chunk_b(2), 2 * s - 4); req_x(rxb, xb_slot(2), chunk_b(2), 2 * s - 3); }
                    }
                    SBAR();
                }
            }
            asm volatile("s_nop 3" : "+v"(acc0), "+v"(acc1));
            u32x4 f[4] = {pack_lo(acc0), pack_hi(acc0), pack_lo(acc1), pack_hi(acc1)};
            {
                const bool all = isv && r32 >= 28, tail = isv ? all : (bool)hi;
                const unsigned va = r32 == 28 ? 0x3F803F80u : 0u, v2 = isv ? va : 0x3F803F80u, v3 = isv ? va : 0x00003F80u;
#pragma unroll
                for (int i = 0; i < 4; ++i) {
                    f[i][0] = all ? va : f[i][0]; f[i][1] = all ? va : f[i][1];
                    if (i & 1) { f[i][2] = tail ? v2 : f[i][2]; f[i][3] = tail ? v3 : f[i][3]; }
                    else       { f[i][2] = all ? va : f[i][2]; f[i][3] = all ? va : f[i][3]; }
                }
                pin(f[0], f[1], f[2], f[3]);
            }
            const unsigned kv_to = WS_KVA + ((h & 14) * 64 + tile) * 4096 + isv * 2048;
            STAMP(4);
            if (wid == 0 && lane == 0) *(LAS unsigned*)(size_t)L_FLAG = 0;
            u32x2 pk_;
            const unsigned pk_word = WS_FK + ((h >> 1) * 64 + wid * 16) * 16 + r32 * 8;
            f32x16 aq = {}, ak = {}, av = {};
            unsigned qba = L_QB + voff;
            asm volatile("s_nop 1" : "+v"(aq), "+v"(ak), "+v"(av), "+v"(qba));
            {
                u32x4 wg[28][3], xg[28];
                static_for<0, 31>([&](auto nc) __attribute__((always_inline)) {
                    constexpr int n = decltype(nc)::value, i = n - 3, p = n >> 2, s = n & 3;
                    if (n < 28) {
                        if (s == 0) {
                            if (p == 0 || p == 5) wait_vm<7>(); else if (p == 2) wait_vm<10>(); else if (p == 6) wait_vm<8>(); else wait_vm<11>();
                            barrier();
                            if (p == 5) { publish(ws, WS_FK + (((h >> 1) * 64 + tile) * 2 + isv) * 8, nonce); STAMP(5); }
                        }
                    }
                    auto request = [&](int slot) __attribute__((always_inline)) {
                        const int r = slot - 1;
                        if (p == 0 || p == 3 || p == 4) {
                            const int kq = p == 0 ? 2 : p + 2, xc = p == 0 ? 3 : p + 2;
                            if (r == 0 || r == 1) req_kvb(kq, r);
                            if (r == 2) req_qb(kq);
                            if (r >= 7 && r < 11) req_x(rxb, xb_slot(xc), chunk_b(xc), r - 7);
                        }
                        if (p == 1) { if (r == 0 || r == 2) req_kvb(3, r >> 1); if (r == 4) req_qb(3); if (r == 6 || r == 8) req_kvb(4, (r - 6) >> 1); if (r == 10) req_qb(4); }
                        if (p == 2) {
                            if (r >= 0 && r < 4) req_x(rxb, xb_slot(4), chunk_b(4), r);
                            if (r == 5) st128_async(f[0], rws, voff, kv_to);
                            if (r == 6) st128_async<1024>(f[1], rws, voff, kv_to);
                            if (r == 8) st128_async(f[2], rws, voff, kv_to + 64 * 4096);
                            if (r == 9) st128_async<1024>(f[3], rws, voff, kv_to + 64 * 4096);
                        }
                        if (p == 5 && r >= 0 && r < 7) req_wq(r);
                        if (p == 6 && r == 0) {
                            const int i = lane >> 2;
                            dma16(make_rsrc((const void*)ro_ptr, WO_BYTES), L_X + 2 * 16384 + wid * 4096, (((gw & 1) && i >= 12 ? i - 4 : i) * XROW + (lane & 3) * 16) | idle, ro_at);
                        }
                    };
                    const unsigned at = l_kvb(p < 7 ? p : 0) + s * 2048 + voff;
                    SBAR();
                    if constexpr (i >= 0) mfma_v(ak, wg[i][0], xg[i]);
                    SBAR();
                    if constexpr (n < 28) { wg[n][0] = lds128(at); xg[n] = lds128(xo[s] + xb_slot(p) * 16384); }
                    request(3 * s);
                    SBAR();
                    if constexpr (i >= 0) mfma_v(av, xg[i], wg[i][1]);
                    SBAR();
                    if constexpr (n < 28) wg[n][1] = lds128(at + 1024);
                    request(3 * s + 1);
                    SBAR();
                    if constexpr (i >= 0) mfma_v(aq, wg[i][2], xg[i]);
                    SBAR();
                    if constexpr (n < 28) wg[n][2] = lds128(qba + n * 1024);
                    request(3 * s + 2);
                    SBAR();
                });
            }
            STAMP(6);
            u32x4 wp[2];
            {
                const unsigned fc = 16 * ((lane >> 4) & 1) + 4 * (lane & 3);
                const unsigned a0 = L_WPHI + (4 * hi + q4) * 56 + (fc < 28 ? fc : 24) * 2;
                wp[0] = tr_frag(a0, a0 + 8 * 56); wp[1] = tr_frag(a0 + 16 * 56, a0 + (hi ? 20 : 24) * 56);
            }
            wait_vm<0>();
            barrier();
            {
                const unsigned at = L_X + 2 * 16384 + wid * 4096 + (4 * hi + q4) * 64 + col;
                st128_async(tr_frag(at, at + 512), rws, rt_v, WS_WO + gw * 1024);
            }
            u32x4 wqf[28];
#pragma unroll
            for (int i = 0; i < 6; ++i) wqf[i] = lds128(l_wq(i >> 2) + (i & 3) * 1024 + voff);
            asm volatile("s_nop 1" : "+v"(aq), "+v"(ak), "+v"(av));
            STAMP(7);
            f32x16 pkk, pqq, qa;
            {
                u32x4 f0 = pack_lo(ak), f1 = pack_hi(ak);
                if (hi) { f1[2] = 0; f1[3] = 0; }
                asm volatile("" : "+v"(f0), "+v"(f1));
                mfma_v0(pkk, wp[0], f0); mfma_v(pkk, wp[1], f1);
            }
            u32x4 g0 = pack_lo(aq), g1 = pack_hi(aq);
            if (hi) { g1[2] = 0; g1[3] = 0; }
            asm volatile("" : "+v"(g0), "+v"(g1));
#define QSTEP(i) do { SBAR(); if (((i) & 3) == 0) pin(wqf[i], wqf[(i) + 1], wqf[(i) + 2], wqf[(i) + 3]); \
                      if ((i) == 0) mfma_v0(qa, wqf[0], xf[0]); else mfma_v(qa, wqf[i], xf[i]); \
                      SBAR(); if ((i) + 6 < 28) wqf[(i) + 6] = lds128(l_wq(((i) + 6) >> 2) + (((i) + 6) & 3) * 1024 + voff); SBAR(); } while (0)
            QSTEP(0);
            u32x4 v0 = pack_lo(av);
            QSTEP(1);
            u32x4 v1 = pack_hi(av);
            if (r32 >= 28) { const unsigned v = r32 == 28 ? 0x3F803F80u : 0u; v0 = u32x4{v, v, v, v}; v1 = v0; }
            asm volatile("" : "+v"(pkk), "+v"(v0), "+v"(v1));
            QSTEP(2);
            const unsigned img = L_PHI + wid * 4096;
            {
                f32x16 ep, en;
                const float hx = max16(pkk), hn = min16(pkk);
                QSTEP(3);
                const float mx = both_max(hx) * LOG2E, mn = both_min(hn) * LOG2E;
                const float live = hi ? 0.f : 1.f;
                float sp = 0.f, sn = 0.f;
                float ap = fmaf(pkk[15], LOG2E, -mx), an = fmaf(pkk[15], -LOG2E, mn);
#pragma unroll
                for (int i = 0; i < 16; ++i) {
                    QSTEP(4 + i);
                    ep[15 - i] = __builtin_amdgcn_exp2f(ap); en[15 - i] = __builtin_amdgcn_exp2f(an);
                    SBAR();
                    if (i < 15) { ap = fmaf(pkk[14 - i], LOG2E, -mx); an = fmaf(pkk[14 - i], -LOG2E, mn); }
                    if (i == 1) { sp = ep[15]; sn = en[15]; }
                    if (i > 1) { sp += ep[16 - i]; sn += en[16 - i]; }
                    if (i == 4) { sp *= live; sn *= live; }
                    if (i == KP_PEEK) peek(pk_, ws, pk_word);
                    if (i == KP_SEEN) { seen<0>(pk_, ws, pk_word, nonce); STAMP(8); LDKV(a, 0); }
                }
                QSTEP(20);
                const float rp = __builtin_amdgcn_rcpf(both_sum(sp + ep[0])), rn = __builtin_amdgcn_rcpf(both_sum(sn + en[0]));
                const float rp3 = rp * live, rn3 = rn * live;
                const unsigned wr = img + (r32 & 15) * 136 + (r32 >> 4) * 16384 + hi * 8;
#pragma unroll
                for (int g = 0; g < 4; ++g) {
                    const float u = g == 3 ? rp3 : rp, v = g == 3 ? rn3 : rn;
                    QSTEP(21 + 2 * g);
                    sts64(wr + g * 16, u32x2{pk(ep[4 * g] * u, ep[4 * g + 1] * u), pk(ep[4 * g + 2] * u, ep[4 * g + 3] * u)});
                    if (g < 3) QSTEP(22 + 2 * g);
                    sts64(wr + 64 + g * 16, u32x2{pk(en[4 * g] * v, en[4 * g + 1] * v), pk(en[4 * g + 2] * v, en[4 * g + 3] * v)});
                }
            }
#undef QSTEP
            u32x4 pf[4];
            {
                const unsigned rd = img + (4 * hi + q4) * 136 + col;
#pragma unroll
                for (int i = 0; i < 4; ++i) pf[i] = tr_frag(rd + (i >> 1) * 64 + (i & 1) * 16384, rd + (i >> 1) * 64 + (i & 1) * 16384 + 8 * 136);
            }
            SBAR(); mfma_v0(pqq, wp[0], g0); SBAR(); LDKV(b, 1); SBAR(); mfma_v(pqq, wp[1], g1); SBAR(); LDKV(c, 2);
            asm volatile("" : "+v"(qa));
            {
                qa *= QSCALE;
                u32x4 q0 = pack_lo(qa), q1 = pack_hi(qa);
                if (hi) { q1[2] = 0; q1[3] = 0; }
                sts128(L_QX + wid * 2048 + voff, q0); sts128(L_QX + wid * 2048 + 1024 + voff, q1);
            }
            f32x16 kv0, kv1, sc[4];
            SBAR(); mfma_v0(kv0, pf[0], v0); SBAR();
            asm volatile("" : "+v"(pqq));
            const float qhx = max16(pqq);
            SBAR(); mfma_v(kv0, pf[1], v1); SBAR();
            lds_bar();
#pragma unroll
            for (int t = 0; t < 4; ++t) { const unsigned at = L_QX + ((wid + t) & 3) * 2048 + voff; q[t][0] = lds128(at); q[t][1] = lds128(at + 1024); }
            const float qhn = min16(pqq);
            SBAR(); mfma_v0(kv1, pf[2], v0); SBAR();
            const float qmx = both_max(qhx) * LOG2E, qmn = both_min(qhn) * LOG2E;
            SBAR(); mfma_v(kv1, pf[3], v1); SBAR();
            landed<8>(a.k0, a.k1, a.v0, a.v1);
            f32x16 eqp, eqn;
#pragma unroll
            for (int i = 0; i < 8; ++i) {
                const float a0 = fmaf(pqq[2 * i], LOG2E, -qmx), a1 = fmaf(pqq[2 * i + 1], LOG2E, -qmx), n0 = fmaf(pqq[2 * i], -LOG2E, qmn), n1 = fmaf(pqq[2 * i + 1], -LOG2E, qmn);
                SBAR();
                if (i & 1) mfma_v(sc[i >> 1], a.k1, q[i >> 1][1]); else mfma_v0(sc[i >> 1], a.k0, q[i >> 1][0]);
                SBAR();
                eqp[2 * i] = __builtin_amdgcn_exp2f(a0); eqp[2 * i + 1] = __builtin_amdgcn_exp2f(a1); eqn[2 * i] = __builtin_amdgcn_exp2f(n0); eqn[2 * i + 1] = __builtin_amdgcn_exp2f(n1);
                if (i == 0) asm volatile("" : "+v"(kv0));
                if (i == 4) asm volatile("" : "+v"(kv1));
                sts128(L_RED + wid * 8192 + i * 1024 + voff, quad(i < 4 ? kv0 : kv1, i & 3));
                SBAR();
            }
            eq[0] = pack_lo(eqp); eq[1] = pack_hi(eqp); eq[2] = pack_lo(eqn); eq[3] = pack_hi(eqn);
            asm volatile("" : "+v"(eq[0]), "+v"(eq[1]), "+v"(eq[2]), "+v"(eq[3]), "+v"(sc[0]), "+v"(sc[1]), "+v"(sc[2]), "+v"(sc[3]));
            {
                const float m[4] = {max16(sc[0]), max16(sc[1]), max16(sc[2]), max16(sc[3])};
                ref_post(wid, lane, m);
            }
            lds_bar();
            {
                ref_take(wid, lane, q, REF_OFFSET);
#pragma unroll
                for (int i = 0; i < 2; ++i) {
                    const unsigned off = (2 * wid + i) * 1024 + voff;
                    f32x4 u = __builtin_bit_cast(f32x4, lds128(L_RED + off));
#pragma unroll
                    for (int v = 1; v < 4; ++v) u += __builtin_bit_cast(f32x4, lds128(L_RED + v * 8192 + off));
                    st128_async(__builtin_bit_cast(u32x4, u), rws, off, WS_KVP + (h * 16 + rb) * 8192);
                }
                STAMP(9);
            }
        }
    }
    const int qt = rb * 4 + (h & 3), nt = (h & 12) + wid;
    const bool live = nt < 14;
    u32x4 wob_f[32], woa_f[32], f0, f1;
    const unsigned wo_at = WS_WO + (live ? nt : 0) * 32768;
    const unsigned o_mine = (tile * 16 + h) * 2048, o_need = (qt * 16 + wid * 4) * 2048;
    {
        const int lane = lane_id(), hi = lane >> 5, r32 = lane & 31;
        const unsigned voff = lane * 16;
        STAMP(10);
        f32x16 o0 = {}, o1 = {}, o2 = {}, o3 = {};
        f32x16 sa, sb; u32x4 pa, pb = {};
        float e12 = 0.f, e13 = 0.f, e14 = 0.f, e15 = 0.f;
        d.v1 = u32x4{};
#define QK0(S, K, Q) asm volatile("v_mfma_f32_32x32x16_bf16 %0, %1, %2, 0" : "=&v"(S) : "v"(K), "v"(Q))
#define QK1(S, K, Q) asm volatile("v_mfma_f32_32x32x16_bf16 %0, %1, %2, %0" : "+v"(S) : "v"(K), "v"(Q))
        asm volatile("s_nop 1" : "+v"(q[0][1]), "+v"(q[1][1]), "+v"(q[2][1]), "+v"(q[3][1]));
        QK0(sa, a.k0, q[0][0]); QK1(sa, a.k1, q[0][1]);
        asm volatile("s_nop 11" : "+v"(sa));
#define EX(S, i) S[i] = __builtin_amdgcn_exp2f(S[i])
#define STEP(NEXT, SC, SN, KN0, KN1, QN0, QN1, OP, VP1, OC, VC0) do { \
    if (NEXT) QK0(SN, KN0, QN0); SBAR(); \
    pb[2] = pk(e12, e13); SBAR(); EX(SC, 0); SBAR(); pb[3] = pk(e14, e15); SBAR(); EX(SC, 1); EX(SC, 2); EX(SC, 3); SBAR(); \
    OP = mfma(VP1, pb, OP); SBAR(); \
    EX(SC, 4); EX(SC, 5); SBAR(); pa[0] = pk(SC[0], SC[1]); SBAR(); EX(SC, 6); SBAR(); pa[1] = pk(SC[2], SC[3]); SBAR(); EX(SC, 7); SBAR(); \
    if (NEXT) QK1(SN, KN1, QN1); SBAR(); \
    EX(SC, 8); SBAR(); pa[2] = pk(SC[4], SC[5]); SBAR(); EX(SC, 9); SBAR(); pa[3] = pk(SC[6], SC[7]); SBAR(); EX(SC, 10); EX(SC, 11); SBAR(); \
    OC = mfma(VC0, pa, OC); SBAR(); \
    e12 = __builtin_amdgcn_exp2f(SC[12]); SBAR(); pb[0] = pk(SC[8], SC[9]); SBAR(); e13 = __builtin_amdgcn_exp2f(SC[13]); SBAR(); pb[1] = pk(SC[10], SC[11]); SBAR(); \
    e14 = __builtin_amdgcn_exp2f(SC[14]); e15 = __builtin_amdgcn_exp2f(SC[15]); SBAR(); } while (0)
#define TILE(J, CUR, PREV, NXT, NW, SEAM, MORE) do { \
    STEP(true, sa, sb, CUR.k0, CUR.k1, q[1][0], q[1][1], o3, PREV.v1, o0, CUR.v0); \
    if (J + 3 <= 15) LDKV(PREV, J + 3); \
    SEAM; SBAR(); \
    STEP(true, sb, sa, CUR.k0, CUR.k1, q[2][0], q[2][1], o0, CUR.v1, o1, CUR.v0); \
    MORE(0, 3); SBAR(); \
    STEP(true, sa, sb, CUR.k0, CUR.k1, q[3][0], q[3][1], o1, CUR.v1, o2, CUR.v0); \
    MORE(3, 6); \
    if (J < 15) landed<NW>(NXT.k0, NXT.k1, NXT.v0, NXT.v1); \
    SBAR(); \
    STEP(J < 15, sb, sa, NXT.k0, NXT.k1, q[0][0], q[0][1], o2, CUR.v1, o3, CUR.v0); \
    MORE(6, 8); SBAR(); } while (0)
        u32x4 pl[2], sf[4];
        u32x4 pp;
        u32x2 ps;
        const unsigned pp_word = lane < 8 ? WS_FP + (h * 4 + (rb >> 2)) * 128 + lane * 16 : WS_FWO + (((lane >> 4) & 1) * 14 + (live ? nt : 0)) * 256 + (lane & 15) * 16;
        const unsigned ps_word = WS_FS + h * 512 + lane * 8;
        auto slice_load = [&]() {
            const unsigned at = WS_KVP + h * 16 * 8192 + tile * 128, v = (lane >> 3) * 8192 + (lane & 7) * 16;
            ld128_async_dev(pl[0], rws, v, at); ld128_async_dev(pl[1], rws, v, at + 8 * 8192);
        };
        auto slice_sum = [&]() {
            asm volatile("s_waitcnt vmcnt(4)" : "+v"(pl[0]), "+v"(pl[1]) :: "memory");
            float u[4];
#pragma unroll
            for (int e = 0; e < 4; ++e) {
                u[e] = both_sum(__uint_as_float(pl[0][e]) + __uint_as_float(pl[1][e]));
                auto r = __builtin_amdgcn_permlane16_swap(__float_as_uint(u[e]), __float_as_uint(u[e]), false, false);
                u[e] = __uint_as_float(r[0]) + __uint_as_float(r[1]);
                u[e] += __uint_as_float(__builtin_amdgcn_update_dpp(0u, __float_as_uint(u[e]), 0x128, 0xf, 0xf, false));
            }
            const u32x2 v = {pk(u[0], u[1]), pk(u[2], u[3])};
            const unsigned at = WS_ST + h * 4096 + (rb >> 2) * 1024 + ((rb & 1) * 32 + wid * 8) * 16 + ((rb >> 1) & 1) * 8;
            asm volatile("buffer_store_dwordx2 %0, %1, %2, %3 offen sc1" ::"v"(v), "v"(lane < 8 ? voff : OOB), "s"(rws), "s"(at) : "memory");
        };
        auto state_load = [&]() {
#pragma unroll
            for (int i = 0; i < 4; ++i) ld_frag<false>(sf[i], rws, voff, WS_ST + h * 4096, i);
        };
        auto wo_load = [&](int n, int first, int last) {
#pragma unroll
            for (int i = 0; i < 8; ++i) if (i >= first && i < last) ld_frag<true>(wob_f[8 * n + i], rws, voff, wo_at + 14 * 32768, 8 * n + i);
        };
        auto wa_load = [&](int n, int first, int last) {
#pragma unroll
            for (int i = 0; i < 8; ++i) if (i >= first && i < last) {
                if (n == 3) ld_frag<true>(woa_f[8 * n + i], rws, voff, wo_at, 8 * n + i); else ld_frag<false>(woa_f[8 * n + i], rws, voff, wo_at, 8 * n + i);
            }
        };
        f32x16 np, nn;
        auto start_b = [&]() {
            landed<4>(sf[0], sf[1], sf[2], sf[3]);
            if (r32 == 29) {
                const unsigned tail = hi ? 0u : 0x3F803F80u;
                sf[0] = u32x4{0x3F803F80u, 0x3F803F80u, 0x3F803F80u, 0x3F803F80u}; sf[2] = sf[0];
                sf[1] = u32x4{0x3F803F80u, 0x3F803F80u, tail, tail}; sf[3] = sf[1];
            }
            asm volatile("" : "+v"(sf[0]), "+v"(sf[1]), "+v"(sf[2]), "+v"(sf[3]));
            mfma_v0(np, sf[0], eq[0]); mfma_v(np, sf[1], eq[1]); mfma_v0(nn, sf[2], eq[2]); mfma_v(nn, sf[3], eq[3]);
        };
        auto finish_b = [&](int first, int last) {
            if (first != 0) return;
            asm volatile("" : "+v"(np), "+v"(nn));
            const float rp_ = __builtin_amdgcn_rcpf(np[13]), rn_ = __builtin_amdgcn_rcpf(nn[13]);
            const float inv = __builtin_amdgcn_rcpf(fmaf(np[12], rp_, fmaf(nn[12], rn_, 1e-6f)));
            const float ap = upper_val(rp_ * inv), an = upper_val(rn_ * inv);
            f32x16 o;
#pragma unroll
            for (int i = 0; i < 16; ++i) o[i] = fmaf(np[i], ap, nn[i] * an);
            u32x4 g0 = pack_lo(o), g1 = pack_hi(o);
            if (hi) { g1[2] = 0; g1[3] = 0; }
            st128_async(g0, rws, voff, WS_OB + o_mine); st128_async<1024>(g1, rws, voff, WS_OB + o_mine);
        };
#define NONE(x, y) (void)0
#define WB0(x, y) wo_load(0, x, y)
#define WB1(x, y) wo_load(1, x, y)
#define WB2(x, y) wo_load(2, x, y)
#define WB3(x, y) wo_load(3, x, y)
#define WA0(x, y) wa_load(0, x, y)
#define WA1(x, y) wa_load(1, x, y)
#define WA2(x, y) wa_load(2, x, y)
#define WA3(x, y) wa_load(3, x, y)
        TILE(0, a, d, b, 10, (void)0, NONE);
        TILE(1, b, a, c, 12, (wait_vm<8>(), publish(ws, WS_FP + ((h * 4 + wid) * 16 + rb) * 8, nonce), publish(ws, (blk < 224 ? WS_FWO : WS_VOID) + (blk * 4 + wid) * 8, nonce)), NONE);
        TSTAMP(19); TILE(2, c, b, d, 10, (void)0, NONE);
        TILE(3, d, c, a, 10, (void)0, NONE);
        TILE(4, a, d, b, 9, peek(pp, ws, pp_word), NONE);
        TSTAMP(20); TILE(5, b, a, c, 11, (seen<4>(pp, ws, pp_word, nonce), slice_load()), NONE);
        TSTAMP(21); TILE(6, c, b, d, 18, slice_sum(), WB0);
        TSTAMP(22); TILE(7, d, c, a, 25, (void)0, WB1);
        TILE(8, a, d, b, 32, (wait_vm<24>(), publish(ws, WS_FS + (h * 64 + tile) * 8, nonce)), WB2);
        TILE(9, b, a, c, 31, (void)0, WB3);
        TILE(10, c, b, d, 32, peek(ps, ws, ps_word), WA3);
        TSTAMP(23); TILE(11, d, c, a, 29, (seen<12>(ps, ws, ps_word, nonce), state_load()), NONE);
        TSTAMP(24); TILE(12, a, d, b, 23, start_b(), finish_b);
        TSTAMP(25); TILE(13, b, a, c, 16, (void)0, WA0);
        TILE(14, c, b, d, 17, (wait_vm<8>(), publish(ws, WS_FOB + (tile * 16 + h) * 8, nonce)), WA1);
        TSTAMP(26); TILE(15, d, c, a, 0, (void)0, WA2);
        pb[2] = pk(e12, e13); pb[3] = pk(e14, e15);
        o3 = mfma(d.v1, pb, o3);
#undef TILE
#undef STEP
#undef EX
#undef QK0
#undef QK1
#undef NONE
#undef WB0
#undef WB1
#undef WB2
#undef WB3
#undef WA0
#undef WA1
#undef WA2
#undef WA3
        STAMP(11);
        combine_post(wid, lane, o1, o2, o3);
        u32x2 pob;
        const unsigned pob_word = WS_FOB + (qt * 16 + wid * 4 + (lane & 3)) * 8;
        peek(pob, ws, pob_word);
        lds_bar();
        const bool bad = combine_take(wid, lane, o0, f0, f1);
        st128_async(f0, rws, voff, WS_OA + o_mine); st128_async<1024>(f1, rws, voff, WS_OA + o_mine);
        STAMP(12);
        if (__any(bad) && lane == 0) *(LAS unsigned*)(size_t)L_FLAG = 1;
        seen<2>(pob, ws, pob_word, nonce);
#pragma unroll
        for (int i = 0; i < 8; ++i) dma_run_dev(rws, L_OIMG + wid * 8192, voff, WS_OB + o_need, i);
        wait_bar<8>();
        if (__builtin_amdgcn_readfirstlane(*(const LAS unsigned*)(size_t)L_FLAG)) {
            float ref[4];
#pragma unroll
            for (int t = 0; t < 4; ++t) { if (hi) { q[t][1][2] = 0; q[t][1][3] = 0; } ref[t] = -__builtin_inff(); }
#pragma unroll 1
            for (int j = 0; j < 16; ++j) {
                const u32x4 k0 = ld128_dev(rws, voff, kv_at + j * 4096), k1 = ld128_dev(rws, voff + 1024, kv_at + j * 4096);
#pragma unroll
                for (int t = 0; t < 4; ++t) { f32x16 s = mfma(k0, q[t][0], zero); s = mfma(k1, q[t][1], s); ref[t] = fmaxf(ref[t], max16(s)); }
            }
            ref_post(wid, lane, ref);
            lds_bar();
            ref_take(wid, lane, q, 0.f);
            lds_bar();
            f32x16 x[4] = {};
#pragma unroll 1
            for (int j = 0; j < 16; ++j) {
                const u32x4 k0 = ld128_dev(rws, voff, kv_at + j * 4096), k1 = ld128_dev(rws, voff + 1024, kv_at + j * 4096);
                const u32x4 v0 = ld128_dev(rws, voff + 2048, kv_at + j * 4096), v1 = ld128_dev(rws, voff + 3072, kv_at + j * 4096);
#pragma unroll
                for (int t = 0; t < 4; ++t) {
                    f32x16 s = mfma(k0, q[t][0], zero); s = mfma(k1, q[t][1], s);
#pragma unroll
                    for (int i = 0; i < 16; ++i) s[i] = __builtin_amdgcn_exp2f(s[i]);
                    x[t] = mfma(v0, pack_lo(s), x[t]); x[t] = mfma(v1, pack_hi(s), x[t]);
                }
            }
            combine_post(wid, lane, x[1], x[2], x[3]);
            lds_bar();
            combine_take(wid, lane, x[0], f0, f1);
            st128_async(f0, rws, voff, WS_OA + o_mine); st128_async<1024>(f1, rws, voff, WS_OA + o_mine);
            wait_vm<0>();
        }
        publish(ws, WS_FOA + (tile * 16 + h) * 8, nonce);
        STAMP(13);
    }
#undef LDKV
    {
        const int lane = lane_id();
        const unsigned voff = lane * 16;
#pragma unroll
        for (int i = 0; i < 32; i += 8)
            asm volatile("" : "+a"(wob_f[i]), "+a"(wob_f[i + 1]), "+a"(wob_f[i + 2]), "+a"(wob_f[i + 3]), "+a"(wob_f[i + 4]), "+a"(wob_f[i + 5]), "+a"(wob_f[i + 6]), "+a"(wob_f[i + 7]));
        f32x16 r = {};
        asm volatile("s_nop 1" : "+v"(r));
        u32x4 of[32];
        u32x2 poa;
        const unsigned poa_word = WS_FOA + (qt * 16 + wid * 4 + (lane & 3)) * 8;
        wait_vm<1>();
        barrier();
#pragma unroll
        for (int i = 0; i < 6; ++i) of[i] = lds128(L_OIMG + i * 1024 + voff);
#pragma unroll
        for (int i = 0; i < 32; ++i) {
            if (i + 6 < 32) of[i + 6] = lds128(L_OIMG + (i + 6) * 1024 + voff);
            SBAR();
            asm volatile("v_mfma_f32_32x32x16_bf16 %0, %1, %2, %0" : "+v"(r) : "a"(wob_f[i]), "v"(of[i]));
            if (i == PEEK_AT) peek(poa, ws, poa_word);
            if (i == SEEN_AT) { seen<0>(poa, ws, poa_word, nonce); STAMP(15); }
            if (i >= SEEN_AT && i < SEEN_AT + 8) dma_run_dev(rws, L_OIMG2 + wid * 8192, voff, WS_OA + o_need, i - SEEN_AT);
            SBAR();
        }
        settle(r);
        stage_out(L_OUT, wid, lane, r);
        STAMP(14);
        wait_bar<0>();
        if (blk == 0 && wid == 0) publish(ws, WS_EPOCH, nonce);
#pragma unroll
        for (int i = 0; i < 24; i += 8)
            asm volatile("" : "+v"(woa_f[i]), "+v"(woa_f[i + 1]), "+v"(woa_f[i + 2]), "+v"(woa_f[i + 3]), "+v"(woa_f[i + 4]), "+v"(woa_f[i + 5]), "+v"(woa_f[i + 6]), "+v"(woa_f[i + 7]));
        asm volatile("" : "+a"(woa_f[24]), "+a"(woa_f[25]), "+a"(woa_f[26]), "+a"(woa_f[27]), "+a"(woa_f[28]), "+a"(woa_f[29]), "+a"(woa_f[30]), "+a"(woa_f[31]));
        f32x16 s;
#pragma unroll
        for (int i = 0; i < 6; ++i) of[i] = lds128(L_OIMG2 + i * 1024 + voff);
#pragma unroll
        for (int i = 0; i < 32; ++i) {
            if (i + 6 < 32) of[i + 6] = lds128(L_OIMG2 + (i + 6) * 1024 + voff);
            SBAR();
            if (i == 0) mfma_v0(s, woa_f[0], of[0]); else if (i < 24) mfma_v(s, woa_f[i], of[i]);
            else asm volatile("v_mfma_f32_32x32x16_bf16 %0, %1, %2, %0" : "+v"(s) : "a"(woa_f[i]), "v"(of[i]));
            if (i == 4) flush_out(L_OUT, wid, lane, h, qt, 1, (u16*)args[7]);
            SBAR();
        }
        settle(s);
        STAMP(16);
        stage_out(L_OUT + 16384, wid, lane, s);
        lds_bar();
        asm volatile(".Lfused_code_end:");
        flush_out(L_OUT + 16384, wid, lane, h, qt, 0, (u16*)args[7]);
    }
    STAMP(17);
    if (blockIdx.x == 77 && wid == 0) {
        const u64 target = dg0 + 10000 + (DIAG < 0 ? 0 : 4 * (dg1 - dg0));
        while (__builtin_amdgcn_s_memrealtime() < target) __builtin_amdgcn_s_sleep(1);
    }
}

}

extern "C" void kernel_launch(void* const* d_in, const int* in_sizes, int n_in, void* d_out, int out_size, void* d_ws, size_t ws_size, hipStream_t stream) {
    (void)hipFuncSetAttribute((const void*)fused, hipFuncAttributeMaxDynamicSharedMemorySize, LDS_BYTES);
    fused<<<256, 256, LDS_BYTES, stream>>>((const u16*)d_in[0], (const u16*)d_in[1], (const u16*)d_in[2], (const u16*)d_in[3], (const u16*)d_in[4], (const u16*)d_in[5],
                                           (const u16*)d_in[6], (u16*)d_out, (char*)d_ws);
}
```

```cpp
#include <hip/hip_runtime.h>
#include <stdint.h>

namespace {

typedef unsigned short u16;
typedef unsigned long long u64;
typedef __attribute__((ext_vector_type(16))) float f32x16;
typedef __attribute__((ext_vector_type(4))) float f32x4;
typedef __attribute__((ext_vector_type(2))) float f32x2;
typedef __attribute__((ext_vector_type(8))) __bf16 bf16x8;
typedef __attribute__((ext_vector_type(2))) __bf16 bf16x2;
typedef __attribute__((ext_vector_type(4))) unsigned u32x4;
typedef __attribute__((ext_vector_type(2))) unsigned u32x2;
typedef __attribute__((ext_vector_type(4))) short s16x4;
typedef __attribute__((ext_vector_type(8))) unsigned long long u64x8;
typedef __amdgpu_buffer_rsrc_t rsrc_t;
#define LAS __attribute__((address_space(3)))
#define GAS __attribute__((address_space(1)))
#define DI __device__ __forceinline__
#define SBAR() __builtin_amdgcn_sched_barrier(0)

constexpr int SEQ = 2048, DM = 448;
constexpr unsigned XROW = DM * 2;
constexpr unsigned WROW = 3 * DM * 2;
constexpr unsigned X_BYTES = SEQ * XROW, W_BYTES = DM * WROW, WO_BYTES = DM * XROW;
constexpr unsigned WS_EPOCH = 0;
constexpr unsigned WS_FK = 4096;
constexpr unsigned WS_FP = WS_FK + 8192;
constexpr unsigned WS_FOA = WS_FP + 8192;
constexpr unsigned WS_FOB = WS_FOA + 8192;
constexpr unsigned WS_FW = WS_FOB + 8192;
constexpr unsigned WS_FWO = WS_FW + 8192;
constexpr unsigned WS_FS = WS_FWO + 8192;
constexpr unsigned WS_VOID = WS_FS + 8192;
constexpr unsigned WS_KVA = 131072;
constexpr unsigned WS_OA = WS_KVA + (4u << 20);
constexpr unsigned WS_OB = WS_OA + (2u << 20);
constexpr unsigned WS_KVP = WS_OB + (2u << 20);
constexpr unsigned WS_WB = WS_KVP + (2u << 20);
constexpr unsigned WS_WO = WS_WB + (2u << 20);
constexpr unsigned WS_ST = WS_WO + (1u << 20);
constexpr unsigned WS_END = WS_ST + 65536;
constexpr int LDS_BYTES = 160 * 1024;
constexpr unsigned L_WKV = 0, L_RET = 57344, L_QB = 57344;
constexpr unsigned L_X = 86016;
constexpr unsigned l_kvb(int ch) { return ch == 0 ? 151552 : (ch - 1) * 8192; }
constexpr unsigned l_wq(int ch) { return ch < 6 ? ch * 4096 : 49152; }
constexpr unsigned L_QX = 151552, L_REF = 40960, L_WPHI = 159744, L_FLAG = 161792;
constexpr unsigned L_PHI = 86016, L_RED = 118784;
constexpr unsigned L_COMB = 0, L_OIMG = 65536, L_OIMG2 = 0, L_OUT = 98304;
constexpr float LOG2E = 1.4426950408889634f;
constexpr float QSCALE = 0.18898223650461362f * LOG2E;
#define REF_OFF 100.f
constexpr float REF_OFFSET = REF_OFF;
constexpr float L_LIMIT = 18446744073709551616.f;
constexpr int SC1 = 16;
constexpr unsigned OOB = 0x80000000u;
#define SPINLOG 16
constexpr unsigned SPIN_CAP = 1u << SPINLOG;
#define PEEK_AT 4
#define SEEN_AT 18
#define KP_PEEK 4
#define KP_SEEN 15
#define PSLEEP 4

DI f32x16 mfma(u32x4 a, u32x4 b, f32x16 c) {
    return __builtin_amdgcn_mfma_f32_32x32x16_bf16(__builtin_bit_cast(bf16x8, a), __builtin_bit_cast(bf16x8, b), c, 0, 0, 0);
}
DI unsigned pk(float lo, float hi) { f32x2 v = {lo, hi}; return __builtin_bit_cast(unsigned, __builtin_convertvector(v, bf16x2)); }
DI u32x4 pack_lo(const f32x16& x) { return u32x4{pk(x[0], x[1]), pk(x[2], x[3]), pk(x[4], x[5]), pk(x[6], x[7])}; }
DI u32x4 pack_hi(const f32x16& x) { return u32x4{pk(x[8], x[9]), pk(x[10], x[11]), pk(x[12], x[13]), pk(x[14], x[15])}; }
DI u32x4 quad(const f32x16& x, int g) { return u32x4{__float_as_uint(x[4 * g]), __float_as_uint(x[4 * g + 1]), __float_as_uint(x[4 * g + 2]), __float_as_uint(x[4 * g + 3])}; }
DI rsrc_t make_rsrc(const void* p, unsigned bytes) { return __builtin_amdgcn_make_buffer_rsrc((void*)p, 0, bytes, 0x00020000); }
DI u32x4 ld128(rsrc_t rs, unsigned voff, unsigned soff) { return __builtin_amdgcn_raw_buffer_load_b128(rs, voff, soff, 0); }
DI u32x4 ld128_dev(rsrc_t rs, unsigned voff, unsigned soff) { return __builtin_amdgcn_raw_buffer_load_b128(rs, voff, soff, SC1); }
DI void st128_dev(u32x4 v, rsrc_t rs, unsigned voff, unsigned soff) { __builtin_amdgcn_raw_buffer_store_b128(v, rs, voff, soff, SC1); }
DI void dma16(rsrc_t rs, unsigned lds_addr, unsigned voff, unsigned soff) {
    asm volatile("s_mov_b32 m0, %0\n\ts_nop 0\n\tbuffer_load_dwordx4 %1, %2, %3 offen lds" ::"s"(lds_addr), "v"(voff), "s"(rs), "s"(soff) : "memory");
}
DI void ld128_async(u32x4& d, rsrc_t rs, unsigned voff, unsigned soff) {
    asm volatile("buffer_load_dwordx4 %0, %1, %2, %3 offen" : "=v"(d) : "v"(voff), "s"(rs), "s"(soff) : "memory");
}
DI void dma16_dev(rsrc_t rs, unsigned lds_addr, unsigned voff, unsigned soff) {
    asm volatile("s_mov_b32 m0, %0\n\ts_nop 0\n\tbuffer_load_dwordx4 %1, %2, %3 offen sc1 lds" ::"s"(lds_addr), "v"(voff), "s"(rs), "s"(soff) : "memory");
}
template <int OFF> DI void dma_next(rsrc_t rs, unsigned voff, unsigned soff) {
    asm volatile("buffer_load_dwordx4 %0, %1, %2 offen offset:%3 lds" ::"v"(voff), "s"(rs), "s"(soff), "n"(OFF) : "memory");
}
template <int OFF> DI void dma_next_dev(rsrc_t rs, unsigned voff, unsigned soff) {
    asm volatile("buffer_load_dwordx4 %0, %1, %2 offen offset:%3 sc1 lds" ::"v"(voff), "s"(rs), "s"(soff), "n"(OFF) : "memory");
}
DI void dma_run_dev(rsrc_t rs, unsigned lds, unsigned voff, unsigned base, int i) {
    const unsigned s = base + (i >> 2) * 4096;
    if ((i & 3) == 0) dma16_dev(rs, lds + i * 1024, voff, s); else if ((i & 3) == 1) dma_next_dev<1024>(rs, voff, s);
    else if ((i & 3) == 2) dma_next_dev<2048>(rs, voff, s); else dma_next_dev<3072>(rs, voff, s);
}
template <int N> DI void wait_vm() { asm volatile("s_waitcnt vmcnt(%0)" ::"n"(N) : "memory"); }
template <int N> DI void wait_bar() { asm volatile("s_waitcnt vmcnt(%0) lgkmcnt(0)\n\ts_barrier" ::"n"(N) : "memory"); }
DI void lds_bar() { asm volatile("s_waitcnt lgkmcnt(0)\n\ts_barrier" ::: "memory"); }
DI u32x4 lds128(unsigned a) { return *(const LAS u32x4*)(size_t)a; }
DI void sts128(unsigned a, u32x4 v) { *(LAS u32x4*)(size_t)a = v; }
DI void sts64(unsigned a, u32x2 v) { *(LAS u32x2*)(size_t)a = v; }
DI u32x2 tr64(unsigned a) { return __builtin_bit_cast(u32x2, __builtin_amdgcn_ds_read_tr16_b64_v4i16((LAS s16x4*)(size_t)a)); }
DI u32x4 tr_frag(unsigned a0, unsigned a1) { u32x2 lo = tr64(a0), hi = tr64(a1); return u32x4{lo[0], lo[1], hi[0], hi[1]}; }
DI float max3(float a, float b, float c) { float r; asm("v_max3_f32 %0, %1, %2, %3" : "=v"(r) : "v"(a), "v"(b), "v"(c)); return r; }
DI float min3(float a, float b, float c) { float r; asm("v_min3_f32 %0, %1, %2, %3" : "=v"(r) : "v"(a), "v"(b), "v"(c)); return r; }
DI float both_max(float v) { auto r = __builtin_amdgcn_permlane32_swap(__float_as_uint(v), __float_as_uint(v), false, false); return max3(__uint_as_float(r[0]), __uint_as_float(r[1]), __uint_as_float(r[1])); }
DI float both_min(float v) { auto r = __builtin_amdgcn_permlane32_swap(__float_as_uint(v), __float_as_uint(v), false, false); return min3(__uint_as_float(r[0]), __uint_as_float(r[1]), __uint_as_float(r[1])); }
DI float both_sum(float v) { auto r = __builtin_amdgcn_permlane32_swap(__float_as_uint(v), __float_as_uint(v), false, false); return __uint_as_float(r[0]) + __uint_as_float(r[1]); }
DI float upper_val(float v) { auto r = __builtin_amdgcn_permlane32_swap(__float_as_uint(v), __float_as_uint(v), false, false); return __uint_as_float(r[1]); }
DI float max16(const f32x16& s) {
    const float a = max3(s[0], s[1], s[2]), b = max3(s[3], s[4], s[5]), c = max3(s[6], s[7], s[8]), d = max3(s[9], s[10], s[11]), e = max3(s[12], s[13], s[14]);
    return max3(max3(a, b, c), max3(d, e, s[15]), a);
}
DI float min16(const f32x16& s) {
    const float a = min3(s[0], s[1], s[2]), b = min3(s[3], s[4], s[5]), c = min3(s[6], s[7], s[8]), d = min3(s[9], s[10], s[11]), e = min3(s[12], s[13], s[14]);
    return min3(min3(a, b, c), min3(d, e, s[15]), a);
}

template <int OFF = 0> DI void ld128_async_dev(u32x4& d, rsrc_t rs, unsigned voff, unsigned soff) {
    asm volatile("buffer_load_dwordx4 %0, %1, %2, %3 offen offset:%4 sc1" : "=v"(d) : "v"(voff), "s"(rs), "s"(soff), "n"(OFF) : "memory");
}
template <int OFF = 0> DI void ld128_async_acc(u32x4& d, rsrc_t rs, unsigned voff, unsigned soff) {
    asm volatile("buffer_load_dwordx4 %0, %1, %2, %3 offen offset:%4 sc1" : "=a"(d) : "v"(voff), "s"(rs), "s"(soff), "n"(OFF) : "memory");
}
template <bool ACC> DI void ld_frag(u32x4& d, rsrc_t rs, unsigned voff, unsigned base, int i) {
    const unsigned s = base + (i >> 2) * 4096;
    if (ACC) { if ((i & 3) == 0) ld128_async_acc(d, rs, voff, s); if ((i & 3) == 1) ld128_async_acc<1024>(d, rs, voff, s); if ((i & 3) == 2) ld128_async_acc<2048>(d, rs, voff, s); if ((i & 3) == 3) ld128_async_acc<3072>(d, rs, voff, s); }
    else     { if ((i & 3) == 0) ld128_async_dev(d, rs, voff, s); if ((i & 3) == 1) ld128_async_dev<1024>(d, rs, voff, s); if ((i & 3) == 2) ld128_async_dev<2048>(d, rs, voff, s); if ((i & 3) == 3) ld128_async_dev<3072>(d, rs, voff, s); }
}
template <int OFF = 0> DI void st128_async(u32x4 v, rsrc_t rs, unsigned voff, unsigned soff) {
    asm volatile("buffer_store_dwordx4 %0, %1, %2, %3 offen offset:%4 sc1\n\ts_nop 1" ::"v"(v), "v"(voff), "s"(rs), "s"(soff), "n"(OFF) : "memory");
}
template <int N> DI void landed(u32x4& a, u32x4& b, u32x4& c, u32x4& d) { asm volatile("s_waitcnt vmcnt(%4)" : "+v"(a), "+v"(b), "+v"(c), "+v"(d) : "n"(N) : "memory"); }
template <int N> DI void landed8(u32x4 (&r)[8]) {
    asm volatile("s_waitcnt vmcnt(%8)" : "+v"(r[0]), "+v"(r[1]), "+v"(r[2]), "+v"(r[3]), "+v"(r[4]), "+v"(r[5]), "+v"(r[6]), "+v"(r[7]) : "n"(N) : "memory");
}
DI void mfma_v(f32x16& acc, u32x4 a, u32x4 b) { asm("v_mfma_f32_32x32x16_bf16 %0, %1, %2, %0" : "+v"(acc) : "v"(a), "v"(b)); }
DI void mfma_v0(f32x16& acc, u32x4 a, u32x4 b) { asm("s_nop 1\n\tv_mfma_f32_32x32x16_bf16 %0, %1, %2, 0" : "=&v"(acc) : "v"(a), "v"(b)); }
DI void settle(f32x16& x) { asm volatile("s_nop 11" : "+v"(x)); }
template <int V> struct Int { static constexpr int value = V; };
template <int I, int N, class F> DI void static_for(F&& f) { if constexpr (I < N) { f(Int<I>{}); static_for<I + 1, N>(f); } }
DI void barrier() { asm volatile("s_barrier" ::: "memory"); }
DI int lane_id() { int l; asm volatile("v_mbcnt_lo_u32_b32 %0, -1, 0\n\tv_mbcnt_hi_u32_b32 %0, -1, %0" : "=v"(l)); return l; }

#define STAMP(id) do {} while (0)
#define TSTAMP(id) do {} while (0)

DI void epoch_request(u64& v, const char* p) { asm volatile("s_load_dwordx2 %0, %1, 0x0" : "=s"(v) : "s"(p) : "memory"); }
DI void publish(char* ws, unsigned word, u64 nonce) {
    asm volatile("s_mov_b64 exec, 1\n\tglobal_store_dwordx2 %0, %1, %2 sc1\n\ts_mov_b64 exec, -1" ::"v"(word), "v"(nonce), "s"(ws) : "memory");
}
DI void peek(u32x2& v, const char* ws, unsigned word) { asm volatile("global_load_dwordx2 %0, %1, %2 sc1" : "=v"(v) : "v"(word), "s"(ws) : "memory"); }
DI void peek(u32x4& v, const char* ws, unsigned word) { asm volatile("global_load_dwordx4 %0, %1, %2 sc1" : "=v"(v) : "v"(word), "s"(ws) : "memory"); }
DI bool differs(const u32x2& v, u64 n) { return __builtin_amdgcn_ballot_w64(__builtin_bit_cast(u64, v) != n) != 0; }
DI bool differs(const u32x4& v, u64 n) {
    return (__builtin_amdgcn_ballot_w64(__builtin_bit_cast(u64, u32x2{v[0], v[1]}) != n) | __builtin_amdgcn_ballot_w64(__builtin_bit_cast(u64, u32x2{v[2], v[3]}) != n)) != 0;
}
template <int N, class V> DI void seen(V& v, const char* ws, unsigned word, u64 nonce) {
    asm volatile("s_waitcnt vmcnt(%1)" : "+v"(v) : "n"(N) : "memory");
    if (__builtin_expect(differs(v, nonce), 0)) {
        unsigned spins = 0;
#pragma unroll 1
        do {
            __builtin_amdgcn_s_sleep(PSLEEP);
            peek(v, ws, word);
            asm volatile("s_waitcnt vmcnt(0)" : "+v"(v) :: "memory");
        } while (differs(v, nonce) && ++spins < SPIN_CAP);
    }
}

DI void mfma_pair(int isv, u32x4 xf, u32x4 w0, u32x4 w1, f32x16& acc0, f32x16& acc1) {
    asm("s_cmp_lg_u32 %5, 0\n\ts_cbranch_scc1 1f\n\t"
        "v_mfma_f32_32x32x16_bf16 %0, %3, %2, %0\n\tv_mfma_f32_32x32x16_bf16 %1, %4, %2, %1\n\ts_branch 2f\n"
        "1:\n\tv_mfma_f32_32x32x16_bf16 %0, %2, %3, %0\n\tv_mfma_f32_32x32x16_bf16 %1, %2, %4, %1\n2:"
        : "+v"(acc0), "+v"(acc1) : "v"(xf), "v"(w0), "v"(w1), "s"(isv) : "scc");
}
DI void pin(u32x4& a, u32x4& b, u32x4& c, u32x4& d) { asm volatile("" : "+v"(a), "+v"(b), "+v"(c), "+v"(d) :: "memory"); }

DI void set_reference(u32x4& q1, float nm, int hi) {
    const unsigned a = __float_as_uint(nm) & 0xFFFF0000u;
    const float r1 = nm - __uint_as_float(a);
    const unsigned b = __float_as_uint(r1) & 0xFFFF0000u;
    const float r2 = r1 - __uint_as_float(b);
    q1[2] = hi ? (a >> 16) | b : q1[2]; q1[3] = hi ? __float_as_uint(r2) >> 16 : q1[3];
}

struct KV { u32x4 k0, k1, v0, v1; };

DI void ref_post(int wid, int lane, const float (&m)[4]) {
#pragma unroll
    for (int t = 0; t < 4; ++t) *(LAS float*)(size_t)(L_REF + ((wid + t) & 3) * 512 + (lane & 31) * 16 + wid * 4) = both_max(m[t]);
}
DI void ref_take(int wid, int lane, u32x4 (&q)[4][2], float offset) {
#pragma unroll
    for (int t = 0; t < 4; ++t) {
        const f32x4 m = __builtin_bit_cast(f32x4, lds128(L_REF + ((wid + t) & 3) * 512 + (lane & 31) * 16));
        set_reference(q[t][1], -(max3(max3(m[0], m[1], m[2]), m[3], m[3]) + offset), lane >> 5);
    }
}

DI void combine_post(int wid, int lane, const f32x16& o1, const f32x16& o2, const f32x16& o3) {
    const f32x16* ot[3] = {&o1, &o2, &o3};
#pragma unroll
    for (int t = 0; t < 3; ++t) {
        const unsigned at = L_COMB + (((wid + t + 1) & 3) * 3 + t) * 4096 + lane * 16;
#pragma unroll
        for (int g = 0; g < 4; ++g) sts128(at + g * 1024, quad(*ot[t], g));
    }
}
DI bool combine_take(int wid, int lane, f32x16 o, u32x4& f0, u32x4& f1) {
#pragma unroll
    for (int v = 0; v < 12; ++v) {
        const u32x4 p = lds128(L_COMB + wid * 12288 + v * 1024 + lane * 16);
#pragma unroll
        for (int i = 0; i < 4; ++i) o[4 * (v & 3) + i] += __uint_as_float(p[i]);
    }
    const float l = upper_val(o[12]);
    o *= __builtin_amdgcn_rcpf(l);
    float chk = 0.f;
#pragma unroll
    for (int i = 0; i < 16; ++i) chk = fmaf(o[i], 0.f, chk);
    f0 = pack_lo(o); f1 = pack_hi(o);
    if (lane >> 5) { f1[2] = 0; f1[3] = 0; }
    return !(l < L_LIMIT) || !(chk == 0.f);
}

DI void stage_out(unsigned img, int wid, int lane, const f32x16& r) {
    const u32x4 lo = pack_lo(r), up = pack_hi(r);
    const unsigned wr = img + (lane & 31) * 272 + wid * 64 + (lane >> 5) * 8;
    sts64(wr, u32x2{lo[0], lo[1]}); sts64(wr + 16, u32x2{lo[2], lo[3]}); sts64(wr + 32, u32x2{up[0], up[1]}); sts64(wr + 48, u32x2{up[2], up[3]});
}
DI void flush_out(unsigned img, int wid, int lane, int h, int qt, int br, u16* out) {
    const unsigned width = h >= 12 ? 128 : 256, colb = (lane & 15) * 16;
    const rsrc_t rd = make_rsrc((char*)out + (size_t)qt * 32 * (2 * XROW) + br * XROW + (h >> 2) * 256, 31 * 2 * XROW + width);
#pragma unroll
    for (int i = 0; i < 2; ++i) {
        const unsigned row = wid * 8 + i * 4 + (lane >> 4);
        st128_async(lds128(img + row * 272 + colb), rd, colb < width ? row * (2 * XROW) + colb : OOB, 0);
    }
}

__global__ __launch_bounds__(256, 1) void fused(const u16*, const u16*, const u16*, const u16*, const u16*, const u16*, const u16*, u16*, char*) {
    u64x8 args;
    u64 arg_ws;
    const auto arg_base = __builtin_amdgcn_kernarg_segment_ptr();
    asm volatile("s_load_dwordx16 %0, %2, 0x0\n\ts_load_dwordx2 %1, %2, 0x40" : "=&s"(args), "=&s"(arg_ws) : "s"(arg_base) : "memory");
    SBAR();
    STAMP(0);
    const int wid = __builtin_amdgcn_readfirstlane(threadIdx.x >> 6);
    const int blk = blockIdx.x, xcd = blk & 7, bi = blk >> 3;
    const int h = (xcd & 1) * 8 + (bi & 7), rb = (xcd >> 1) * 4 + (bi >> 3), tile = rb * 4 + wid;
    const int rot = ((bi >> 3) + (bi & 7)) % 3;
    const int rotb = (2 * (bi >> 3) + (bi & 7)) % 7;
    auto chunk_b = [&](int p) { const int v = p + rotb; return v >= 7 ? v - 7 : v; };
    auto chunk_at = [&](int p) { const int v = p % 3 + rot; return p == 6 ? 6 : p - p % 3 + (v >= 3 ? v - 3 : v); };
    u64 epoch, code_pc;
    unsigned code_probe;
    u64 nonce;
    const f32x16 zero = {};
    u32x4 q[4][2], eq[4];
    KV a, b, c, d;
    char* ws;
    rsrc_t rws;
    const unsigned kv_at = WS_KVA + (h * 64 + wid * 16) * 4096;
#define LDKV(X, j) do { const unsigned s_ = kv_at + (j) * 4096; ld128_async_dev<3072>(X.v1, rws, voff, s_); ld128_async_dev<2048>(X.v0, rws, voff, s_); \
                        ld128_async_dev<1024>(X.k1, rws, voff, s_); ld128_async_dev(X.k0, rws, voff, s_); } while (0)
    {
        const int lane = lane_id(), hi = lane >> 5, r32 = lane & 31, q4 = (lane & 15) >> 2;
        const unsigned voff = lane * 16, col = 32 * ((lane >> 4) & 1) + 8 * (lane & 3);
        const int isv = h & 1, row8 = lane >> 3;
        u32x4 xf[28];
        f32x16 acc0 = {}, acc1 = {};
        u32x2 pw;
        {
            const unsigned piece = (lane & 7) ^ (((row8 >> 1) & 1) << 2);
            unsigned vw0 = (16 * wid + row8) * WROW + (1 + isv) * XROW + (h >> 1) * 112 + ((piece < 7 ? piece : 6) << 4), vw1 = vw0 + 8 * WROW - 1024;
            unsigned vx[4];
#pragma unroll
            for (int i = 0; i < 4; ++i) vx[i] = (tile * 32 + row8) * XROW + i * (8 * XROW - 1024) + (((lane & 7) ^ (4 * (i & 1) + (lane >> 4))) << 4);
            int rot0 = rot;
            asm volatile("s_waitcnt lgkmcnt(0)" : "+s"(args), "+s"(arg_ws), "+v"(vw0), "+v"(vw1), "+v"(vx[0]), "+v"(vx[1]), "+v"(vx[2]), "+v"(vx[3]), "+s"(rot0) : "s"(arg_base) : "memory");
            const rsrc_t rxa = make_rsrc((const void*)args[0], X_BYTES), rwa = make_rsrc((const void*)args[1], W_BYTES);
            auto req_x = [&](rsrc_t rx, int slot, unsigned k, int i) {
                if (i == 0) dma16(rx, L_X + slot * 16384 + wid * 4096, vx[0], k * 128); else if (i == 1) dma_next<1024>(rx, vx[1], k * 128);
                else if (i == 2) dma_next<2048>(rx, vx[2], k * 128); else dma_next<3072>(rx, vx[3], k * 128);
            };
            auto req_w = [&](int ch, int i) {
                if (i == 0) dma16(rwa, L_WKV + ch * 8192 + wid * 2048, vw0, chunk_at(ch) * 64 * WROW); else dma_next<1024>(rwa, vw1, chunk_at(ch) * 64 * WROW);
            };
            auto issue = [&](int ch) {
                req_w(ch, 0); req_w(ch, 1);
#pragma unroll
                for (int i = 0; i < 4; ++i) req_x(rxa, ch & 3, chunk_at(ch), i);
            };
            issue(0);
            SBAR();
            ws = (char*)arg_ws;
            rws = make_rsrc(ws, WS_END);
            const rsrc_t rxb = make_rsrc((const void*)args[3], X_BYTES);
            epoch_request(epoch, ws + WS_EPOCH);
            {
                unsigned code_len;
                asm volatile("s_getpc_b64 %0\n.Lfused_code_begin:\n\ts_mov_b32 %1, .Lfused_code_end-.Lfused_code_begin" : "=s"(code_pc), "=s"(code_len));
                const unsigned at = (bi * 16 + wid * 4 + (threadIdx.x & 3)) * 128;
                asm volatile("global_load_dword %0, %1, %2" : "=v"(code_probe) : "v"(at < code_len ? at : 0), "s"(code_pc) : "memory");
            }
            int gw = blk * 4 + wid;
            asm volatile("" : "+s"(gw));
            const unsigned idle = gw < 896 ? 0u : OOB;
            {
                const int uh = gw / 56, uj = gw - uh * 56, i = lane >> 2;
                const bool kvp = uj < 28;
                const int ks = kvp ? uj : 2 * (uj < 42 ? uj - 28 : uj - 42);
                const rsrc_t rr = make_rsrc((const void*)(uj < 42 ? args[4] : args[1]), W_BYTES);
                const unsigned v = (((i & 7) + 32 * (i >> 3)) * WROW + (lane & 3) * 16) | idle;
                const unsigned s0 = (64 * (ks >> 2) + 8 * (ks & 3)) * WROW + (kvp ? XROW : 0) + uh * 56;
                dma16(rr, L_RET + wid * 2048, v, s0); dma16(rr, L_RET + wid * 2048 + 1024, v, s0 + (kvp ? XROW : 8 * WROW));
            }
            issue(1);
            u64 ro_ptr = gw >= 448 ? args[6] : args[2];
            unsigned ro_at = (28 * ((gw & 31) >> 1) + 16 * (gw & 1)) * XROW + ((gw >= 448 ? gw - 448 : gw) >> 5) * 64;
            asm volatile("" : "+s"(ro_ptr), "+s"(ro_at));
            unsigned rt_at = L_RET + wid * 2048 + (8 * hi + q4) * 64 + col, rt_v = voff | idle;
            unsigned rt_word = gw < 896 ? WS_FW + (gw + (gw / 56) * 8) * 8 : WS_VOID + gw * 8;
            unsigned w0 = L_WKV + (32 * hi + q4) * 128 + (col ^ ((q4 >> 1) << 6)), w1 = L_WKV + (32 * hi + q4) * 128 + ((col + 56) ^ ((q4 >> 1) << 6));
            unsigned xo[4];
#pragma unroll
            for (int s = 0; s < 4; ++s) xo[s] = L_X + wid * 4096 + r32 * 128 + (((4 * hi + s) ^ ((r32 >> 1) & 7)) << 4);
            const unsigned pw_word = WS_FW + h * 512 + (lane < 56 ? lane : 55) * 8;
            asm volatile("" : "+s"(rt_word), "+v"(rt_at), "+v"(rt_v), "+v"(w0), "+v"(w1), "+v"(xo[0]), "+v"(xo[1]), "+v"(xo[2]), "+v"(xo[3]), "+v"(acc0), "+v"(acc1));
            asm volatile("s_waitcnt lgkmcnt(0)" : "+s"(epoch) :: "memory");
            nonce = epoch + 1;
            issue(2);
            STAMP(1);
            const unsigned wb_at = WS_WB + h * 114688;
            auto req_kvb = [&](int ch, int j) {
                const unsigned s = wb_at + chunk_b(ch) * 8192 + wid * 2048;
                if (j == 0) dma16_dev(rws, l_kvb(ch) + wid * 2048, voff, s); else dma_next_dev<1024>(rws, voff, s);
            };
            auto req_qb = [&](int ch) { dma16_dev(rws, L_QB + ch * 4096 + wid * 1024, voff, wb_at + 57344 + chunk_b(ch) * 4096 + wid * 1024); };
            auto req_wq = [&](int j) { dma16_dev(rws, l_wq(j) + wid * 1024, voff, wb_at + 86016 + chunk_at(j) * 4096 + wid * 1024); };
            auto xb_slot = [](int ch) { return (ch + 3) & 3; };
            u32x4 wf[2][8];
#pragma unroll
            for (int p = 0; p <= 7; ++p) {
                if (p < 7) {
                    if (p == 0 || p == 3) wait_vm<12>(); else if (p <= 2) wait_vm<14>(); else if (p == 4) wait_vm<13>(); else if (p == 5) wait_vm<11>(); else wait_vm<10>();
                    if (p == 3) publish(ws, rt_word, nonce);
                    barrier();
                    if (p == 0) asm volatile("" :: "v"(code_probe), "s"(code_pc));
                    if (p == 0) { STAMP(2); }
                    if (p == 6) { STAMP(3); }
                    if (p == 6) peek(pw, ws, pw_word);
                } else {
                    seen<4>(pw, ws, pw_word, nonce);
                    lds_bar();
                }
#pragma unroll
                for (int s = 0; s < 4; ++s) {
                    SBAR();
                    if (p > 0) mfma_pair(isv, xf[4 * p - 4 + s], wf[(p - 1) & 1][2 * s], wf[(p - 1) & 1][2 * s + 1], acc0, acc1);
                    SBAR();
                    if (p < 7) {
                        const unsigned o = p * 8192 + s * 1024;
                        xf[4 * p + s] = lds128(xo[s] + (p & 3) * 16384);
                        wf[p & 1][2 * s] = tr_frag(w0 + o, w0 + o + 512); wf[p & 1][2 * s + 1] = tr_frag(w1 + o, w1 + o + 512);
                    }
                    if (p == 0 && s == 0) {
                        st128_async(tr_frag(rt_at, rt_at + 256), rws, rt_v, WS_WB + gw * 2048);
                        st128_async<1024>(tr_frag(rt_at + 1024, rt_at + 1280), rws, rt_v, WS_WB + gw * 2048);
                    }
                    if (p >= 1 && p <= 6 && s == 0) pin(xf[4 * p - 4], xf[4 * p - 3], xf[4 * p - 2], xf[4 * p - 1]);
                    if (p <= 3) {
                        if (s == 0) { req_w(p + 3, 0); req_w(p + 3, 1); }
                        if (s == 1) { req_x(rxa, (p + 3) & 3, chunk_at(p + 3), 0); req_x(rxa, (p + 3) & 3, chunk_at(p + 3), 1); }
                        if (s >= 2) req_x(rxa, (p + 3) & 3, chunk_at(p + 3), s);
                    }
                    if (p >= 4 && p <= 6) req_x(rxb, xb_slot(p - 4), chunk_b(p - 4), s);
                    if (p == 5 && s == 3) {
                        const rsrc_t rp = make_rsrc((const char*)args[5] + h * 1568, 1568);
                        dma16(rp, L_WPHI, voff, 0); dma_next<1024>(rp, voff, 0);
                    }
                    if (p == 7) {
                        if (s < 3) { req_kvb(s, 0); req_kvb(s, 1); req_qb(s); }
                    }
                    SBAR();
                }
            }
            asm volatile("s_nop 3" : "+v"(acc0), "+v"(acc1));
            u32x4 f[4] = {pack_lo(acc0), pack_hi(acc0), pack_lo(acc1), pack_hi(acc1)};
            {
                const bool all = isv && r32 >= 28, tail = isv ? all : (bool)hi;
                const unsigned va = r32 == 28 ? 0x3F803F80u : 0u, v2 = isv ? va : 0x3F803F80u, v3 = isv ? va : 0x00003F80u;
#pragma unroll
                for (int i = 0; i < 4; ++i) {
                    f[i][0] = all ? va : f[i][0]; f[i][1] = all ? va : f[i][1];
                    if (i & 1) { f[i][2] = tail ? v2 : f[i][2]; f[i][3] = tail ? v3 : f[i][3]; }
                    else       { f[i][2] = all ? va : f[i][2]; f[i][3] = all ? va : f[i][3]; }
                }
                pin(f[0], f[1], f[2], f[3]);
            }
            const unsigned kv_to = WS_KVA + ((h & 14) * 64 + tile) * 4096 + isv * 2048;
            STAMP(4);
            if (wid == 0 && lane == 0) *(LAS unsigned*)(size_t)L_FLAG = 0;
            u32x2 pk_;
            const unsigned pk_word = WS_FK + ((h >> 1) * 64 + wid * 16) * 16 + r32 * 8;
            f32x16 aq = {}, ak = {}, av = {};
            unsigned qba = L_QB + voff;
            asm volatile("s_nop 1" : "+v"(aq), "+v"(ak), "+v"(av), "+v"(qba));
            {
                u32x4 wg[28][3], xg[28];
                static_for<0, 31>([&](auto nc) __attribute__((always_inline)) {
                    constexpr int n = decltype(nc)::value, i = n - 3, p = n >> 2, s = n & 3;
                    if (n < 28) {
                        if (s == 0) {
                            if (p == 0) wait_vm<6>(); else if (p == 1) wait_vm<10>(); else if (p == 2) wait_vm<14>(); else if (p <= 4) wait_vm<18>(); else if (p == 5) wait_vm<7>(); else wait_vm<8>();
                            barrier();
                            if (p == 5) { publish(ws, WS_FK + (((h >> 1) * 64 + tile) * 2 + isv) * 8, nonce); STAMP(5); }
                        }
                    }
                    auto request = [&](int slot) __attribute__((always_inline)) {
                        const int r = slot - 1;
                        if (p <= 3) {
                            if (r == 4 || r == 5) req_kvb(p + 3, r - 4);
                            if (r == 6) req_qb(p + 3);
                            if (r >= 7 && r < 11) req_x(rxb, xb_slot(p + 3), chunk_b(p + 3), r - 7);
                        }
                        if (p == 2) {
                            if (r == 0) st128_async(f[0], rws, voff, kv_to);
                            if (r == 1) st128_async<1024>(f[1], rws, voff, kv_to);
                            if (r == 2) st128_async(f[2], rws, voff, kv_to + 64 * 4096);
                            if (r == 3) st128_async<1024>(f[3], rws, voff, kv_to + 64 * 4096);
                        }
                        if (p == 5 && r >= 0 && r < 7) req_wq(r);
                        if (p == 6 && r == 0) {
                            const int i = lane >> 2;
                            dma16(make_rsrc((const void*)ro_ptr, WO_BYTES), L_X + 2 * 16384 + wid * 4096, (((gw & 1) && i >= 12 ? i - 4 : i) * XROW + (lane & 3) * 16) | idle, ro_at);
                        }
                    };
                    const unsigned at = l_kvb(p < 7 ? p : 0) + s * 2048 + voff;
                    SBAR();
                    if constexpr (i >= 0) mfma_v(ak, wg[i][0], xg[i]);
                    SBAR();
                    if constexpr (n < 28) { wg[n][0] = lds128(at); xg[n] = lds128(xo[s] + xb_slot(p) * 16384); }
                    request(3 * s);
                    SBAR();
                    if constexpr (i >= 0) mfma_v(av, xg[i], wg[i][1]);
                    SBAR();
                    if constexpr (n < 28) wg[n][1] = lds128(at + 1024);
                    request(3 * s + 1);
                    SBAR();
                    if constexpr (i >= 0) mfma_v(aq, wg[i][2], xg[i]);
                    SBAR();
                    if constexpr (n < 28) wg[n][2] = lds128(qba + n * 1024);
                    request(3 * s + 2);
                    SBAR();
                });
            }
            STAMP(6);
            u32x4 wp[2];
            {
                const unsigned fc = 16 * ((lane >> 4) & 1) + 4 * (lane & 3);
                const unsigned a0 = L_WPHI + (4 * hi + q4) * 56 + (fc < 28 ? fc : 24) * 2;
                wp[0] = tr_frag(a0, a0 + 8 * 56); wp[1] = tr_frag(a0 + 16 * 56, a0 + (hi ? 20 : 24) * 56);
            }
            wait_vm<0>();
            barrier();
            {
                const unsigned at = L_X + 2 * 16384 + wid * 4096 + (4 * hi + q4) * 64 + col;
                st128_async(tr_frag(at, at + 512), rws, rt_v, WS_WO + gw * 1024);
            }
            u32x4 wqf[28];
#pragma unroll
            for (int i = 0; i < 6; ++i) wqf[i] = lds128(l_wq(i >> 2) + (i & 3) * 1024 + voff);
            asm volatile("s_nop 1" : "+v"(aq), "+v"(ak), "+v"(av));
            STAMP(7);
            f32x16 pkk, pqq, qa;
            {
                u32x4 f0 = pack_lo(ak), f1 = pack_hi(ak);
                if (hi) { f1[2] = 0; f1[3] = 0; }
                asm volatile("" : "+v"(f0), "+v"(f1));
                mfma_v0(pkk, wp[0], f0); mfma_v(pkk, wp[1], f1);
            }
            u32x4 g0 = pack_lo(aq), g1 = pack_hi(aq);
            if (hi) { g1[2] = 0; g1[3] = 0; }
            asm volatile("" : "+v"(g0), "+v"(g1));
#define QSTEP(i) do { SBAR(); if (((i) & 3) == 0) pin(wqf[i], wqf[(i) + 1], wqf[(i) + 2], wqf[(i) + 3]); \
                      if ((i) == 0) mfma_v0(qa, wqf[0], xf[0]); else mfma_v(qa, wqf[i], xf[i]); \
                      SBAR(); if ((i) + 6 < 28) wqf[(i) + 6] = lds128(l_wq(((i) + 6) >> 2) + (((i) + 6) & 3) * 1024 + voff); SBAR(); } while (0)
            QSTEP(0);
            u32x4 v0 = pack_lo(av);
            QSTEP(1);
            u32x4 v1 = pack_hi(av);
            if (r32 >= 28) { const unsigned v = r32 == 28 ? 0x3F803F80u : 0u; v0 = u32x4{v, v, v, v}; v1 = v0; }
            asm volatile("" : "+v"(pkk), "+v"(v0), "+v"(v1));
            QSTEP(2);
            const unsigned img = L_PHI + wid * 4096;
            {
                f32x16 ep, en;
                const float hx = max16(pkk), hn = min16(pkk);
                QSTEP(3);
                const float mx = both_max(hx) * LOG2E, mn = both_min(hn) * LOG2E;
                const float live = hi ? 0.f : 1.f;
                float sp = 0.f, sn = 0.f;
                float ap = fmaf(pkk[15], LOG2E, -mx), an = fmaf(pkk[15], -LOG2E, mn);
#pragma unroll
                for (int i = 0; i < 16; ++i) {
                    QSTEP(4 + i);
                    ep[15 - i] = __builtin_amdgcn_exp2f(ap); en[15 - i] = __builtin_amdgcn_exp2f(an);
                    SBAR();
                    if (i < 15) { ap = fmaf(pkk[14 - i], LOG2E, -mx); an = fmaf(pkk[14 - i], -LOG2E, mn); }
                    if (i == 1) { sp = ep[15]; sn = en[15]; }
                    if (i > 1) { sp += ep[16 - i]; sn += en[16 - i]; }
                    if (i == 4) { sp *= live; sn *= live; }
                    if (i == KP_PEEK) peek(pk_, ws, pk_word);
                    if (i == KP_SEEN) { seen<0>(pk_, ws, pk_word, nonce); STAMP(8); LDKV(a, 0); }
                }
                QSTEP(20);
                const float rp = __builtin_amdgcn_rcpf(both_sum(sp + ep[0])), rn = __builtin_amdgcn_rcpf(both_sum(sn + en[0]));
                const float rp3 = rp * live, rn3 = rn * live;
                const unsigned wr = img + (r32 & 15) * 136 + (r32 >> 4) * 16384 + hi * 8;
#pragma unroll
                for (int g = 0; g < 4; ++g) {
                    const float u = g == 3 ? rp3 : rp, v = g == 3 ? rn3 : rn;
                    QSTEP(21 + 2 * g);
                    sts64(wr + g * 16, u32x2{pk(ep[4 * g] * u, ep[4 * g + 1] * u), pk(ep[4 * g + 2] * u, ep[4 * g + 3] * u)});
                    if (g < 3) QSTEP(22 + 2 * g);
                    sts64(wr + 64 + g * 16, u32x2{pk(en[4 * g] * v, en[4 * g + 1] * v), pk(en[4 * g + 2] * v, en[4 * g + 3] * v)});
                }
            }
#undef QSTEP
            u32x4 pf[4];
            {
                const unsigned rd = img + (4 * hi + q4) * 136 + col;
#pragma unroll
                for (int i = 0; i < 4; ++i) pf[i] = tr_frag(rd + (i >> 1) * 64 + (i & 1) * 16384, rd + (i >> 1) * 64 + (i & 1) * 16384 + 8 * 136);
            }
            SBAR(); mfma_v0(pqq, wp[0], g0); SBAR(); LDKV(b, 1); SBAR(); mfma_v(pqq, wp[1], g1); SBAR(); LDKV(c, 2);
            asm volatile("" : "+v"(qa));
            {
                qa *= QSCALE;
                u32x4 q0 = pack_lo(qa), q1 = pack_hi(qa);
                if (hi) { q1[2] = 0; q1[3] = 0; }
                sts128(L_QX + wid * 2048 + voff, q0); sts128(L_QX + wid * 2048 + 1024 + voff, q1);
            }
            f32x16 kv0, kv1, sc[4];
            SBAR(); mfma_v0(kv0, pf[0], v0); SBAR();
            asm volatile("" : "+v"(pqq));
            const float qhx = max16(pqq);
            SBAR(); mfma_v(kv0, pf[1], v1); SBAR();
            lds_bar();
#pragma unroll
            for (int t = 0; t < 4; ++t) { const unsigned at = L_QX + ((wid + t) & 3) * 2048 + voff; q[t][0] = lds128(at); q[t][1] = lds128(at + 1024); }
            const float qhn = min16(pqq);
            SBAR(); mfma_v0(kv1, pf[2], v0); SBAR();
            const float qmx = both_max(qhx) * LOG2E, qmn = both_min(qhn) * LOG2E;
            SBAR(); mfma_v(kv1, pf[3], v1); SBAR();
            landed<8>(a.k0, a.k1, a.v0, a.v1);
            f32x16 eqp, eqn;
#pragma unroll
            for (int i = 0; i < 8; ++i) {
                const float a0 = fmaf(pqq[2 * i], LOG2E, -qmx), a1 = fmaf(pqq[2 * i + 1], LOG2E, -qmx), n0 = fmaf(pqq[2 * i], -LOG2E, qmn), n1 = fmaf(pqq[2 * i + 1], -LOG2E, qmn);
                SBAR();
                if (i & 1) mfma_v(sc[i >> 1], a.k1, q[i >> 1][1]); else mfma_v0(sc[i >> 1], a.k0, q[i >> 1][0]);
                SBAR();
                eqp[2 * i] = __builtin_amdgcn_exp2f(a0); eqp[2 * i + 1] = __builtin_amdgcn_exp2f(a1); eqn[2 * i] = __builtin_amdgcn_exp2f(n0); eqn[2 * i + 1] = __builtin_amdgcn_exp2f(n1);
                if (i == 0) asm volatile("" : "+v"(kv0));
                if (i == 4) asm volatile("" : "+v"(kv1));
                sts128(L_RED + wid * 8192 + i * 1024 + voff, quad(i < 4 ? kv0 : kv1, i & 3));
                SBAR();
            }
            eq[0] = pack_lo(eqp); eq[1] = pack_hi(eqp); eq[2] = pack_lo(eqn); eq[3] = pack_hi(eqn);
            asm volatile("" : "+v"(eq[0]), "+v"(eq[1]), "+v"(eq[2]), "+v"(eq[3]), "+v"(sc[0]), "+v"(sc[1]), "+v"(sc[2]), "+v"(sc[3]));
            {
                const float m[4] = {max16(sc[0]), max16(sc[1]), max16(sc[2]), max16(sc[3])};
                ref_post(wid, lane, m);
            }
            lds_bar();
            {
                ref_take(wid, lane, q, REF_OFFSET);
#pragma unroll
                for (int i = 0; i < 2; ++i) {
                    const unsigned off = (2 * wid + i) * 1024 + voff;
                    f32x4 u = __builtin_bit_cast(f32x4, lds128(L_RED + off));
#pragma unroll
                    for (int v = 1; v < 4; ++v) u += __builtin_bit_cast(f32x4, lds128(L_RED + v * 8192 + off));
                    st128_async(__builtin_bit_cast(u32x4, u), rws, off, WS_KVP + (h * 16 + rb) * 8192);
                }
                STAMP(9);
            }
        }
    }
    const int qt = rb * 4 + (h & 3), nt = (h & 12) + wid;
    const bool live = nt < 14;
    u32x4 wob_f[32], woa_f[32], f0, f1;
    const unsigned wo_at = WS_WO + (live ? nt : 0) * 32768;
    const unsigned o_mine = (tile * 16 + h) * 2048, o_need = (qt * 16 + wid * 4) * 2048;
    {
        const int lane = lane_id(), hi = lane >> 5, r32 = lane & 31;
        const unsigned voff = lane * 16;
        STAMP(10);
        f32x16 o0 = {}, o1 = {}, o2 = {}, o3 = {};
        f32x16 sa, sb; u32x4 pa, pb = {};
        float e12 = 0.f, e13 = 0.f, e14 = 0.f, e15 = 0.f;
        d.v1 = u32x4{};
#define QK0(S, K, Q) asm volatile("v_mfma_f32_32x32x16_bf16 %0, %1, %2, 0" : "=&v"(S) : "v"(K), "v"(Q))
#define QK1(S, K, Q) asm volatile("v_mfma_f32_32x32x16_bf16 %0, %1, %2, %0" : "+v"(S) : "v"(K), "v"(Q))
        asm volatile("s_nop 1" : "+v"(q[0][1]), "+v"(q[1][1]), "+v"(q[2][1]), "+v"(q[3][1]));
        QK0(sa, a.k0, q[0][0]); QK1(sa, a.k1, q[0][1]);
        asm volatile("s_nop 11" : "+v"(sa));
#define EX(S, i) S[i] = __builtin_amdgcn_exp2f(S[i])
#define STEP(NEXT, SC, SN, KN0, KN1, QN0, QN1, OP, VP1, OC, VC0) do { \
    if (NEXT) QK0(SN, KN0, QN0); SBAR(); \
    pb[2] = pk(e12, e13); SBAR(); EX(SC, 0); SBAR(); pb[3] = pk(e14, e15); SBAR(); EX(SC, 1); EX(SC, 2); EX(SC, 3); SBAR(); \
    OP = mfma(VP1, pb, OP); SBAR(); \
    EX(SC, 4); EX(SC, 5); SBAR(); pa[0] = pk(SC[0], SC[1]); SBAR(); EX(SC, 6); SBAR(); pa[1] = pk(SC[2], SC[3]); SBAR(); EX(SC, 7); SBAR(); \
    if (NEXT) QK1(SN, KN1, QN1); SBAR(); \
    EX(SC, 8); SBAR(); pa[2] = pk(SC[4], SC[5]); SBAR(); EX(SC, 9); SBAR(); pa[3] = pk(SC[6], SC[7]); SBAR(); EX(SC, 10); EX(SC, 11); SBAR(); \
    OC = mfma(VC0, pa, OC); SBAR(); \
    e12 = __builtin_amdgcn_exp2f(SC[12]); SBAR(); pb[0] = pk(SC[8], SC[9]); SBAR(); e13 = __builtin_amdgcn_exp2f(SC[13]); SBAR(); pb[1] = pk(SC[10], SC[11]); SBAR(); \
    e14 = __builtin_amdgcn_exp2f(SC[14]); e15 = __builtin_amdgcn_exp2f(SC[15]); SBAR(); } while (0)
#define TILE(J, CUR, PREV, NXT, NW, SEAM, MORE) do { \
    STEP(true, sa, sb, CUR.k0, CUR.k1, q[1][0], q[1][1], o3, PREV.v1, o0, CUR.v0); \
    if (J + 3 <= 15) LDKV(PREV, J + 3); \
    SEAM; SBAR(); \
    STEP(true, sb, sa, CUR.k0, CUR.k1, q[2][0], q[2][1], o0, CUR.v1, o1, CUR.v0); \
    MORE(0, 3); SBAR(); \
    STEP(true, sa, sb, CUR.k0, CUR.k1, q[3][0], q[3][1], o1, CUR.v1, o2, CUR.v0); \
    MORE(3, 6); \
    if (J < 15) landed<NW>(NXT.k0, NXT.k1, NXT.v0, NXT.v1); \
    SBAR(); \
    STEP(J < 15, sb, sa, NXT.k0, NXT.k1, q[0][0], q[0][1], o2, CUR.v1, o3, CUR.v0); \
    MORE(6, 8); SBAR(); } while (0)
        u32x4 pl[2], sf[4];
        u32x4 pp;
        u32x2 ps;
        const unsigned pp_word = lane < 8 ? WS_FP + (h * 4 + (rb >> 2)) * 128 + lane * 16 : WS_FWO + (((lane >> 4) & 1) * 14 + (live ? nt : 0)) * 256 + (lane & 15) * 16;
        const unsigned ps_word = WS_FS + h * 512 + lane * 8;
        auto slice_load = [&]() {
            const unsigned at = WS_KVP + h * 16 * 8192 + tile * 128, v = (lane >> 3) * 8192 + (lane & 7) * 16;
            ld128_async_dev(pl[0], rws, v, at); ld128_async_dev(pl[1], rws, v, at + 8 * 8192);
        };
        auto slice_sum = [&]() {
            asm volatile("s_waitcnt vmcnt(4)" : "+v"(pl[0]), "+v"(pl[1]) :: "memory");
            float u[4];
#pragma unroll
            for (int e = 0; e < 4; ++e) {
                u[e] = both_sum(__uint_as_float(pl[0][e]) + __uint_as_float(pl[1][e]));
                auto r = __builtin_amdgcn_permlane16_swap(__float_as_uint(u[e]), __float_as_uint(u[e]), false, false);
                u[e] = __uint_as_float(r[0]) + __uint_as_float(r[1]);
                u[e] += __uint_as_float(__builtin_amdgcn_update_dpp(0u, __float_as_uint(u[e]), 0x128, 0xf, 0xf, false));
            }
            const u32x2 v = {pk(u[0], u[1]), pk(u[2], u[3])};
            const unsigned at = WS_ST + h * 4096 + (rb >> 2) * 1024 + ((rb & 1) * 32 + wid * 8) * 16 + ((rb >> 1) & 1) * 8;
            asm volatile("buffer_store_dwordx2 %0, %1, %2, %3 offen sc1" ::"v"(v), "v"(lane < 8 ? voff : OOB), "s"(rws), "s"(at) : "memory");
        };
        auto state_load = [&]() {
#pragma unroll
            for (int i = 0; i < 4; ++i) ld_frag<false>(sf[i], rws, voff, WS_ST + h * 4096, i);
        };
        auto wo_load = [&](int n, int first, int last) {
#pragma unroll
            for (int i = 0; i < 8; ++i) if (i >= first && i < last) ld_frag<true>(wob_f[8 * n + i], rws, voff, wo_at + 14 * 32768, 8 * n + i);
        };
        auto wa_load = [&](int n, int first, int last) {
#pragma unroll
            for (int i = 0; i < 8; ++i) if (i >= first && i < last) {
                if (n == 3) ld_frag<true>(woa_f[8 * n + i], rws, voff, wo_at, 8 * n + i); else ld_frag<false>(woa_f[8 * n + i], rws, voff, wo_at, 8 * n + i);
            }
        };
        f32x16 np, nn;
        auto start_b = [&]() {
            landed<4>(sf[0], sf[1], sf[2], sf[3]);
            if (r32 == 29) {
                const unsigned tail = hi ? 0u : 0x3F803F80u;
                sf[0] = u32x4{0x3F803F80u, 0x3F803F80u, 0x3F803F80u, 0x3F803F80u}; sf[2] = sf[0];
                sf[1] = u32x4{0x3F803F80u, 0x3F803F80u, tail, tail}; sf[3] = sf[1];
            }
            asm volatile("" : "+v"(sf[0]), "+v"(sf[1]), "+v"(sf[2]), "+v"(sf[3]));
            mfma_v0(np, sf[0], eq[0]); mfma_v(np, sf[1], eq[1]); mfma_v0(nn, sf[2], eq[2]); mfma_v(nn, sf[3], eq[3]);
        };
        auto finish_b = [&](int first, int last) {
            if (first != 0) return;
            asm volatile("" : "+v"(np), "+v"(nn));
            const float rp_ = __builtin_amdgcn_rcpf(np[13]), rn_ = __builtin_amdgcn_rcpf(nn[13]);
            const float inv = __builtin_amdgcn_rcpf(fmaf(np[12], rp_, fmaf(nn[12], rn_, 1e-6f)));
            const float ap = upper_val(rp_ * inv), an = upper_val(rn_ * inv);
            f32x16 o;
#pragma unroll
            for (int i = 0; i < 16; ++i) o[i] = fmaf(np[i], ap, nn[i] * an);
            u32x4 g0 = pack_lo(o), g1 = pack_hi(o);
            if (hi) { g1[2] = 0; g1[3] = 0; }
            st128_async(g0, rws, voff, WS_OB + o_mine); st128_async<1024>(g1, rws, voff, WS_OB + o_mine);
        };
#define NONE(x, y) (void)0
#define WB0(x, y) wo_load(0, x, y)
#define WB1(x, y) wo_load(1, x, y)
#define WB2(x, y) wo_load(2, x, y)
#define WB3(x, y) wo_load(3, x, y)
#define WA0(x, y) wa_load(0, x, y)
#define WA1(x, y) wa_load(1, x, y)
#define WA2(x, y) wa_load(2, x, y)
#define WA3(x, y) wa_load(3, x, y)
        TILE(0, a, d, b, 10, (void)0, NONE);
        TILE(1, b, a, c, 12, (wait_vm<8>(), publish(ws, WS_FP + ((h * 4 + wid) * 16 + rb) * 8, nonce), publish(ws, (blk < 224 ? WS_FWO : WS_VOID) + (blk * 4 + wid) * 8, nonce)), NONE);
        TSTAMP(19); TILE(2, c, b, d, 10, (void)0, NONE);
        TILE(3, d, c, a, 10, (void)0, NONE);
        TILE(4, a, d, b, 9, peek(pp, ws, pp_word), NONE);
        TSTAMP(20); TILE(5, b, a, c, 11, (seen<4>(pp, ws, pp_word, nonce), slice_load()), NONE);
        TSTAMP(21); TILE(6, c, b, d, 18, slice_sum(), WB0);
        TSTAMP(22); TILE(7, d, c, a, 25, (void)0, WB1);
        TILE(8, a, d, b, 32, (wait_vm<24>(), publish(ws, WS_FS + (h * 64 + tile) * 8, nonce)), WB2);
        TILE(9, b, a, c, 31, (void)0, WB3);
        TILE(10, c, b, d, 32, peek(ps, ws, ps_word), WA3);
        TSTAMP(23); TILE(11, d, c, a, 29, (seen<12>(ps, ws, ps_word, nonce), state_load()), NONE);
        TSTAMP(24); TILE(12, a, d, b, 23, start_b(), finish_b);
        TSTAMP(25); TILE(13, b, a, c, 16, (void)0, WA0);
        TILE(14, c, b, d, 17, (wait_vm<8>(), publish(ws, WS_FOB + (tile * 16 + h) * 8, nonce)), WA1);
        TSTAMP(26); TILE(15, d, c, a, 0, (void)0, WA2);
        pb[2] = pk(e12, e13); pb[3] = pk(e14, e15);
        o3 = mfma(d.v1, pb, o3);
#undef TILE
#undef STEP
#undef EX
#undef QK0
#undef QK1
#undef NONE
#undef WB0
#undef WB1
#undef WB2
#undef WB3
#undef WA0
#undef WA1
#undef WA2
#undef WA3
        STAMP(11);
        combine_post(wid, lane, o1, o2, o3);
        u32x2 pob;
        const unsigned pob_word = WS_FOB + (qt * 16 + wid * 4 + (lane & 3)) * 8;
        peek(pob, ws, pob_word);
        lds_bar();
        const bool bad = combine_take(wid, lane, o0, f0, f1);
        st128_async(f0, rws, voff, WS_OA + o_mine); st128_async<1024>(f1, rws, voff, WS_OA + o_mine);
        STAMP(12);
        if (__any(bad) && lane == 0) *(LAS unsigned*)(size_t)L_FLAG = 1;
        seen<2>(pob, ws, pob_word, nonce);
#pragma unroll
        for (int i = 0; i < 8; ++i) dma_run_dev(rws, L_OIMG + wid * 8192, voff, WS_OB + o_need, i);
        wait_bar<8>();
        if (__builtin_amdgcn_readfirstlane(*(const LAS unsigned*)(size_t)L_FLAG)) {
            float ref[4];
#pragma unroll
            for (int t = 0; t < 4; ++t) { if (hi) { q[t][1][2] = 0; q[t][1][3] = 0; } ref[t] = -__builtin_inff(); }
#pragma unroll 1
            for (int j = 0; j < 16; ++j) {
                const u32x4 k0 = ld128_dev(rws, voff, kv_at + j * 4096), k1 = ld128_dev(rws, voff + 1024, kv_at + j * 4096);
#pragma unroll
                for (int t = 0; t < 4; ++t) { f32x16 s = mfma(k0, q[t][0], zero); s = mfma(k1, q[t][1], s); ref[t] = fmaxf(ref[t], max16(s)); }
            }
            ref_post(wid, lane, ref);
            lds_bar();
            ref_take(wid, lane, q, 0.f);
            lds_bar();
            f32x16 x[4] = {};
#pragma unroll 1
            for (int j = 0; j < 16; ++j) {
                const u32x4 k0 = ld128_dev(rws, voff, kv_at + j * 4096), k1 = ld128_dev(rws, voff + 1024, kv_at + j * 4096);
                const u32x4 v0 = ld128_dev(rws, voff + 2048, kv_at + j * 4096), v1 = ld128_dev(rws, voff + 3072, kv_at + j * 4096);
#pragma unroll
                for (int t = 0; t < 4; ++t) {
                    f32x16 s = mfma(k0, q[t][0], zero); s = mfma(k1, q[t][1], s);
#pragma unroll
                    for (int i = 0; i < 16; ++i) s[i] = __builtin_amdgcn_exp2f(s[i]);
                    x[t] = mfma(v0, pack_lo(s), x[t]); x[t] = mfma(v1, pack_hi(s), x[t]);
                }
            }
            combine_post(wid, lane, x[1], x[2], x[3]);
            lds_bar();
            combine_take(wid, lane, x[0], f0, f1);
            st128_async(f0, rws, voff, WS_OA + o_mine); st128_async<1024>(f1, rws, voff, WS_OA + o_mine);
            wait_vm<0>();
        }
        publish(ws, WS_FOA + (tile * 16 + h) * 8, nonce);
        STAMP(13);
    }
#undef LDKV
    {
        const int lane = lane_id();
        const unsigned voff = lane * 16;
#pragma unroll
        for (int i = 0; i < 32; i += 8)
            asm volatile("" : "+a"(wob_f[i]), "+a"(wob_f[i + 1]), "+a"(wob_f[i + 2]), "+a"(wob_f[i + 3]), "+a"(wob_f[i + 4]), "+a"(wob_f[i + 5]), "+a"(wob_f[i + 6]), "+a"(wob_f[i + 7]));
        f32x16 r = {};
        asm volatile("s_nop 1" : "+v"(r));
        u32x4 of[32];
        u32x2 poa;
        const unsigned poa_word = WS_FOA + (qt * 16 + wid * 4 + (lane & 3)) * 8;
        wait_vm<1>();
        barrier();
#pragma unroll
        for (int i = 0; i < 6; ++i) of[i] = lds128(L_OIMG + i * 1024 + voff);
#pragma unroll
        for (int i = 0; i < 32; ++i) {
            if (i + 6 < 32) of[i + 6] = lds128(L_OIMG + (i + 6) * 1024 + voff);
            SBAR();
            asm volatile("v_mfma_f32_32x32x16_bf16 %0, %1, %2, %0" : "+v"(r) : "a"(wob_f[i]), "v"(of[i]));
            if (i == PEEK_AT) peek(poa, ws, poa_word);
            if (i == SEEN_AT) { seen<0>(poa, ws, poa_word, nonce); STAMP(15); }
            if (i >= SEEN_AT && i < SEEN_AT + 8) dma_run_dev(rws, L_OIMG2 + wid * 8192, voff, WS_OA + o_need, i - SEEN_AT);
            SBAR();
        }
        settle(r);
        stage_out(L_OUT, wid, lane, r);
        STAMP(14);
        wait_bar<0>();
        if (blk == 0 && wid == 0) publish(ws, WS_EPOCH, nonce);
#pragma unroll
        for (int i = 0; i < 24; i += 8)
            asm volatile("" : "+v"(woa_f[i]), "+v"(woa_f[i + 1]), "+v"(woa_f[i + 2]), "+v"(woa_f[i + 3]), "+v"(woa_f[i + 4]), "+v"(woa_f[i + 5]), "+v"(woa_f[i + 6]), "+v"(woa_f[i + 7]));
        asm volatile("" : "+a"(woa_f[24]), "+a"(woa_f[25]), "+a"(woa_f[26]), "+a"(woa_f[27]), "+a"(woa_f[28]), "+a"(woa_f[29]), "+a"(woa_f[30]), "+a"(woa_f[31]));
        f32x16 s;
#pragma unroll
        for (int i = 0; i < 6; ++i) of[i] = lds128(L_OIMG2 + i * 1024 + voff);
#pragma unroll
        for (int i = 0; i < 32; ++i) {
            if (i + 6 < 32) of[i + 6] = lds128(L_OIMG2 + (i + 6) * 1024 + voff);
            SBAR();
            if (i == 0) mfma_v0(s, woa_f[0], of[0]); else if (i < 24) mfma_v(s, woa_f[i], of[i]);
            else asm volatile("v_mfma_f32_32x32x16_bf16 %0, %1, %2, %0" : "+v"(s) : "a"(woa_f[i]), "v"(of[i]));
            if (i == 4) flush_out(L_OUT, wid, lane, h, qt, 1, (u16*)args[7]);
            SBAR();
        }
        settle(s);
        STAMP(16);
        stage_out(L_OUT + 16384, wid, lane, s);
        lds_bar();
        asm volatile(".Lfused_code_end:");
        flush_out(L_OUT + 16384, wid, lane, h, qt, 0, (u16*)args[7]);
    }
}

}

extern "C" void kernel_launch(void* const* d_in, const int* in_sizes, int n_in, void* d_out, int out_size, void* d_ws, size_t ws_size, hipStream_t stream) {
    (void)hipFuncSetAttribute((const void*)fused, hipFuncAttributeMaxDynamicSharedMemorySize, LDS_BYTES);
    fused<<<256, 256, LDS_BYTES, stream>>>((const u16*)d_in[0], (const u16*)d_in[1], (const u16*)d_in[2], (const u16*)d_in[3], (const u16*)d_in[4], (const u16*)d_in[5],
                                           (const u16*)d_in[6], (u16*)d_out, (char*)d_ws);
}
```

```cpp
#include <hip/hip_runtime.h>
#include <stdint.h>

namespace {

typedef unsigned short u16;
typedef unsigned long long u64;
typedef __attribute__((ext_vector_type(16))) float f32x16;
typedef __attribute__((ext_vector_type(4))) float f32x4;
typedef __attribute__((ext_vector_type(2))) float f32x2;
typedef __attribute__((ext_vector_type(8))) __bf16 bf16x8;
typedef __attribute__((ext_vector_type(2))) __bf16 bf16x2;
typedef __attribute__((ext_vector_type(4))) unsigned u32x4;
typedef __attribute__((ext_vector_type(2))) unsigned u32x2;
typedef __attribute__((ext_vector_type(4))) short s16x4;
typedef __attribute__((ext_vector_type(8))) unsigned long long u64x8;
typedef __amdgpu_buffer_rsrc_t rsrc_t;
#define LAS __attribute__((address_space(3)))
#define GAS __attribute__((address_space(1)))
#define DI __device__ __forceinline__
#define SBAR() __builtin_amdgcn_sched_barrier(0)

constexpr int SEQ = 2048, DM = 448;
constexpr unsigned XROW = DM * 2;
constexpr unsigned WROW = 3 * DM * 2;
constexpr unsigned X_BYTES = SEQ * XROW, W_BYTES = DM * WROW, WO_BYTES = DM * XROW;
constexpr unsigned WS_EPOCH = 0;
constexpr unsigned WS_FK = 4096;
constexpr unsigned WS_FP = WS_FK + 8192;
constexpr unsigned WS_FOA = WS_FP + 8192;
constexpr unsigned WS_FOB = WS_FOA + 8192;
constexpr unsigned WS_FW = WS_FOB + 8192;
constexpr unsigned WS_FWO = WS_FW + 8192;
constexpr unsigned WS_FS = WS_FWO + 8192;
constexpr unsigned WS_VOID = WS_FS + 8192;
constexpr unsigned WS_KVA = 131072;
constexpr unsigned WS_OA = WS_KVA + (4u << 20);
constexpr unsigned WS_OB = WS_OA + (2u << 20);
constexpr unsigned WS_KVP = WS_OB + (2u << 20);
constexpr unsigned WS_WB = WS_KVP + (2u << 20);
constexpr unsigned WS_WO = WS_WB + (2u << 20);
constexpr unsigned WS_ST = WS_WO + (1u << 20);
constexpr unsigned WS_END = WS_ST + 65536;
constexpr int LDS_BYTES = 160 * 1024;
constexpr unsigned L_WKV = 0, L_RET = 57344, L_QB = 57344;
constexpr unsigned L_X = 86016;
constexpr unsigned l_kvb(int ch) { return ch == 0 ? 151552 : (ch - 1) * 8192; }
constexpr unsigned L_QA = 86016;
constexpr int X_SPARE = -1;
constexpr unsigned L_QX = 151552, L_REF = 40960, L_WPHI = 159744, L_FLAG = 161792;
constexpr unsigned L_PHI = 118784, L_RED = 0;
constexpr unsigned L_COMB = 0, L_OIMG = 65536, L_OIMG2 = 0, L_OUT = 98304;
constexpr float LOG2E = 1.4426950408889634f;
constexpr float QSCALE = 0.18898223650461362f * LOG2E;
#define REF_OFF 100.f
constexpr float REF_OFFSET = REF_OFF;
constexpr float L_LIMIT = 18446744073709551616.f;
constexpr int SC1 = 16;
constexpr unsigned OOB = 0x80000000u;
#define SPINLOG 16
constexpr unsigned SPIN_CAP = 1u << SPINLOG;
#define PEEK_AT 4
#define SEEN_AT 18
#define KP_PEEK 14
#define REF_AT 5
#define KP_SEEN 3
#define PSLEEP 4

DI f32x16 mfma(u32x4 a, u32x4 b, f32x16 c) {
    return __builtin_amdgcn_mfma_f32_32x32x16_bf16(__builtin_bit_cast(bf16x8, a), __builtin_bit_cast(bf16x8, b), c, 0, 0, 0);
}
DI unsigned pk(float lo, float hi) { f32x2 v = {lo, hi}; return __builtin_bit_cast(unsigned, __builtin_convertvector(v, bf16x2)); }
DI u32x4 pack_lo(const f32x16& x) { return u32x4{pk(x[0], x[1]), pk(x[2], x[3]), pk(x[4], x[5]), pk(x[6], x[7])}; }
DI u32x4 pack_hi(const f32x16& x) { return u32x4{pk(x[8], x[9]), pk(x[10], x[11]), pk(x[12], x[13]), pk(x[14], x[15])}; }
DI u32x4 quad(const f32x16& x, int g) { return u32x4{__float_as_uint(x[4 * g]), __float_as_uint(x[4 * g + 1]), __float_as_uint(x[4 * g + 2]), __float_as_uint(x[4 * g + 3])}; }
DI rsrc_t make_rsrc(const void* p, unsigned bytes) { return __builtin_amdgcn_make_buffer_rsrc((void*)p, 0, bytes, 0x00020000); }
DI u32x4 ld128(rsrc_t rs, unsigned voff, unsigned soff) { return __builtin_amdgcn_raw_buffer_load_b128(rs, voff, soff, 0); }
DI u32x4 ld128_dev(rsrc_t rs, unsigned voff, unsigned soff) { return __builtin_amdgcn_raw_buffer_load_b128(rs, voff, soff, SC1); }
DI void st128_dev(u32x4 v, rsrc_t rs, unsigned voff, unsigned soff) { __builtin_amdgcn_raw_buffer_store_b128(v, rs, voff, soff, SC1); }
DI void dma16(rsrc_t rs, unsigned lds_addr, unsigned voff, unsigned soff) {
    asm volatile("s_mov_b32 m0, %0\n\ts_nop 0\n\tbuffer_load_dwordx4 %1, %2, %3 offen lds" ::"s"(lds_addr), "v"(voff), "s"(rs), "s"(soff) : "memory");
}
DI void ld128_async(u32x4& d, rsrc_t rs, unsigned voff, unsigned soff) {
    asm volatile("buffer_load_dwordx4 %0, %1, %2, %3 offen" : "=v"(d) : "v"(voff), "s"(rs), "s"(soff) : "memory");
}
DI void dma16_dev(rsrc_t rs, unsigned lds_addr, unsigned voff, unsigned soff) {
    asm volatile("s_mov_b32 m0, %0\n\ts_nop 0\n\tbuffer_load_dwordx4 %1, %2, %3 offen sc1 lds" ::"s"(lds_addr), "v"(voff), "s"(rs), "s"(soff) : "memory");
}
template <int OFF> DI void dma_next(rsrc_t rs, unsigned voff, unsigned soff) {
    asm volatile("buffer_load_dwordx4 %0, %1, %2 offen offset:%3 lds" ::"v"(voff), "s"(rs), "s"(soff), "n"(OFF) : "memory");
}
template <int OFF> DI void dma_next_dev(rsrc_t rs, unsigned voff, unsigned soff) {
    asm volatile("buffer_load_dwordx4 %0, %1, %2 offen offset:%3 sc1 lds" ::"v"(voff), "s"(rs), "s"(soff), "n"(OFF) : "memory");
}
DI void dma_run_dev(rsrc_t rs, unsigned lds, unsigned voff, unsigned base, int i) {
    const unsigned s = base + (i >> 2) * 4096;
    if ((i & 3) == 0) dma16_dev(rs, lds + i * 1024, voff, s); else if ((i & 3) == 1) dma_next_dev<1024>(rs, voff, s);
    else if ((i & 3) == 2) dma_next_dev<2048>(rs, voff, s); else dma_next_dev<3072>(rs, voff, s);
}
template <int N> DI void wait_vm() { asm volatile("s_waitcnt vmcnt(%0)" ::"n"(N) : "memory"); }
template <int N> DI void wait_bar() { asm volatile("s_waitcnt vmcnt(%0) lgkmcnt(0)\n\ts_barrier" ::"n"(N) : "memory"); }
DI void lds_bar() { asm volatile("s_waitcnt lgkmcnt(0)\n\ts_barrier" ::: "memory"); }
DI u32x4 lds128(unsigned a) { return *(const LAS u32x4*)(size_t)a; }
DI void sts128(unsigned a, u32x4 v) { *(LAS u32x4*)(size_t)a = v; }
DI void sts64(unsigned a, u32x2 v) { *(LAS u32x2*)(size_t)a = v; }
DI u32x2 tr64(unsigned a) { return __builtin_bit_cast(u32x2, __builtin_amdgcn_ds_read_tr16_b64_v4i16((LAS s16x4*)(size_t)a)); }
DI u32x4 tr_frag(unsigned a0, unsigned a1) { u32x2 lo = tr64(a0), hi = tr64(a1); return u32x4{lo[0], lo[1], hi[0], hi[1]}; }
DI float max3(float a, float b, float c) { float r; asm("v_max3_f32 %0, %1, %2, %3" : "=v"(r) : "v"(a), "v"(b), "v"(c)); return r; }
DI float min3(float a, float b, float c) { float r; asm("v_min3_f32 %0, %1, %2, %3" : "=v"(r) : "v"(a), "v"(b), "v"(c)); return r; }
DI float both_max(float v) { auto r = __builtin_amdgcn_permlane32_swap(__float_as_uint(v), __float_as_uint(v), false, false); return max3(__uint_as_float(r[0]), __uint_as_float(r[1]), __uint_as_float(r[1])); }
DI float both_min(float v) { auto r = __builtin_amdgcn_permlane32_swap(__float_as_uint(v), __float_as_uint(v), false, false); return min3(__uint_as_float(r[0]), __uint_as_float(r[1]), __uint_as_float(r[1])); }
DI float both_sum(float v) { auto r = __builtin_amdgcn_permlane32_swap(__float_as_uint(v), __float_as_uint(v), false, false); return __uint_as_float(r[0]) + __uint_as_float(r[1]); }
DI float upper_val(float v) { auto r = __builtin_amdgcn_permlane32_swap(__float_as_uint(v), __float_as_uint(v), false, false); return __uint_as_float(r[1]); }
DI float max16(const f32x16& s) {
    const float a = max3(s[0], s[1], s[2]), b = max3(s[3], s[4], s[5]), c = max3(s[6], s[7], s[8]), d = max3(s[9], s[10], s[11]), e = max3(s[12], s[13], s[14]);
    return max3(max3(a, b, c), max3(d, e, s[15]), a);
}
DI float min16(const f32x16& s) {
    const float a = min3(s[0], s[1], s[2]), b = min3(s[3], s[4], s[5]), c = min3(s[6], s[7], s[8]), d = min3(s[9], s[10], s[11]), e = min3(s[12], s[13], s[14]);
    return min3(min3(a, b, c), min3(d, e, s[15]), a);
}

template <int OFF = 0> DI void ld128_async_dev(u32x4& d, rsrc_t rs, unsigned voff, unsigned soff) {
    asm volatile("buffer_load_dwordx4 %0, %1, %2, %3 offen offset:%4 sc1" : "=v"(d) : "v"(voff), "s"(rs), "s"(soff), "n"(OFF) : "memory");
}
template <int OFF = 0> DI void ld128_async_acc(u32x4& d, rsrc_t rs, unsigned voff, unsigned soff) {
    asm volatile("buffer_load_dwordx4 %0, %1, %2, %3 offen offset:%4 sc1" : "=a"(d) : "v"(voff), "s"(rs), "s"(soff), "n"(OFF) : "memory");
}
template <bool ACC> DI void ld_frag(u32x4& d, rsrc_t rs, unsigned voff, unsigned base, int i) {
    const unsigned s = base + (i >> 2) * 4096;
    if (ACC) { if ((i & 3) == 0) ld128_async_acc(d, rs, voff, s); if ((i & 3) == 1) ld128_async_acc<1024>(d, rs, voff, s); if ((i & 3) == 2) ld128_async_acc<2048>(d, rs, voff, s); if ((i & 3) == 3) ld128_async_acc<3072>(d, rs, voff, s); }
    else     { if ((i & 3) == 0) ld128_async_dev(d, rs, voff, s); if ((i & 3) == 1) ld128_async_dev<1024>(d, rs, voff, s); if ((i & 3) == 2) ld128_async_dev<2048>(d, rs, voff, s); if ((i & 3) == 3) ld128_async_dev<3072>(d, rs, voff, s); }
}
template <int OFF = 0> DI void st128_async(u32x4 v, rsrc_t rs, unsigned voff, unsigned soff) {
    asm volatile("buffer_store_dwordx4 %0, %1, %2, %3 offen offset:%4 sc1\n\ts_nop 1" ::"v"(v), "v"(voff), "s"(rs), "s"(soff), "n"(OFF) : "memory");
}
template <int N> DI void landed(u32x4& a, u32x4& b, u32x4& c, u32x4& d) { asm volatile("s_waitcnt vmcnt(%4)" : "+v"(a), "+v"(b), "+v"(c), "+v"(d) : "n"(N) : "memory"); }
template <int N> DI void landed8(u32x4 (&r)[8]) {
    asm volatile("s_waitcnt vmcnt(%8)" : "+v"(r[0]), "+v"(r[1]), "+v"(r[2]), "+v"(r[3]), "+v"(r[4]), "+v"(r[5]), "+v"(r[6]), "+v"(r[7]) : "n"(N) : "memory");
}
DI void mfma_v(f32x16& acc, u32x4 a, u32x4 b) { asm("v_mfma_f32_32x32x16_bf16 %0, %1, %2, %0" : "+v"(acc) : "v"(a), "v"(b)); }
DI void mfma_v0(f32x16& acc, u32x4 a, u32x4 b) { asm("s_nop 1\n\tv_mfma_f32_32x32x16_bf16 %0, %1, %2, 0" : "=&v"(acc) : "v"(a), "v"(b)); }
DI void settle(f32x16& x) { asm volatile("s_nop 11" : "+v"(x)); }
template <int V> struct Int { static constexpr int value = V; };
template <int I, int N, class F> DI void static_for(F&& f) { if constexpr (I < N) { f(Int<I>{}); static_for<I + 1, N>(f); } }
DI void barrier() { asm volatile("s_barrier" ::: "memory"); }
DI int lane_id() { int l; asm volatile("v_mbcnt_lo_u32_b32 %0, -1, 0\n\tv_mbcnt_hi_u32_b32 %0, -1, %0" : "=v"(l)); return l; }

#define STAMP(id) do {} while (0)
#define TSTAMP(id) do {} while (0)

DI void epoch_request(u64& v, const char* p) { asm volatile("s_load_dwordx2 %0, %1, 0x0" : "=s"(v) : "s"(p) : "memory"); }
DI void publish(char* ws, unsigned word, u64 nonce) {
    asm volatile("s_mov_b64 exec, 1\n\tglobal_store_dwordx2 %0, %1, %2 sc1\n\ts_mov_b64 exec, -1" ::"v"(word), "v"(nonce), "s"(ws) : "memory");
}
DI void peek(u32x2& v, const char* ws, unsigned word) { asm volatile("global_load_dwordx2 %0, %1, %2 sc1" : "=v"(v) : "v"(word), "s"(ws) : "memory"); }
DI void peek(u32x4& v, const char* ws, unsigned word) { asm volatile("global_load_dwordx4 %0, %1, %2 sc1" : "=v"(v) : "v"(word), "s"(ws) : "memory"); }
DI bool differs(const u32x2& v, u64 n) { return __builtin_amdgcn_ballot_w64(__builtin_bit_cast(u64, v) != n) != 0; }
DI bool differs(const u32x4& v, u64 n) {
    return (__builtin_amdgcn_ballot_w64(__builtin_bit_cast(u64, u32x2{v[0], v[1]}) != n) | __builtin_amdgcn_ballot_w64(__builtin_bit_cast(u64, u32x2{v[2], v[3]}) != n)) != 0;
}
template <int N, class V> DI void seen(V& v, const char* ws, unsigned word, u64 nonce) {
    asm volatile("s_waitcnt vmcnt(%1)" : "+v"(v) : "n"(N) : "memory");
    if (__builtin_expect(differs(v, nonce), 0)) {
        unsigned spins = 0;
#pragma unroll 1
        do {
            __builtin_amdgcn_s_sleep(PSLEEP);
            peek(v, ws, word);
            asm volatile("s_waitcnt vmcnt(0)" : "+v"(v) :: "memory");
        } while (differs(v, nonce) && ++spins < SPIN_CAP);
    }
}

DI void mfma_pair(int isv, u32x4 xf, u32x4 w0, u32x4 w1, f32x16& acc0, f32x16& acc1) {
    asm("s_cmp_lg_u32 %5, 0\n\ts_cbranch_scc1 1f\n\t"
        "v_mfma_f32_32x32x16_bf16 %0, %3, %2, %0\n\tv_mfma_f32_32x32x16_bf16 %1, %4, %2, %1\n\ts_branch 2f\n"
        "1:\n\tv_mfma_f32_32x32x16_bf16 %0, %2, %3, %0\n\tv_mfma_f32_32x32x16_bf16 %1, %2, %4, %1\n2:"
        : "+v"(acc0), "+v"(acc1) : "a"(xf), "v"(w0), "v"(w1), "s"(isv) : "scc");
}
DI void mfma_wx(f32x16& acc, u32x4 w, u32x4 x) { asm("v_mfma_f32_32x32x16_bf16 %0, %1, %2, %0" : "+v"(acc) : "v"(w), "a"(x)); }
DI void mfma_xw(f32x16& acc, u32x4 x, u32x4 w) { asm("v_mfma_f32_32x32x16_bf16 %0, %1, %2, %0" : "+v"(acc) : "a"(x), "v"(w)); }
DI void mfma_wx0(f32x16& acc, u32x4 w, u32x4 x) { asm("v_mfma_f32_32x32x16_bf16 %0, %1, %2, 0" : "=&v"(acc) : "v"(w), "a"(x)); }
DI void mfma_xw0(f32x16& acc, u32x4 x, u32x4 w) { asm("v_mfma_f32_32x32x16_bf16 %0, %1, %2, 0" : "=&v"(acc) : "a"(x), "v"(w)); }
template <int OFF> DI void lds128_acc(u32x4& d, unsigned at) { asm volatile("ds_read_b128 %0, %1 offset:%2" : "=a"(d) : "v"(at), "n"(OFF) : "memory"); }
template <int N> DI void wait_vm_lds() { asm volatile("s_waitcnt vmcnt(%0) lgkmcnt(0)" ::"n"(N) : "memory"); }
DI void pin(u32x4& a, u32x4& b, u32x4& c, u32x4& d) { asm volatile("" : "+v"(a), "+v"(b), "+v"(c), "+v"(d) :: "memory"); }

DI void set_reference(u32x4& q1, float nm, int hi) {
    const unsigned a = __float_as_uint(nm) & 0xFFFF0000u;
    const float r1 = nm - __uint_as_float(a);
    const unsigned b = __float_as_uint(r1) & 0xFFFF0000u;
    const float r2 = r1 - __uint_as_float(b);
    q1[2] = hi ? (a >> 16) | b : q1[2]; q1[3] = hi ? __float_as_uint(r2) >> 16 : q1[3];
}

struct KV { u32x4 k0, k1, v0, v1; };

DI void ref_post(int wid, int lane, const float (&m)[4]) {
#pragma unroll
    for (int t = 0; t < 4; ++t) *(LAS float*)(size_t)(L_REF + ((wid + t) & 3) * 512 + (lane & 31) * 16 + wid * 4) = both_max(m[t]);
}
DI void ref_take(int wid, int lane, u32x4 (&q)[4][2], float offset) {
#pragma unroll
    for (int t = 0; t < 4; ++t) {
        const f32x4 m = __builtin_bit_cast(f32x4, lds128(L_REF + ((wid + t) & 3) * 512 + (lane & 31) * 16));
        set_reference(q[t][1], -(max3(max3(m[0], m[1], m[2]), m[3], m[3]) + offset), lane >> 5);
    }
}

DI void combine_post(int wid, int lane, const f32x16& o1, const f32x16& o2, const f32x16& o3) {
    const f32x16* ot[3] = {&o1, &o2, &o3};
#pragma unroll
    for (int t = 0; t < 3; ++t) {
        const unsigned at = L_COMB + (((wid + t + 1) & 3) * 3 + t) * 4096 + lane * 16;
#pragma unroll
        for (int g = 0; g < 4; ++g) sts128(at + g * 1024, quad(*ot[t], g));
    }
}
DI bool combine_take(int wid, int lane, f32x16 o, u32x4& f0, u32x4& f1) {
#pragma unroll
    for (int v = 0; v < 12; ++v) {
        const u32x4 p = lds128(L_COMB + wid * 12288 + v * 1024 + lane * 16);
#pragma unroll
        for (int i = 0; i < 4; ++i) o[4 * (v & 3) + i] += __uint_as_float(p[i]);
    }
    const float l = upper_val(o[12]);
    o *= __builtin_amdgcn_rcpf(l);
    float chk = 0.f;
#pragma unroll
    for (int i = 0; i < 16; ++i) chk = fmaf(o[i], 0.f, chk);
    f0 = pack_lo(o); f1 = pack_hi(o);
    if (lane >> 5) { f1[2] = 0; f1[3] = 0; }
    return !(l < L_LIMIT) || !(chk == 0.f);
}

DI void stage_out(unsigned img, int wid, int lane, const f32x16& r) {
    const u32x4 lo = pack_lo(r), up = pack_hi(r);
    const unsigned wr = img + (lane & 31) * 272 + wid * 64 + (lane >> 5) * 8;
    sts64(wr, u32x2{lo[0], lo[1]}); sts64(wr + 16, u32x2{lo[2], lo[3]}); sts64(wr + 32, u32x2{up[0], up[1]}); sts64(wr + 48, u32x2{up[2], up[3]});
}
DI void flush_out(unsigned img, int wid, int lane, int h, int qt, int br, u16* out) {
    const unsigned width = h >= 12 ? 128 : 256, colb = (lane & 15) * 16;
    const rsrc_t rd = make_rsrc((char*)out + (size_t)qt * 32 * (2 * XROW) + br * XROW + (h >> 2) * 256, 31 * 2 * XROW + width);
#pragma unroll
    for (int i = 0; i < 2; ++i) {
        const unsigned row = wid * 8 + i * 4 + (lane >> 4);
        st128_async(lds128(img + row * 272 + colb), rd, colb < width ? row * (2 * XROW) + colb : OOB, 0);
    }
}

__global__ __launch_bounds__(256, 1) void fused(const u16*, const u16*, const u16*, const u16*, const u16*, const u16*, const u16*, u16*, char*) {
    u64x8 args;
    u64 arg_ws;
    const auto arg_base = __builtin_amdgcn_kernarg_segment_ptr();
    asm volatile("s_load_dwordx16 %0, %2, 0x0\n\ts_load_dwordx2 %1, %2, 0x40" : "=&s"(args), "=&s"(arg_ws) : "s"(arg_base) : "memory");
    SBAR();
    STAMP(0);
    const int wid = __builtin_amdgcn_readfirstlane(threadIdx.x >> 6);
    const int blk = blockIdx.x, xcd = blk & 7, bi = blk >> 3;
    const int h = (xcd & 1) * 8 + (bi & 7), rb = (xcd >> 1) * 4 + (bi >> 3), tile = rb * 4 + wid;
    const int rot = ((bi >> 3) + (bi & 7)) % 3;
    const int rotb = (2 * (bi >> 3) + (bi & 7)) % 7;
    auto chunk_b = [&](int p) { const int v = p + rotb; return v >= 7 ? v - 7 : v; };
    auto chunk_at = [&](int p) { const int v = p % 3 + rot; return p == 6 ? 6 : p - p % 3 + (v >= 3 ? v - 3 : v); };
    u64 epoch, code_pc;
    unsigned code_probe;
    u64 nonce;
    const f32x16 zero = {};
    u32x4 q[4][2], eq[4];
    KV a, b, c, d;
    char* ws;
    rsrc_t rws;
    const unsigned kv_at = WS_KVA + (h * 64 + wid * 16) * 4096;
#define LDKV(X, j) do { const unsigned s_ = kv_at + (j) * 4096; ld128_async_dev<3072>(X.v1, rws, voff, s_); ld128_async_dev<2048>(X.v0, rws, voff, s_); \
                        ld128_async_dev<1024>(X.k1, rws, voff, s_); ld128_async_dev(X.k0, rws, voff, s_); } while (0)
    {
        const int lane = lane_id(), hi = lane >> 5, r32 = lane & 31, q4 = (lane & 15) >> 2;
        const unsigned voff = lane * 16, col = 32 * ((lane >> 4) & 1) + 8 * (lane & 3);
        const int isv = h & 1, row8 = lane >> 3;
        u32x4 xf[56];
        f32x16 acc0 = {}, acc1 = {};
        u32x2 pw;
        {
            const unsigned piece = (lane & 7) ^ (((row8 >> 1) & 1) << 2);
            unsigned vw0 = (16 * wid + row8) * WROW + (1 + isv) * XROW + (h >> 1) * 112 + ((piece < 7 ? piece : 6) << 4), vw1 = vw0 + 8 * WROW - 1024;
            unsigned vx[4];
#pragma unroll
            for (int i = 0; i < 4; ++i) vx[i] = (tile * 32 + row8) * XROW + i * (8 * XROW - 1024) + (((lane & 7) ^ (4 * (i & 1) + (lane >> 4))) << 4);
            int rot0 = rot;
            asm volatile("s_waitcnt lgkmcnt(0)" : "+s"(args), "+s"(arg_ws), "+v"(vw0), "+v"(vw1), "+v"(vx[0]), "+v"(vx[1]), "+v"(vx[2]), "+v"(vx[3]), "+s"(rot0) : "s"(arg_base) : "memory");
            const rsrc_t rxa = make_rsrc((const void*)args[0], X_BYTES), rwa = make_rsrc((const void*)args[1], W_BYTES);
            auto req_x = [&](rsrc_t rx, int slot, unsigned k, int i) {
                if (i == 0) dma16(rx, L_X + slot * 16384 + wid * 4096, vx[0], k * 128); else if (i == 1) dma_next<1024>(rx, vx[1], k * 128);
                else if (i == 2) dma_next<2048>(rx, vx[2], k * 128); else dma_next<3072>(rx, vx[3], k * 128);
            };
            auto req_w = [&](int ch, int i) {
                if (i == 0) dma16(rwa, L_WKV + ch * 8192 + wid * 2048, vw0, chunk_at(ch) * 64 * WROW); else dma_next<1024>(rwa, vw1, chunk_at(ch) * 64 * WROW);
            };
            auto issue = [&](int ch) {
                req_w(ch, 0); req_w(ch, 1);
#pragma unroll
                for (int i = 0; i < 4; ++i) req_x(rxa, ch & 3, chunk_at(ch), i);
            };
            issue(0);
            SBAR();
            ws = (char*)arg_ws;
            rws = make_rsrc(ws, WS_END);
            const rsrc_t rxb = make_rsrc((const void*)args[3], X_BYTES);
            epoch_request(epoch, ws + WS_EPOCH);
            {
                unsigned code_len;
                asm volatile("s_getpc_b64 %0\n.Lfused_code_begin:\n\ts_mov_b32 %1, .Lfused_code_end-.Lfused_code_begin" : "=s"(code_pc), "=s"(code_len));
                const unsigned at = (bi * 16 + wid * 4 + (threadIdx.x & 3)) * 128;
                asm volatile("global_load_dword %0, %1, %2" : "=v"(code_probe) : "v"(at < code_len ? at : 0), "s"(code_pc) : "memory");
            }
            int gw = blk * 4 + wid, uu = xcd * 28 + bi;
            asm volatile("" : "+s"(gw), "+s"(uu));
            const unsigned idle = gw < 896 ? 0u : OOB;
            const int uj = uu >> 2, uh = (uu & 3) * 4 + wid;
            {
                const int i = lane >> 2;
                const bool kvp = uj < 28;
                const int ks = kvp ? uj : 2 * (uj < 42 ? uj - 28 : uj - 42);
                const rsrc_t rr = make_rsrc((const void*)(uj < 42 ? args[4] : args[1]), W_BYTES);
                const unsigned v = (((i & 7) + 32 * (i >> 3)) * WROW + (lane & 3) * 16) | idle;
                const unsigned s0 = (64 * (ks >> 2) + 8 * (ks & 3)) * WROW + (kvp ? XROW : 0) + uh * 56;
                dma16(rr, L_RET + wid * 2048, v, s0); dma16(rr, L_RET + wid * 2048 + 1024, v, s0 + (kvp ? XROW : 8 * WROW));
            }
            issue(1);
            u64 ro_ptr = gw >= 448 ? args[6] : args[2];
            unsigned ro_at = (28 * ((gw & 31) >> 1) + 16 * (gw & 1)) * XROW + ((gw >= 448 ? gw - 448 : gw) >> 5) * 64;
            asm volatile("" : "+s"(ro_ptr), "+s"(ro_at));
            rsrc_t rro = make_rsrc((const void*)ro_ptr, WO_BYTES);
            asm volatile("" : "+s"(rro));
            unsigned rt_at = L_RET + wid * 2048 + (8 * hi + q4) * 64 + col, rt_v = voff | idle, rt_to = WS_WB + (uh * 56 + uj) * 2048;
            unsigned rt_word = gw < 896 ? WS_FW + (uh * 64 + uj) * 8 : WS_VOID + gw * 8;
            unsigned w0 = L_WKV + (32 * hi + q4) * 128 + (col ^ ((q4 >> 1) << 6)), w1 = L_WKV + (32 * hi + q4) * 128 + ((col + 56) ^ ((q4 >> 1) << 6));
            unsigned xo[4];
#pragma unroll
            for (int s = 0; s < 4; ++s) xo[s] = L_X + wid * 4096 + r32 * 128 + (((4 * hi + s) ^ ((r32 >> 1) & 7)) << 4);
            const unsigned pw_word = WS_FW + h * 512 + (lane < 56 ? lane : 55) * 8;
            asm volatile("" : "+s"(rt_word), "+s"(rt_to), "+v"(rt_at), "+v"(rt_v), "+v"(w0), "+v"(w1), "+v"(xo[0]), "+v"(xo[1]), "+v"(xo[2]), "+v"(xo[3]), "+v"(acc0), "+v"(acc1));
            asm volatile("s_waitcnt lgkmcnt(0)" : "+s"(epoch) :: "memory");
            nonce = epoch + 1;
            issue(2);
            STAMP(1);
            const unsigned wb_at = WS_WB + h * 114688;
            auto req_kvb = [&](int ch, int j) {
                const unsigned s = wb_at + chunk_b(ch) * 8192 + wid * 2048;
                if (j == 0) dma16_dev(rws, l_kvb(ch) + wid * 2048, voff, s); else dma_next_dev<1024>(rws, voff, s);
            };
            auto req_qb = [&](int ch) { dma16_dev(rws, L_QB + ch * 4096 + wid * 1024, voff, wb_at + 57344 + chunk_b(ch) * 4096 + wid * 1024); };
            auto req_wq = [&](int j) { dma16_dev(rws, L_QA + j * 4096 + wid * 1024, voff, wb_at + 86016 + chunk_at(j) * 4096 + wid * 1024); };
            auto xb_slot = [](int ch) { return ch == 0 || ch == 5 ? 3 : ch == 1 || ch == 6 ? X_SPARE : ch - 2; };
            unsigned xs[4];
#pragma unroll
            for (int s = 0; s < 4; ++s) xs[s] = xo[s] - 16384;
            auto pump = [&](int ch, int s) {
                const int slot = xb_slot(ch);
                if (slot == X_SPARE) lds128_acc<0>(xf[28 + 4 * ch + s], xs[s]); else if (slot == 0) lds128_acc<16384>(xf[28 + 4 * ch + s], xs[s]);
                else if (slot == 1) lds128_acc<32768>(xf[28 + 4 * ch + s], xs[s]); else if (slot == 2) lds128_acc<49152>(xf[28 + 4 * ch + s], xs[s]);
                else lds128_acc<49152>(xf[28 + 4 * ch + s], xo[s]);
            };
            u32x4 wf[2][8];
#pragma unroll
            for (int p = 0; p <= 7; ++p) {
                if (p < 7) {
                    if (p == 0 || p == 3) wait_vm<12>(); else if (p <= 2) wait_vm<14>(); else if (p == 4) wait_vm<13>(); else if (p == 5) wait_vm<15>(); else wait_vm<14>();
                    if (p == 3) publish(ws, rt_word, nonce);
                    barrier();
                    if (p == 0) asm volatile("" :: "v"(code_probe), "s"(code_pc));
                    if (p == 0) { STAMP(2); }
                    if (p == 6) { STAMP(3); }
                    if (p == 6) peek(pw, ws, pw_word);
                } else {
                    seen<4>(pw, ws, pw_word, nonce);
                    lds_bar();
                }
#pragma unroll
                for (int s = 0; s < 4; ++s) {
                    SBAR();
                    if (p > 0) mfma_pair(isv, xf[4 * p - 4 + s], wf[(p - 1) & 1][2 * s], wf[(p - 1) & 1][2 * s + 1], acc0, acc1);
                    SBAR();
                    if (p < 7) {
                        const unsigned o = p * 8192 + s * 1024;
                        if ((p & 3) == 0) lds128_acc<0>(xf[4 * p + s], xo[s]); else if ((p & 3) == 1) lds128_acc<16384>(xf[4 * p + s], xo[s]);
                        else if ((p & 3) == 2) lds128_acc<32768>(xf[4 * p + s], xo[s]); else lds128_acc<49152>(xf[4 * p + s], xo[s]);
                        wf[p & 1][2 * s] = tr_frag(w0 + o, w0 + o + 512); wf[p & 1][2 * s + 1] = tr_frag(w1 + o, w1 + o + 512);
                    }
                    if (p == 0 && s == 0) {
                        u32x4 g0 = tr_frag(rt_at, rt_at + 256), g1 = tr_frag(rt_at + 1024, rt_at + 1280);
                        if (r32 >= 28) { g0 = u32x4{}; g1 = u32x4{}; }
                        st128_async(g0, rws, rt_v, rt_to); st128_async<1024>(g1, rws, rt_v, rt_to);
                    }
                    if (p >= 1 && p <= 6 && s == 0) pin(wf[(p - 1) & 1][4], wf[(p - 1) & 1][5], wf[(p - 1) & 1][6], wf[(p - 1) & 1][7]);
                    if (p <= 3) {
                        if (s == 0) { req_w(p + 3, 0); req_w(p + 3, 1); }
                        if (s == 1) { req_x(rxa, (p + 3) & 3, chunk_at(p + 3), 0); req_x(rxa, (p + 3) & 3, chunk_at(p + 3), 1); }
                        if (s >= 2) req_x(rxa, (p + 3) & 3, chunk_at(p + 3), s);
                    }
                    if (p == 4) { req_x(rxb, xb_slot(s >> 1), chunk_b(s >> 1), 2 * (s & 1)); req_x(rxb, xb_slot(s >> 1), chunk_b(s >> 1), 2 * (s & 1) + 1); }
                    if (p == 5 || p == 6) req_x(rxb, xb_slot(p - 3), chunk_b(p - 3), s);
                    if (p == 5 && s == 3) {
                        const rsrc_t rp = make_rsrc((const char*)args[5] + h * 1568, 1568);
                        dma16(rp, L_WPHI, voff, 0); dma_next<1024>(rp, voff, 0);
                    }
                    if (p == 7) {
                        pump(0, s); pump(1, s);
                        if (s < 2) { req_kvb(s, 0); req_kvb(s, 1); }
                        if (s == 2) { req_kvb(2, 0); req_kvb(2, 1); req_x(rxb, xb_slot(4), chunk_b(4), 0); }
                        if (s == 3) { req_x(rxb, xb_slot(4), chunk_b(4), 1); req_x(rxb, xb_slot(4), chunk_b(4), 2); req_x(rxb, xb_slot(4), chunk_b(4), 3); }
                    }
                    SBAR();
                }
            }
            asm volatile("s_nop 3" : "+v"(acc0), "+v"(acc1));
            asm volatile("s_waitcnt lgkmcnt(0)" ::: "memory");
            u32x4 f[4];
#pragma unroll
            for (int i = 0; i < 4; ++i) {
                req_x(rxb, xb_slot(5 + (i >> 1)), chunk_b(5 + (i >> 1)), 2 * (i & 1)); req_x(rxb, xb_slot(5 + (i >> 1)), chunk_b(5 + (i >> 1)), 2 * (i & 1) + 1);
                f[i] = i == 0 ? pack_lo(acc0) : i == 1 ? pack_hi(acc0) : i == 2 ? pack_lo(acc1) : pack_hi(acc1);
                asm volatile("" : "+v"(f[i]));
                SBAR();
            }
            {
                const bool all = isv && r32 >= 28, tail = isv ? all : (bool)hi;
                const unsigned va = r32 == 28 ? 0x3F803F80u : 0u, v2 = isv ? va : 0x3F803F80u, v3 = isv ? va : 0x00003F80u;
#pragma unroll
                for (int i = 0; i < 4; ++i) {
                    f[i][0] = all ? va : f[i][0]; f[i][1] = all ? va : f[i][1];
                    if (i & 1) { f[i][2] = tail ? v2 : f[i][2]; f[i][3] = tail ? v3 : f[i][3]; }
                    else       { f[i][2] = all ? va : f[i][2]; f[i][3] = all ? va : f[i][3]; }
                }
                pin(f[0], f[1], f[2], f[3]);
            }
            const unsigned kv_to = WS_KVA + ((h & 14) * 64 + tile) * 4096 + isv * 2048;
            STAMP(4);
            if (wid == 0 && lane == 0) *(LAS unsigned*)(size_t)L_FLAG = 0;
            u32x2 pk_;
            const unsigned pk_word = WS_FK + ((h >> 1) * 64 + wid * 16) * 16 + r32 * 8;
            f32x16 ak, av;
            {
                u32x4 wg[28][2];
                static_for<0, 31>([&](auto nc) __attribute__((always_inline)) {
                    constexpr int n = decltype(nc)::value, i = n - 3, p = n >> 2, s = n & 3;
                    if constexpr (n < 28 && s == 0) {
                        if constexpr (p == 0 || p == 2) wait_vm<16>(); else if constexpr (p == 1) wait_vm<18>(); else if constexpr (p == 3) wait_vm<10>();
                        else if constexpr (p == 4) wait_vm<11>(); else if constexpr (p == 5) wait_vm<12>(); else wait_vm<14>();
                        barrier();
                        if constexpr (p == 5) { STAMP(5); }
                    }
                    auto request = [&](int r) __attribute__((always_inline)) {
                        if (p <= 1 && !(r & 1)) req_kvb(3 + 2 * p + (r >> 2), (r >> 1) & 1);
                        if (p == 2) {
                            if (r == 0) st128_async(f[0], rws, voff, kv_to);
                            if (r == 2) st128_async<1024>(f[1], rws, voff, kv_to);
                            if (r == 4) st128_async(f[2], rws, voff, kv_to + 64 * 4096);
                            if (r == 6) st128_async<1024>(f[3], rws, voff, kv_to + 64 * 4096);
                        }
                        if (p == 3 && r < 6 && !(r & 1)) req_qb(r >> 1);
                        if (p == 4 && r < 6 && !(r & 1)) req_wq(r >> 1);
                        if (p == 5 && !(r & 1)) { if (r & 2) req_wq(3 + (r >> 2)); else req_qb(3 + (r >> 2)); }
                    };
                    const unsigned at = l_kvb(p < 7 ? p : 0) + s * 2048 + voff;
                    SBAR();
                    if constexpr (i == 0) mfma_wx0(ak, wg[i][0], xf[28 + i]); else if constexpr (i > 0) mfma_wx(ak, wg[i][0], xf[28 + i]);
                    SBAR();
                    if constexpr (p <= 3) pump(p + 2, s);
                    if constexpr (p == 3) pump(6, s);
                    if constexpr (n < 28) wg[n][0] = lds128(at);
                    request(2 * s);
                    SBAR();
                    if constexpr (i == 0) mfma_xw0(av, xf[28 + i], wg[i][1]); else if constexpr (i > 0) mfma_xw(av, xf[28 + i], wg[i][1]);
                    SBAR();
                    if constexpr (n < 28) wg[n][1] = lds128(at + 1024);
                    request(2 * s + 1);
                    SBAR();
                });
            }
            STAMP(6);
            u32x4 wp[2];
            {
                const unsigned fc = 16 * ((lane >> 4) & 1) + 4 * (lane & 3);
                const unsigned a0 = L_WPHI + (4 * hi + q4) * 56 + (fc < 28 ? fc : 24) * 2;
                wp[0] = tr_frag(a0, a0 + 8 * 56); wp[1] = tr_frag(a0 + 16 * 56, a0 + (hi ? 20 : 24) * 56);
            }
            wait_vm<4>();
            publish(ws, WS_FK + (((h >> 1) * 64 + tile) * 2 + isv) * 8, nonce);
            barrier();
            u32x4 wq[28][2];
            unsigned qba = L_QB + voff;
            asm volatile("" : "+v"(qba));
#pragma unroll
            for (int i = 0; i < 4; ++i) { wq[i][0] = lds128(qba + (L_QA - L_QB) + i * 1024); wq[i][1] = lds128(qba + i * 1024); }
            asm volatile("s_nop 1" : "+v"(ak), "+v"(av));
            STAMP(7);
            f32x16 pkk, pqq, qa, qb;
            {
                u32x4 f0 = pack_lo(ak), f1 = pack_hi(ak);
                asm volatile("" : "+v"(f0), "+v"(f1));
                mfma_v0(pkk, wp[0], f0); mfma_v(pkk, wp[1], f1);
            }
#define QA(i) do { SBAR(); if (((i) & 1) == 0) pin(wq[i][0], wq[i][1], wq[(i) + 1][0], wq[(i) + 1][1]); \
                   if ((i) == 8) { wait_vm<6>(); barrier(); } \
                   if ((i) == 16) { wait_vm<0>(); barrier(); } \
                   if ((i) == 0) mfma_wx0(qa, wq[0][0], xf[0]); else mfma_wx(qa, wq[i][0], xf[i]); \
                   SBAR(); if ((i) + 4 < 28) wq[(i) + 4][0] = lds128(qba + (L_QA - L_QB) + ((i) + 4) * 1024); \
                   if ((i) == 0 || (i) == 2) req_qb(5 + (i) / 2); \
                   if ((i) == 1 || (i) == 3) req_wq(5 + (i) / 2); \
                   if ((i) == 4) { const int r_ = lane >> 2;     \
                       dma16(rro, L_PHI + wid * 4096 + 3072, (((gw & 1) && r_ >= 12 ? r_ - 4 : r_) * XROW + (lane & 3) * 16) | idle, ro_at); } \
                   SBAR(); } while (0)
#define QB(i) do { SBAR(); if ((i) == 0) mfma_wx0(qb, wq[0][1], xf[28]); else mfma_wx(qb, wq[i][1], xf[28 + (i)]); \
                   SBAR(); if ((i) + 4 < 28) wq[(i) + 4][1] = lds128(qba + ((i) + 4) * 1024); SBAR(); } while (0)
            QA(0);
            u32x4 v0 = pack_lo(av);
            QB(0);
            u32x4 v1 = pack_hi(av);
            QA(1);
            if (r32 == 28) { v0 = u32x4{0x3F803F80u, 0x3F803F80u, 0x3F803F80u, 0x3F803F80u}; v1 = v0; }
            asm volatile("" : "+v"(pkk), "+v"(v0), "+v"(v1));
            QB(1);
            const unsigned img = L_PHI + wid * 4096;
            {
                f32x16 ep, en;
                const float hx = max16(pkk);
                QA(2);
                const float hn = min16(pkk);
                QB(2);
                const float mx = both_max(hx) * LOG2E;
                QA(3);
                const float mn = both_min(hn) * LOG2E;
                QB(3);
                const float live = hi ? 0.f : 1.f;
                float sp = 0.f, sn = 0.f;
                float ap = fmaf(pkk[15], LOG2E, -mx), an = fmaf(pkk[15], -LOG2E, mn);
#pragma unroll
                for (int i = 0; i < 16; ++i) {
                    QA(4 + i);
                    ep[15 - i] = __builtin_amdgcn_exp2f(ap); en[15 - i] = __builtin_amdgcn_exp2f(an);
                    SBAR();
                    if (i < 15) ap = fmaf(pkk[14 - i], LOG2E, -mx);
                    QB(4 + i);
                    if (i < 15) an = fmaf(pkk[14 - i], -LOG2E, mn);
                    if (i == 1) { sp = ep[15]; sn = en[15]; }
                    if (i > 1) { sp += ep[16 - i]; sn += en[16 - i]; }
                    if (i == 4) { sp *= live; sn *= live; }
                    if (i == KP_PEEK) peek(pk_, ws, pk_word);
                    if (i == 13) {
                        const unsigned at = img + 3072 + (4 * hi + q4) * 64 + col;
                        st128_async(tr_frag(at, at + 512), rws, rt_v, WS_WO + gw * 1024);
                    }
                    }
                QA(20);
                const float tp = both_sum(sp + ep[0]);
                QB(20);
                const float rp = __builtin_amdgcn_rcpf(tp), rn = __builtin_amdgcn_rcpf(both_sum(sn + en[0]));
                const float rp3 = rp * live, rn3 = rn * live;
                const unsigned wr = img + (r32 & 15) * 136 + (r32 >> 4) * 16384 + hi * 8;
#pragma unroll
                for (int g = 0; g < 4; ++g) {
                    const float u = g == 3 ? rp3 : rp, v = g == 3 ? rn3 : rn;
                    QA(21 + 2 * g);
                    if (g == KP_SEEN) { seen<0>(pk_, ws, pk_word, nonce); STAMP(8); LDKV(a, 0); }
                    const unsigned e0 = pk(ep[4 * g] * u, ep[4 * g + 1] * u);
                    QB(21 + 2 * g);
                    sts64(wr + g * 16, u32x2{e0, pk(ep[4 * g + 2] * u, ep[4 * g + 3] * u)});
                    if (g < 3) QA(22 + 2 * g);
                    const unsigned e1 = pk(en[4 * g] * v, en[4 * g + 1] * v);
                    if (g < 3) QB(22 + 2 * g);
                    sts64(wr + 64 + g * 16, u32x2{e1, pk(en[4 * g + 2] * v, en[4 * g + 3] * v)});
                }
            }
#undef QA
#undef QB
            u32x4 pf[4];
            {
                const unsigned rd = img + (4 * hi + q4) * 136 + col;
#pragma unroll
                for (int i = 0; i < 4; ++i) pf[i] = tr_frag(rd + (i >> 1) * 64 + (i & 1) * 16384, rd + (i >> 1) * 64 + (i & 1) * 16384 + 8 * 136);
            }
            if (KP_SEEN > 3) { seen<0>(pk_, ws, pk_word, nonce); STAMP(8); LDKV(a, 0); }
            asm volatile("s_nop 1" : "+v"(qa), "+v"(qb));
            f32x16 kv0, kv1, sd;
            {
                u32x4 g0 = pack_lo(qb), g1 = pack_hi(qb);
                asm volatile("" : "+v"(g0), "+v"(g1));
                mfma_v0(pqq, wp[0], g0); SBAR(); LDKV(b, 1); SBAR(); mfma_v(pqq, wp[1], g1); SBAR(); LDKV(c, 2);
            }
            SBAR(); mfma_v0(kv0, pf[0], v0); SBAR();
            qa *= QSCALE;
            SBAR(); mfma_v(kv0, pf[1], v1); SBAR();
            u32x4 q0 = pack_lo(qa), q1 = pack_hi(qa);
            asm volatile("" : "+v"(q0), "+v"(q1), "+v"(pqq));
            SBAR(); mfma_v0(kv1, pf[2], v0); SBAR();
            const float qhx = max16(pqq);
            SBAR(); mfma_v(kv1, pf[3], v1); SBAR();
            const float qhn = min16(pqq);
            const float qmx = both_max(qhx) * LOG2E, qmn = both_min(qhn) * LOG2E;
            f32x16 eqp, eqn;
            asm volatile("" : "+v"(kv0), "+v"(kv1));
#pragma unroll
            for (int i = 0; i < 8; ++i) {
                const float a0 = fmaf(pqq[2 * i], LOG2E, -qmx), a1 = fmaf(pqq[2 * i + 1], LOG2E, -qmx), n0 = fmaf(pqq[2 * i], -LOG2E, qmn), n1 = fmaf(pqq[2 * i + 1], -LOG2E, qmn);
                SBAR();
                if (i == REF_AT) { landed<8>(a.k0, a.k1, a.v0, a.v1); mfma_v0(sd, a.k0, q0); }
                if (i == REF_AT + 1) mfma_v(sd, a.k1, q1);
                sts128(L_RED + wid * 8192 + i * 1024 + voff, quad(i < 4 ? kv0 : kv1, i & 3));
                SBAR();
                eqp[2 * i] = __builtin_amdgcn_exp2f(a0); eqp[2 * i + 1] = __builtin_amdgcn_exp2f(a1); eqn[2 * i] = __builtin_amdgcn_exp2f(n0); eqn[2 * i + 1] = __builtin_amdgcn_exp2f(n1);
                SBAR();
            }
            eq[0] = pack_lo(eqp); eq[1] = pack_hi(eqp);
            asm volatile("" : "+v"(eq[0]), "+v"(eq[1]), "+v"(sd));
            set_reference(q1, -(both_max(max16(sd)) + REF_OFFSET), hi);
            sts128(L_QX + wid * 2048 + voff, q0); sts128(L_QX + wid * 2048 + 1024 + voff, q1);
            lds_bar();
#pragma unroll
            for (int t = 0; t < 4; ++t) { const unsigned at = L_QX + ((wid + t) & 3) * 2048 + voff; q[t][0] = lds128(at); q[t][1] = lds128(at + 1024); }
            u32x4 red[8];
#pragma unroll
            for (int i = 0; i < 8; ++i) red[i] = lds128(L_RED + (i >> 1) * 8192 + (2 * wid + (i & 1)) * 1024 + voff);
            SBAR();
            eq[2] = pack_lo(eqn); eq[3] = pack_hi(eqn);
            asm volatile("" : "+v"(eq[2]), "+v"(eq[3]));
            SBAR();
#pragma unroll
            for (int i = 0; i < 2; ++i) {
                const f32x4 u = (__builtin_bit_cast(f32x4, red[i]) + __builtin_bit_cast(f32x4, red[2 + i])) + (__builtin_bit_cast(f32x4, red[4 + i]) + __builtin_bit_cast(f32x4, red[6 + i]));
                st128_async(__builtin_bit_cast(u32x4, u), rws, (2 * wid + i) * 1024 + voff, WS_KVP + (h * 16 + rb) * 8192);
            }
            STAMP(9);
        }
    }
    const int qt = rb * 4 + (h & 3), nt = (h & 12) + wid;
    const bool live = nt < 14;
    u32x4 wob_f[32], woa_f[32], f0, f1;
    const unsigned wo_at = WS_WO + (live ? nt : 0) * 32768;
    const unsigned o_mine = (tile * 16 + h) * 2048, o_need = (qt * 16 + wid * 4) * 2048;
    {
        const int lane = lane_id(), hi = lane >> 5, r32 = lane & 31;
        const unsigned voff = lane * 16;
        STAMP(10);
        f32x16 o0 = {}, o1 = {}, o2 = {}, o3 = {};
        f32x16 sa, sb; u32x4 pa, pb = {};
        float e12 = 0.f, e13 = 0.f, e14 = 0.f, e15 = 0.f;
        d.v1 = u32x4{};
#define QK0(S, K, Q) asm volatile("v_mfma_f32_32x32x16_bf16 %0, %1, %2, 0" : "=&v"(S) : "v"(K), "v"(Q))
#define QK1(S, K, Q) asm volatile("v_mfma_f32_32x32x16_bf16 %0, %1, %2, %0" : "+v"(S) : "v"(K), "v"(Q))
        asm volatile("s_nop 1" : "+v"(q[0][1]), "+v"(q[1][1]), "+v"(q[2][1]), "+v"(q[3][1]));
        QK0(sa, a.k0, q[0][0]); QK1(sa, a.k1, q[0][1]);
        asm volatile("s_nop 11" : "+v"(sa));
#define EX(S, i) S[i] = __builtin_amdgcn_exp2f(S[i])
#define STEP(NEXT, SC, SN, KN0, KN1, QN0, QN1, OP, VP1, OC, VC0) do { \
    if (NEXT) QK0(SN, KN0, QN0); SBAR(); \
    pb[2] = pk(e12, e13); SBAR(); EX(SC, 0); SBAR(); pb[3] = pk(e14, e15); SBAR(); EX(SC, 1); EX(SC, 2); EX(SC, 3); SBAR(); \
    OP = mfma(VP1, pb, OP); SBAR(); \
    EX(SC, 4); EX(SC, 5); SBAR(); pa[0] = pk(SC[0], SC[1]); SBAR(); EX(SC, 6); SBAR(); pa[1] = pk(SC[2], SC[3]); SBAR(); EX(SC, 7); SBAR(); \
    if (NEXT) QK1(SN, KN1, QN1); SBAR(); \
    EX(SC, 8); SBAR(); pa[2] = pk(SC[4], SC[5]); SBAR(); EX(SC, 9); SBAR(); pa[3] = pk(SC[6], SC[7]); SBAR(); EX(SC, 10); EX(SC, 11); SBAR(); \
    OC = mfma(VC0, pa, OC); SBAR(); \
    e12 = __builtin_amdgcn_exp2f(SC[12]); SBAR(); pb[0] = pk(SC[8], SC[9]); SBAR(); e13 = __builtin_amdgcn_exp2f(SC[13]); SBAR(); pb[1] = pk(SC[10], SC[11]); SBAR(); \
    e14 = __builtin_amdgcn_exp2f(SC[14]); e15 = __builtin_amdgcn_exp2f(SC[15]); SBAR(); } while (0)
#define TILE(J, CUR, PREV, NXT, NW, SEAM, MORE) do { \
    STEP(true, sa, sb, CUR.k0, CUR.k1, q[1][0], q[1][1], o3, PREV.v1, o0, CUR.v0); \
    if (J + 3 <= 15) LDKV(PREV, J + 3); \
    SEAM; SBAR(); \
    STEP(true, sb, sa, CUR.k0, CUR.k1, q[2][0], q[2][1], o0, CUR.v1, o1, CUR.v0); \
    MORE(0, 3); SBAR(); \
    STEP(true, sa, sb, CUR.k0, CUR.k1, q[3][0], q[3][1], o1, CUR.v1, o2, CUR.v0); \
    MORE(3, 6); \
    if (J < 15) landed<NW>(NXT.k0, NXT.k1, NXT.v0, NXT.v1); \
    SBAR(); \
    STEP(J < 15, sb, sa, NXT.k0, NXT.k1, q[0][0], q[0][1], o2, CUR.v1, o3, CUR.v0); \
    MORE(6, 8); SBAR(); } while (0)
        u32x4 pl[2], sf[4];
        u32x4 pp;
        u32x2 ps;
        const unsigned pp_word = lane < 8 ? WS_FP + (h * 4 + (rb >> 2)) * 128 + lane * 16 : WS_FWO + (((lane >> 4) & 1) * 14 + (live ? nt : 0)) * 256 + (lane & 15) * 16;
        const unsigned ps_word = WS_FS + h * 512 + lane * 8;
        auto slice_load = [&]() {
            const unsigned at = WS_KVP + h * 16 * 8192 + tile * 128, v = (lane >> 3) * 8192 + (lane & 7) * 16;
            ld128_async_dev(pl[0], rws, v, at); ld128_async_dev(pl[1], rws, v, at + 8 * 8192);
        };
        auto slice_sum = [&]() {
            asm volatile("s_waitcnt vmcnt(4)" : "+v"(pl[0]), "+v"(pl[1]) :: "memory");
            float u[4];
#pragma unroll
            for (int e = 0; e < 4; ++e) {
                u[e] = both_sum(__uint_as_float(pl[0][e]) + __uint_as_float(pl[1][e]));
                auto r = __builtin_amdgcn_permlane16_swap(__float_as_uint(u[e]), __float_as_uint(u[e]), false, false);
                u[e] = __uint_as_float(r[0]) + __uint_as_float(r[1]);
                u[e] += __uint_as_float(__builtin_amdgcn_update_dpp(0u, __float_as_uint(u[e]), 0x128, 0xf, 0xf, false));
            }
            const u32x2 v = {pk(u[0], u[1]), pk(u[2], u[3])};
            const unsigned at = WS_ST + h * 4096 + (rb >> 2) * 1024 + ((rb & 1) * 32 + wid * 8) * 16 + ((rb >> 1) & 1) * 8;
            asm volatile("buffer_store_dwordx2 %0, %1, %2, %3 offen sc1" ::"v"(v), "v"(lane < 8 ? voff : OOB), "s"(rws), "s"(at) : "memory");
        };
        auto state_load = [&]() {
#pragma unroll
            for (int i = 0; i < 4; ++i) ld_frag<false>(sf[i], rws, voff, WS_ST + h * 4096, i);
        };
        auto wo_load = [&](int n, int first, int last) {
#pragma unroll
            for (int i = 0; i < 8; ++i) if (i >= first && i < last) ld_frag<true>(wob_f[8 * n + i], rws, voff, wo_at + 14 * 32768, 8 * n + i);
        };
        auto wa_load = [&](int n, int first, int last) {
#pragma unroll
            for (int i = 0; i < 8; ++i) if (i >= first && i < last) {
                if (n == 3) ld_frag<true>(woa_f[8 * n + i], rws, voff, wo_at, 8 * n + i); else ld_frag<false>(woa_f[8 * n + i], rws, voff, wo_at, 8 * n + i);
            }
        };
        f32x16 np, nn;
        auto start_b = [&]() {
            landed<4>(sf[0], sf[1], sf[2], sf[3]);
            if (r32 == 29) {
                const unsigned tail = hi ? 0u : 0x3F803F80u;
                sf[0] = u32x4{0x3F803F80u, 0x3F803F80u, 0x3F803F80u, 0x3F803F80u}; sf[2] = sf[0];
                sf[1] = u32x4{0x3F803F80u, 0x3F803F80u, tail, tail}; sf[3] = sf[1];
            }
            asm volatile("" : "+v"(sf[0]), "+v"(sf[1]), "+v"(sf[2]), "+v"(sf[3]));
            mfma_v0(np, sf[0], eq[0]); mfma_v(np, sf[1], eq[1]); mfma_v0(nn, sf[2], eq[2]); mfma_v(nn, sf[3], eq[3]);
        };
        auto finish_b = [&](int first, int last) {
            if (first != 0) return;
            asm volatile("" : "+v"(np), "+v"(nn));
            const float rp_ = __builtin_amdgcn_rcpf(np[13]), rn_ = __builtin_amdgcn_rcpf(nn[13]);
            const float inv = __builtin_amdgcn_rcpf(fmaf(np[12], rp_, fmaf(nn[12], rn_, 1e-6f)));
            const float ap = upper_val(rp_ * inv), an = upper_val(rn_ * inv);
            f32x16 o;
#pragma unroll
            for (int i = 0; i < 16; ++i) o[i] = fmaf(np[i], ap, nn[i] * an);
            u32x4 g0 = pack_lo(o), g1 = pack_hi(o);
            if (hi) { g1[2] = 0; g1[3] = 0; }
            st128_async(g0, rws, voff, WS_OB + o_mine); st128_async<1024>(g1, rws, voff, WS_OB + o_mine);
        };
#define NONE(x, y) (void)0
#define WB0(x, y) wo_load(0, x, y)
#define WB1(x, y) wo_load(1, x, y)
#define WB2(x, y) wo_load(2, x, y)
#define WB3(x, y) wo_load(3, x, y)
#define WA0(x, y) wa_load(0, x, y)
#define WA1(x, y) wa_load(1, x, y)
#define WA2(x, y) wa_load(2, x, y)
#define WA3(x, y) wa_load(3, x, y)
        TILE(0, a, d, b, 10, (void)0, NONE);
        TILE(1, b, a, c, 12, (wait_vm<8>(), publish(ws, WS_FP + ((h * 4 + wid) * 16 + rb) * 8, nonce), publish(ws, (blk < 224 ? WS_FWO : WS_VOID) + (blk * 4 + wid) * 8, nonce)), NONE);
        TSTAMP(19); TILE(2, c, b, d, 10, (void)0, NONE);
        TILE(3, d, c, a, 10, (void)0, NONE);
        TILE(4, a, d, b, 9, peek(pp, ws, pp_word), NONE);
        TSTAMP(20); TILE(5, b, a, c, 11, (seen<4>(pp, ws, pp_word, nonce), slice_load()), NONE);
        TSTAMP(21); TILE(6, c, b, d, 18, slice_sum(), WB0);
        TSTAMP(22); TILE(7, d, c, a, 25, (void)0, WB1);
        TILE(8, a, d, b, 32, (wait_vm<24>(), publish(ws, WS_FS + (h * 64 + tile) * 8, nonce)), WB2);
        TILE(9, b, a, c, 31, (void)0, WB3);
        TILE(10, c, b, d, 32, peek(ps, ws, ps_word), WA3);
        TSTAMP(23); TILE(11, d, c, a, 29, (seen<12>(ps, ws, ps_word, nonce), state_load()), NONE);
        TSTAMP(24); TILE(12, a, d, b, 23, start_b(), finish_b);
        TSTAMP(25); TILE(13, b, a, c, 16, (void)0, WA0);
        TILE(14, c, b, d, 17, (wait_vm<8>(), publish(ws, WS_FOB + (tile * 16 + h) * 8, nonce)), WA1);
        TSTAMP(26); TILE(15, d, c, a, 0, (void)0, WA2);
        pb[2] = pk(e12, e13); pb[3] = pk(e14, e15);
        o3 = mfma(d.v1, pb, o3);
#undef TILE
#undef STEP
#undef EX
#undef QK0
#undef QK1
#undef NONE
#undef WB0
#undef WB1
#undef WB2
#undef WB3
#undef WA0
#undef WA1
#undef WA2
#undef WA3
        STAMP(11);
        combine_post(wid, lane, o1, o2, o3);
        u32x2 pob;
        const unsigned pob_word = WS_FOB + (qt * 16 + wid * 4 + (lane & 3)) * 8;
        peek(pob, ws, pob_word);
        lds_bar();
        const bool bad = combine_take(wid, lane, o0, f0, f1);
        st128_async(f0, rws, voff, WS_OA + o_mine); st128_async<1024>(f1, rws, voff, WS_OA + o_mine);
        STAMP(12);
        if (__any(bad) && lane == 0) *(LAS unsigned*)(size_t)L_FLAG = 1;
        seen<2>(pob, ws, pob_word, nonce);
#pragma unroll
        for (int i = 0; i < 8; ++i) dma_run_dev(rws, L_OIMG + wid * 8192, voff, WS_OB + o_need, i);
        wait_bar<8>();
        if (__builtin_amdgcn_readfirstlane(*(const LAS unsigned*)(size_t)L_FLAG)) {
            float ref[4];
#pragma unroll
            for (int t = 0; t < 4; ++t) { if (hi) { q[t][1][2] = 0; q[t][1][3] = 0; } ref[t] = -__builtin_inff(); }
#pragma unroll 1
            for (int j = 0; j < 16; ++j) {
                const u32x4 k0 = ld128_dev(rws, voff, kv_at + j * 4096), k1 = ld128_dev(rws, voff + 1024, kv_at + j * 4096);
#pragma unroll
                for (int t = 0; t < 4; ++t) { f32x16 s = mfma(k0, q[t][0], zero); s = mfma(k1, q[t][1], s); ref[t] = fmaxf(ref[t], max16(s)); }
            }
            ref_post(wid, lane, ref);
            lds_bar();
            ref_take(wid, lane, q, 0.f);
            lds_bar();
            f32x16 x[4] = {};
#pragma unroll 1
            for (int j = 0; j < 16; ++j) {
                const u32x4 k0 = ld128_dev(rws, voff, kv_at + j * 4096), k1 = ld128_dev(rws, voff + 1024, kv_at + j * 4096);
                const u32x4 v0 = ld128_dev(rws, voff + 2048, kv_at + j * 4096), v1 = ld128_dev(rws, voff + 3072, kv_at + j * 4096);
#pragma unroll
                for (int t = 0; t < 4; ++t) {
                    f32x16 s = mfma(k0, q[t][0], zero); s = mfma(k1, q[t][1], s);
#pragma unroll
                    for (int i = 0; i < 16; ++i) s[i] = __builtin_amdgcn_exp2f(s[i]);
                    x[t] = mfma(v0, pack_lo(s), x[t]); x[t] = mfma(v1, pack_hi(s), x[t]);
                }
            }
            combine_post(wid, lane, x[1], x[2], x[3]);
            lds_bar();
            combine_take(wid, lane, x[0], f0, f1);
            st128_async(f0, rws, voff, WS_OA + o_mine); st128_async<1024>(f1, rws, voff, WS_OA + o_mine);
            wait_vm<0>();
        }
        publish(ws, WS_FOA + (tile * 16 + h) * 8, nonce);
        STAMP(13);
    }
#undef LDKV
    {
        const int lane = lane_id();
        const unsigned voff = lane * 16;
#pragma unroll
        for (int i = 0; i < 32; i += 8)
            asm volatile("" : "+a"(wob_f[i]), "+a"(wob_f[i + 1]), "+a"(wob_f[i + 2]), "+a"(wob_f[i + 3]), "+a"(wob_f[i + 4]), "+a"(wob_f[i + 5]), "+a"(wob_f[i + 6]), "+a"(wob_f[i + 7]));
        f32x16 r = {};
        asm volatile("s_nop 1" : "+v"(r));
        u32x4 of[32];
        u32x2 poa;
        const unsigned poa_word = WS_FOA + (qt * 16 + wid * 4 + (lane & 3)) * 8;
        wait_vm<1>();
        barrier();
#pragma unroll
        for (int i = 0; i < 6; ++i) of[i] = lds128(L_OIMG + i * 1024 + voff);
#pragma unroll
        for (int i = 0; i < 32; ++i) {
            if (i + 6 < 32) of[i + 6] = lds128(L_OIMG + (i + 6) * 1024 + voff);
            SBAR();
            asm volatile("v_mfma_f32_32x32x16_bf16 %0, %1, %2, %0" : "+v"(r) : "a"(wob_f[i]), "v"(of[i]));
            if (i == PEEK_AT) peek(poa, ws, poa_word);
            if (i == SEEN_AT) { seen<0>(poa, ws, poa_word, nonce); STAMP(15); }
            if (i >= SEEN_AT && i < SEEN_AT + 8) dma_run_dev(rws, L_OIMG2 + wid * 8192, voff, WS_OA + o_need, i - SEEN_AT);
            SBAR();
        }
        settle(r);
        stage_out(L_OUT, wid, lane, r);
        STAMP(14);
        wait_bar<0>();
        if (blk == 0 && wid == 0) publish(ws, WS_EPOCH, nonce);
#pragma unroll
        for (int i = 0; i < 24; i += 8)
            asm volatile("" : "+v"(woa_f[i]), "+v"(woa_f[i + 1]), "+v"(woa_f[i + 2]), "+v"(woa_f[i + 3]), "+v"(woa_f[i + 4]), "+v"(woa_f[i + 5]), "+v"(woa_f[i + 6]), "+v"(woa_f[i + 7]));
        asm volatile("" : "+a"(woa_f[24]), "+a"(woa_f[25]), "+a"(woa_f[26]), "+a"(woa_f[27]), "+a"(woa_f[28]), "+a"(woa_f[29]), "+a"(woa_f[30]), "+a"(woa_f[31]));
        f32x16 s;
#pragma unroll
        for (int i = 0; i < 6; ++i) of[i] = lds128(L_OIMG2 + i * 1024 + voff);
#pragma unroll
        for (int i = 0; i < 32; ++i) {
            if (i + 6 < 32) of[i + 6] = lds128(L_OIMG2 + (i + 6) * 1024 + voff);
            SBAR();
            if (i == 0) mfma_v0(s, woa_f[0], of[0]); else if (i < 24) mfma_v(s, woa_f[i], of[i]);
            else asm volatile("v_mfma_f32_32x32x16_bf16 %0, %1, %2, %0" : "+v"(s) : "a"(woa_f[i]), "v"(of[i]));
            if (i == 4) flush_out(L_OUT, wid, lane, h, qt, 1, (u16*)args[7]);
            SBAR();
        }
        settle(s);
        STAMP(16);
        stage_out(L_OUT + 16384, wid, lane, s);
        lds_bar();
        asm volatile(".Lfused_code_end:");
        flush_out(L_OUT + 16384, wid, lane, h, qt, 0, (u16*)args[7]);
    }
}

}

extern "C" void kernel_launch(void* const* d_in, const int* in_sizes, int n_in, void* d_out, int out_size, void* d_ws, size_t ws_size, hipStream_t stream) {
    (void)hipFuncSetAttribute((const void*)fused, hipFuncAttributeMaxDynamicSharedMemorySize, LDS_BYTES);
    fused<<<256, 256, LDS_BYTES, stream>>>((const u16*)d_in[0], (const u16*)d_in[1], (const u16*)d_in[2], (const u16*)d_in[3], (const u16*)d_in[4], (const u16*)d_in[5],
                                           (const u16*)d_in[6], (u16*)d_out, (char*)d_ws);
}
```

```cpp
#include <hip/hip_runtime.h>
#include <stdint.h>

namespace {

typedef unsigned short u16;
typedef unsigned long long u64;
typedef __attribute__((ext_vector_type(16))) float f32x16;
typedef __attribute__((ext_vector_type(4))) float f32x4;
typedef __attribute__((ext_vector_type(2))) float f32x2;
typedef __attribute__((ext_vector_type(8))) __bf16 bf16x8;
typedef __attribute__((ext_vector_type(2))) __bf16 bf16x2;
typedef __attribute__((ext_vector_type(4))) unsigned u32x4;
typedef __attribute__((ext_vector_type(2))) unsigned u32x2;
typedef __attribute__((ext_vector_type(4))) short s16x4;
typedef __attribute__((ext_vector_type(8))) unsigned long long u64x8;
typedef __amdgpu_buffer_rsrc_t rsrc_t;
#define LAS __attribute__((address_space(3)))
#define GAS __attribute__((address_space(1)))
#define DI __device__ __forceinline__
#define SBAR() __builtin_amdgcn_sched_barrier(0)

constexpr int SEQ = 2048, DM = 448;
constexpr unsigned XROW = DM * 2;
constexpr unsigned WROW = 3 * DM * 2;
constexpr unsigned X_BYTES = SEQ * XROW, W_BYTES = DM * WROW, WO_BYTES = DM * XROW;
constexpr unsigned WS_EPOCH = 0;
constexpr unsigned WS_FK = 4096;
constexpr unsigned WS_FP = WS_FK + 8192;
constexpr unsigned WS_FOA = WS_FP + 8192;
constexpr unsigned WS_FOB = WS_FOA + 8192;
constexpr unsigned WS_FW = WS_FOB + 8192;
constexpr unsigned WS_FWO = WS_FW + 8192;
constexpr unsigned WS_FS = WS_FWO + 8192;
constexpr unsigned WS_VOID = WS_FS + 8192;
constexpr unsigned WS_KVA = 131072;
constexpr unsigned WS_OA = WS_KVA + (4u << 20);
constexpr unsigned WS_OB = WS_OA + (2u << 20);
constexpr unsigned WS_KVP = WS_OB + (2u << 20);
constexpr unsigned WS_WB = WS_KVP + (2u << 20);
constexpr unsigned WS_WO = WS_WB + (2u << 20);
constexpr unsigned WS_ST = WS_WO + (1u << 20);
constexpr unsigned WS_END = WS_ST + 65536;
constexpr int LDS_BYTES = 160 * 1024;
constexpr unsigned L_WKV = 0, L_RET = 57344, L_QB = 57344;
constexpr unsigned L_X = 86016;
constexpr unsigned l_kvb(int ch) { return ch == 0 ? 151552 : (ch - 1) * 8192; }
constexpr unsigned L_QA = 86016;
constexpr int X_SPARE = -1;
constexpr unsigned L_QX = 151552, L_REF = 40960, L_WPHI = 159744, L_FLAG = 161792;
constexpr unsigned L_PHI = 118784, L_RED = 0;
constexpr unsigned L_COMB = 0, L_OIMG = 65536, L_OIMG2 = 0, L_OUT = 98304;
constexpr float LOG2E = 1.4426950408889634f;
constexpr float QSCALE = 0.18898223650461362f * LOG2E;
#define REF_OFF 100.f
constexpr float REF_OFFSET = REF_OFF;
constexpr float L_LIMIT = 18446744073709551616.f;
constexpr int SC1 = 16;
constexpr unsigned OOB = 0x80000000u;
#define SPINLOG 16
constexpr unsigned SPIN_CAP = 1u << SPINLOG;
#define PEEK_AT 4
#define SEEN_AT 18
#define KP_PEEK 0
#define REF_AT 4
#define S2_STEP 4
#define PSLEEP 4

DI f32x16 mfma(u32x4 a, u32x4 b, f32x16 c) {
    return __builtin_amdgcn_mfma_f32_32x32x16_bf16(__builtin_bit_cast(bf16x8, a), __builtin_bit_cast(bf16x8, b), c, 0, 0, 0);
}
DI unsigned pk(float lo, float hi) { f32x2 v = {lo, hi}; return __builtin_bit_cast(unsigned, __builtin_convertvector(v, bf16x2)); }
DI u32x4 pack_lo(const f32x16& x) { return u32x4{pk(x[0], x[1]), pk(x[2], x[3]), pk(x[4], x[5]), pk(x[6], x[7])}; }
DI u32x4 pack_hi(const f32x16& x) { return u32x4{pk(x[8], x[9]), pk(x[10], x[11]), pk(x[12], x[13]), pk(x[14], x[15])}; }
DI u32x4 quad(const f32x16& x, int g) { return u32x4{__float_as_uint(x[4 * g]), __float_as_uint(x[4 * g + 1]), __float_as_uint(x[4 * g + 2]), __float_as_uint(x[4 * g + 3])}; }
DI rsrc_t make_rsrc(const void* p, unsigned bytes) { return __builtin_amdgcn_make_buffer_rsrc((void*)p, 0, bytes, 0x00020000); }
DI u32x4 ld128(rsrc_t rs, unsigned voff, unsigned soff) { return __builtin_amdgcn_raw_buffer_load_b128(rs, voff, soff, 0); }
DI u32x4 ld128_dev(rsrc_t rs, unsigned voff, unsigned soff) { return __builtin_amdgcn_raw_buffer_load_b128(rs, voff, soff, SC1); }
DI void st128_dev(u32x4 v, rsrc_t rs, unsigned voff, unsigned soff) { __builtin_amdgcn_raw_buffer_store_b128(v, rs, voff, soff, SC1); }
DI void dma16(rsrc_t rs, unsigned lds_addr, unsigned voff, unsigned soff) {
    asm volatile("s_mov_b32 m0, %0\n\ts_nop 0\n\tbuffer_load_dwordx4 %1, %2, %3 offen lds" ::"s"(lds_addr), "v"(voff), "s"(rs), "s"(soff) : "memory");
}
DI void ld128_async(u32x4& d, rsrc_t rs, unsigned voff, unsigned soff) {
    asm volatile("buffer_load_dwordx4 %0, %1, %2, %3 offen" : "=v"(d) : "v"(voff), "s"(rs), "s"(soff) : "memory");
}
DI void dma16_dev(rsrc_t rs, unsigned lds_addr, unsigned voff, unsigned soff) {
    asm volatile("s_mov_b32 m0, %0\n\ts_nop 0\n\tbuffer_load_dwordx4 %1, %2, %3 offen sc1 lds" ::"s"(lds_addr), "v"(voff), "s"(rs), "s"(soff) : "memory");
}
template <int OFF> DI void dma_next(rsrc_t rs, unsigned voff, unsigned soff) {
    asm volatile("buffer_load_dwordx4 %0, %1, %2 offen offset:%3 lds" ::"v"(voff), "s"(rs), "s"(soff), "n"(OFF) : "memory");
}
template <int OFF> DI void dma_next_dev(rsrc_t rs, unsigned voff, unsigned soff) {
    asm volatile("buffer_load_dwordx4 %0, %1, %2 offen offset:%3 sc1 lds" ::"v"(voff), "s"(rs), "s"(soff), "n"(OFF) : "memory");
}
DI void dma_run_dev(rsrc_t rs, unsigned lds, unsigned voff, unsigned base, int i) {
    const unsigned s = base + (i >> 2) * 4096;
    if ((i & 3) == 0) dma16_dev(rs, lds + i * 1024, voff, s); else if ((i & 3) == 1) dma_next_dev<1024>(rs, voff, s);
    else if ((i & 3) == 2) dma_next_dev<2048>(rs, voff, s); else dma_next_dev<3072>(rs, voff, s);
}
template <int N> DI void wait_vm() { asm volatile("s_waitcnt vmcnt(%0)" ::"n"(N) : "memory"); }
template <int N> DI void wait_bar() { asm volatile("s_waitcnt vmcnt(%0) lgkmcnt(0)\n\ts_barrier" ::"n"(N) : "memory"); }
DI void lds_bar() { asm volatile("s_waitcnt lgkmcnt(0)\n\ts_barrier" ::: "memory"); }
DI u32x4 lds128(unsigned a) { return *(const LAS u32x4*)(size_t)a; }
DI void sts128(unsigned a, u32x4 v) { *(LAS u32x4*)(size_t)a = v; }
DI void sts64(unsigned a, u32x2 v) { *(LAS u32x2*)(size_t)a = v; }
DI u32x2 tr64(unsigned a) { return __builtin_bit_cast(u32x2, __builtin_amdgcn_ds_read_tr16_b64_v4i16((LAS s16x4*)(size_t)a)); }
DI u32x4 tr_frag(unsigned a0, unsigned a1) { u32x2 lo = tr64(a0), hi = tr64(a1); return u32x4{lo[0], lo[1], hi[0], hi[1]}; }
DI float max3(float a, float b, float c) { float r; asm("v_max3_f32 %0, %1, %2, %3" : "=v"(r) : "v"(a), "v"(b), "v"(c)); return r; }
DI float min3(float a, float b, float c) { float r; asm("v_min3_f32 %0, %1, %2, %3" : "=v"(r) : "v"(a), "v"(b), "v"(c)); return r; }
DI float both_max(float v) { auto r = __builtin_amdgcn_permlane32_swap(__float_as_uint(v), __float_as_uint(v), false, false); return max3(__uint_as_float(r[0]), __uint_as_float(r[1]), __uint_as_float(r[1])); }
DI float both_min(float v) { auto r = __builtin_amdgcn_permlane32_swap(__float_as_uint(v), __float_as_uint(v), false, false); return min3(__uint_as_float(r[0]), __uint_as_float(r[1]), __uint_as_float(r[1])); }
DI float both_sum(float v) { auto r = __builtin_amdgcn_permlane32_swap(__float_as_uint(v), __float_as_uint(v), false, false); return __uint_as_float(r[0]) + __uint_as_float(r[1]); }
DI float upper_val(float v) { auto r = __builtin_amdgcn_permlane32_swap(__float_as_uint(v), __float_as_uint(v), false, false); return __uint_as_float(r[1]); }
DI float max16(const f32x16& s) {
    const float a = max3(s[0], s[1], s[2]), b = max3(s[3], s[4], s[5]), c = max3(s[6], s[7], s[8]), d = max3(s[9], s[10], s[11]), e = max3(s[12], s[13], s[14]);
    return max3(max3(a, b, c), max3(d, e, s[15]), a);
}
DI float min16(const f32x16& s) {
    const float a = min3(s[0], s[1], s[2]), b = min3(s[3], s[4], s[5]), c = min3(s[6], s[7], s[8]), d = min3(s[9], s[10], s[11]), e = min3(s[12], s[13], s[14]);
    return min3(min3(a, b, c), min3(d, e, s[15]), a);
}

template <int OFF = 0> DI void ld128_async_dev(u32x4& d, rsrc_t rs, unsigned voff, unsigned soff) {
    asm volatile("buffer_load_dwordx4 %0, %1, %2, %3 offen offset:%4 sc1" : "=v"(d) : "v"(voff), "s"(rs), "s"(soff), "n"(OFF) : "memory");
}
template <int OFF = 0> DI void ld128_async_acc(u32x4& d, rsrc_t rs, unsigned voff, unsigned soff) {
    asm volatile("buffer_load_dwordx4 %0, %1, %2, %3 offen offset:%4 sc1" : "=a"(d) : "v"(voff), "s"(rs), "s"(soff), "n"(OFF) : "memory");
}
template <bool ACC> DI void ld_frag(u32x4& d, rsrc_t rs, unsigned voff, unsigned base, int i) {
    const unsigned s = base + (i >> 2) * 4096;
    if (ACC) { if ((i & 3) == 0) ld128_async_acc(d, rs, voff, s); if ((i & 3) == 1) ld128_async_acc<1024>(d, rs, voff, s); if ((i & 3) == 2) ld128_async_acc<2048>(d, rs, voff, s); if ((i & 3) == 3) ld128_async_acc<3072>(d, rs, voff, s); }
    else     { if ((i & 3) == 0) ld128_async_dev(d, rs, voff, s); if ((i & 3) == 1) ld128_async_dev<1024>(d, rs, voff, s); if ((i & 3) == 2) ld128_async_dev<2048>(d, rs, voff, s); if ((i & 3) == 3) ld128_async_dev<3072>(d, rs, voff, s); }
}
template <int OFF = 0> DI void st128_async(u32x4 v, rsrc_t rs, unsigned voff, unsigned soff) {
    asm volatile("buffer_store_dwordx4 %0, %1, %2, %3 offen offset:%4 sc1\n\ts_nop 1" ::"v"(v), "v"(voff), "s"(rs), "s"(soff), "n"(OFF) : "memory");
}
template <int N> DI void landed(u32x4& a, u32x4& b, u32x4& c, u32x4& d) { asm volatile("s_waitcnt vmcnt(%4)" : "+v"(a), "+v"(b), "+v"(c), "+v"(d) : "n"(N) : "memory"); }
template <int N> DI void landed8(u32x4 (&r)[8]) {
    asm volatile("s_waitcnt vmcnt(%8)" : "+v"(r[0]), "+v"(r[1]), "+v"(r[2]), "+v"(r[3]), "+v"(r[4]), "+v"(r[5]), "+v"(r[6]), "+v"(r[7]) : "n"(N) : "memory");
}
DI void mfma_v(f32x16& acc, u32x4 a, u32x4 b) { asm("v_mfma_f32_32x32x16_bf16 %0, %1, %2, %0" : "+v"(acc) : "v"(a), "v"(b)); }
DI void mfma_v0(f32x16& acc, u32x4 a, u32x4 b) { asm("s_nop 1\n\tv_mfma_f32_32x32x16_bf16 %0, %1, %2, 0" : "=&v"(acc) : "v"(a), "v"(b)); }
DI void settle(f32x16& x) { asm volatile("s_nop 11" : "+v"(x)); }
template <int V> struct Int { static constexpr int value = V; };
template <int I, int N, class F> DI void static_for(F&& f) { if constexpr (I < N) { f(Int<I>{}); static_for<I + 1, N>(f); } }
DI void barrier() { asm volatile("s_barrier" ::: "memory"); }
DI int lane_id() { int l; asm volatile("v_mbcnt_lo_u32_b32 %0, -1, 0\n\tv_mbcnt_hi_u32_b32 %0, -1, %0" : "=v"(l)); return l; }

#define STAMP(id) do {} while (0)
#define TSTAMP(id) do {} while (0)

DI void epoch_request(u64& v, const char* p) { asm volatile("s_load_dwordx2 %0, %1, 0x0" : "=s"(v) : "s"(p) : "memory"); }
DI void publish(char* ws, unsigned word, u64 nonce) {
    asm volatile("s_mov_b64 exec, 1\n\tglobal_store_dwordx2 %0, %1, %2 sc1\n\ts_mov_b64 exec, -1" ::"v"(word), "v"(nonce), "s"(ws) : "memory");
}
DI void peek(u32x2& v, const char* ws, unsigned word) { asm volatile("global_load_dwordx2 %0, %1, %2 sc1" : "=v"(v) : "v"(word), "s"(ws) : "memory"); }
DI void peek(u32x4& v, const char* ws, unsigned word) { asm volatile("global_load_dwordx4 %0, %1, %2 sc1" : "=v"(v) : "v"(word), "s"(ws) : "memory"); }
DI bool differs(const u32x2& v, u64 n) { return __builtin_amdgcn_ballot_w64(__builtin_bit_cast(u64, v) != n) != 0; }
DI bool differs(const u32x4& v, u64 n) {
    return (__builtin_amdgcn_ballot_w64(__builtin_bit_cast(u64, u32x2{v[0], v[1]}) != n) | __builtin_amdgcn_ballot_w64(__builtin_bit_cast(u64, u32x2{v[2], v[3]}) != n)) != 0;
}
template <int N, class V> DI void seen(V& v, const char* ws, unsigned word, u64 nonce) {
    asm volatile("s_waitcnt vmcnt(%1)" : "+v"(v) : "n"(N) : "memory");
    if (__builtin_expect(differs(v, nonce), 0)) {
        unsigned spins = 0;
#pragma unroll 1
        do {
            __builtin_amdgcn_s_sleep(PSLEEP);
            peek(v, ws, word);
            asm volatile("s_waitcnt vmcnt(0)" : "+v"(v) :: "memory");
        } while (differs(v, nonce) && ++spins < SPIN_CAP);
    }
}

DI void mfma_pair(int isv, u32x4 xf, u32x4 w0, u32x4 w1, f32x16& acc0, f32x16& acc1) {
    asm("s_cmp_lg_u32 %5, 0\n\ts_cbranch_scc1 1f\n\t"
        "v_mfma_f32_32x32x16_bf16 %0, %3, %2, %0\n\tv_mfma_f32_32x32x16_bf16 %1, %4, %2, %1\n\ts_branch 2f\n"
        "1:\n\tv_mfma_f32_32x32x16_bf16 %0, %2, %3, %0\n\tv_mfma_f32_32x32x16_bf16 %1, %2, %4, %1\n2:"
        : "+v"(acc0), "+v"(acc1) : "a"(xf), "v"(w0), "v"(w1), "s"(isv) : "scc");
}
DI void mfma_wx(f32x16& acc, u32x4 w, u32x4 x) { asm("v_mfma_f32_32x32x16_bf16 %0, %1, %2, %0" : "+v"(acc) : "v"(w), "a"(x)); }
DI void mfma_xw(f32x16& acc, u32x4 x, u32x4 w) { asm("v_mfma_f32_32x32x16_bf16 %0, %1, %2, %0" : "+v"(acc) : "a"(x), "v"(w)); }
DI void mfma_wx0(f32x16& acc, u32x4 w, u32x4 x) { asm("v_mfma_f32_32x32x16_bf16 %0, %1, %2, 0" : "=&v"(acc) : "v"(w), "a"(x)); }
DI void mfma_xw0(f32x16& acc, u32x4 x, u32x4 w) { asm("v_mfma_f32_32x32x16_bf16 %0, %1, %2, 0" : "=&v"(acc) : "a"(x), "v"(w)); }
template <int OFF> DI void lds128_acc(u32x4& d, unsigned at) { asm volatile("ds_read_b128 %0, %1 offset:%2" : "=a"(d) : "v"(at), "n"(OFF) : "memory"); }
template <int N> DI void wait_vm_lds() { asm volatile("s_waitcnt vmcnt(%0) lgkmcnt(0)" ::"n"(N) : "memory"); }
DI void pin(u32x4& a, u32x4& b, u32x4& c, u32x4& d) { asm volatile("" : "+v"(a), "+v"(b), "+v"(c), "+v"(d) :: "memory"); }

DI void set_reference(u32x4& q1, float nm, int hi) {
    const unsigned a = __float_as_uint(nm) & 0xFFFF0000u;
    const float r1 = nm - __uint_as_float(a);
    const unsigned b = __float_as_uint(r1) & 0xFFFF0000u;
    const float r2 = r1 - __uint_as_float(b);
    q1[2] = hi ? (a >> 16) | b : q1[2]; q1[3] = hi ? __float_as_uint(r2) >> 16 : q1[3];
}

struct KV { u32x4 k0, k1, v0, v1; };

DI void ref_post(int wid, int lane, const float (&m)[4]) {
#pragma unroll
    for (int t = 0; t < 4; ++t) *(LAS float*)(size_t)(L_REF + ((wid + t) & 3) * 512 + (lane & 31) * 16 + wid * 4) = both_max(m[t]);
}
DI void ref_take(int wid, int lane, u32x4 (&q)[4][2], float offset) {
#pragma unroll
    for (int t = 0; t < 4; ++t) {
        const f32x4 m = __builtin_bit_cast(f32x4, lds128(L_REF + ((wid + t) & 3) * 512 + (lane & 31) * 16));
        set_reference(q[t][1], -(max3(max3(m[0], m[1], m[2]), m[3], m[3]) + offset), lane >> 5);
    }
}

DI void combine_post(int wid, int lane, const f32x16& o1, const f32x16& o2, const f32x16& o3) {
    const f32x16* ot[3] = {&o1, &o2, &o3};
#pragma unroll
    for (int t = 0; t < 3; ++t) {
        const unsigned at = L_COMB + (((wid + t + 1) & 3) * 3 + t) * 4096 + lane * 16;
#pragma unroll
        for (int g = 0; g < 4; ++g) sts128(at + g * 1024, quad(*ot[t], g));
    }
}
DI bool combine_take(int wid, int lane, f32x16 o, u32x4& f0, u32x4& f1) {
#pragma unroll
    for (int v = 0; v < 12; ++v) {
        const u32x4 p = lds128(L_COMB + wid * 12288 + v * 1024 + lane * 16);
#pragma unroll
        for (int i = 0; i < 4; ++i) o[4 * (v & 3) + i] += __uint_as_float(p[i]);
    }
    const float l = upper_val(o[12]);
    o *= __builtin_amdgcn_rcpf(l);
    float chk = 0.f;
#pragma unroll
    for (int i = 0; i < 16; ++i) chk = fmaf(o[i], 0.f, chk);
    f0 = pack_lo(o); f1 = pack_hi(o);
    if (lane >> 5) { f1[2] = 0; f1[3] = 0; }
    return !(l < L_LIMIT) || !(chk == 0.f);
}

DI void stage_out(unsigned img, int wid, int lane, const f32x16& r) {
    const u32x4 lo = pack_lo(r), up = pack_hi(r);
    const unsigned wr = img + (lane & 31) * 272 + wid * 64 + (lane >> 5) * 8;
    sts64(wr, u32x2{lo[0], lo[1]}); sts64(wr + 16, u32x2{lo[2], lo[3]}); sts64(wr + 32, u32x2{up[0], up[1]}); sts64(wr + 48, u32x2{up[2], up[3]});
}
DI void flush_out(unsigned img, int wid, int lane, int h, int qt, int br, u16* out) {
    const unsigned width = h >= 12 ? 128 : 256, colb = (lane & 15) * 16;
    const rsrc_t rd = make_rsrc((char*)out + (size_t)qt * 32 * (2 * XROW) + br * XROW + (h >> 2) * 256, 31 * 2 * XROW + width);
#pragma unroll
    for (int i = 0; i < 2; ++i) {
        const unsigned row = wid * 8 + i * 4 + (lane >> 4);
        st128_async(lds128(img + row * 272 + colb), rd, colb < width ? row * (2 * XROW) + colb : OOB, 0);
    }
}

__global__ __launch_bounds__(256, 1) void fused(const u16*, const u16*, const u16*, const u16*, const u16*, const u16*, const u16*, u16*, char*) {
    u64x8 args;
    u64 arg_ws;
    const auto arg_base = __builtin_amdgcn_kernarg_segment_ptr();
    asm volatile("s_load_dwordx16 %0, %2, 0x0\n\ts_load_dwordx2 %1, %2, 0x40" : "=&s"(args), "=&s"(arg_ws) : "s"(arg_base) : "memory");
    SBAR();
    STAMP(0);
    const int wid = __builtin_amdgcn_readfirstlane(threadIdx.x >> 6);
    const int blk = blockIdx.x, xcd = blk & 7, bi = blk >> 3;
    const int h = (xcd & 1) * 8 + (bi & 7), rb = (xcd >> 1) * 4 + (bi >> 3), tile = rb * 4 + wid;
    const int rot = ((bi >> 3) + (bi & 7)) % 3;
    const int rotb = (2 * (bi >> 3) + (bi & 7)) % 7;
    auto chunk_b = [&](int p) { const int v = p + rotb; return v >= 7 ? v - 7 : v; };
    auto chunk_at = [&](int p) { const int v = p % 3 + rot; return p == 6 ? 6 : p - p % 3 + (v >= 3 ? v - 3 : v); };
    u64 epoch, code_pc;
    unsigned code_probe;
    u64 nonce;
    const f32x16 zero = {};
    u32x4 q[4][2], eq[4];
    KV a, b, c, d;
    char* ws;
    rsrc_t rws;
    const unsigned kv_at = WS_KVA + (h * 64 + wid * 16) * 4096;
#define LDKV(X, j) do { const unsigned s_ = kv_at + (j) * 4096; ld128_async_dev<3072>(X.v1, rws, voff, s_); ld128_async_dev<2048>(X.v0, rws, voff, s_); \
                        ld128_async_dev<1024>(X.k1, rws, voff, s_); ld128_async_dev(X.k0, rws, voff, s_); } while (0)
    {
        const int lane = lane_id(), hi = lane >> 5, r32 = lane & 31, q4 = (lane & 15) >> 2;
        const unsigned voff = lane * 16, col = 32 * ((lane >> 4) & 1) + 8 * (lane & 3);
        const int isv = h & 1, row8 = lane >> 3;
        u32x4 xf[56];
        f32x16 acc0 = {}, acc1 = {};
        u32x2 pw;
        {
            const unsigned piece = (lane & 7) ^ (((row8 >> 1) & 1) << 2);
            unsigned vw0 = (16 * wid + row8) * WROW + (1 + isv) * XROW + (h >> 1) * 112 + ((piece < 7 ? piece : 6) << 4), vw1 = vw0 + 8 * WROW - 1024;
            unsigned vx[4];
#pragma unroll
            for (int i = 0; i < 4; ++i) vx[i] = (tile * 32 + row8) * XROW + i * (8 * XROW - 1024) + (((lane & 7) ^ (4 * (i & 1) + (lane >> 4))) << 4);
            int rot0 = rot;
            asm volatile("s_waitcnt lgkmcnt(0)" : "+s"(args), "+s"(arg_ws), "+v"(vw0), "+v"(vw1), "+v"(vx[0]), "+v"(vx[1]), "+v"(vx[2]), "+v"(vx[3]), "+s"(rot0) : "s"(arg_base) : "memory");
            const rsrc_t rxa = make_rsrc((const void*)args[0], X_BYTES), rwa = make_rsrc((const void*)args[1], W_BYTES);
            auto req_x = [&](rsrc_t rx, int slot, unsigned k, int i) {
                if (i == 0) dma16(rx, L_X + slot * 16384 + wid * 4096, vx[0], k * 128); else if (i == 1) dma_next<1024>(rx, vx[1], k * 128);
                else if (i == 2) dma_next<2048>(rx, vx[2], k * 128); else dma_next<3072>(rx, vx[3], k * 128);
            };
            auto req_w = [&](int ch, int i) {
                if (i == 0) dma16(rwa, L_WKV + ch * 8192 + wid * 2048, vw0, chunk_at(ch) * 64 * WROW); else dma_next<1024>(rwa, vw1, chunk_at(ch) * 64 * WROW);
            };
            auto issue = [&](int ch) {
                req_w(ch, 0); req_w(ch, 1);
#pragma unroll
                for (int i = 0; i < 4; ++i) req_x(rxa, ch & 3, chunk_at(ch), i);
            };
            issue(0);
            SBAR();
            ws = (char*)arg_ws;
            rws = make_rsrc(ws, WS_END);
            const rsrc_t rxb = make_rsrc((const void*)args[3], X_BYTES);
            epoch_request(epoch, ws + WS_EPOCH);
            {
                unsigned code_len;
                asm volatile("s_getpc_b64 %0\n.Lfused_code_begin:\n\ts_mov_b32 %1, .Lfused_code_end-.Lfused_code_begin" : "=s"(code_pc), "=s"(code_len));
                const unsigned at = (bi * 16 + wid * 4 + (threadIdx.x & 3)) * 128;
                asm volatile("global_load_dword %0, %1, %2" : "=v"(code_probe) : "v"(at < code_len ? at : 0), "s"(code_pc) : "memory");
            }
            int gw = blk * 4 + wid, uu = xcd * 28 + bi;
            asm volatile("" : "+s"(gw), "+s"(uu));
            const unsigned idle = gw < 896 ? 0u : OOB;
            const int uj = uu >> 2, uh = (uu & 3) * 4 + wid;
            {
                const int i = lane >> 2;
                const bool kvp = uj < 28;
                const int ks = kvp ? uj : 2 * (uj < 42 ? uj - 28 : uj - 42);
                const rsrc_t rr = make_rsrc((const void*)(uj < 42 ? args[4] : args[1]), W_BYTES);
                const unsigned v = (((i & 7) + 32 * (i >> 3)) * WROW + (lane & 3) * 16) | idle;
                const unsigned s0 = (64 * (ks >> 2) + 8 * (ks & 3)) * WROW + (kvp ? XROW : 0) + uh * 56;
                dma16(rr, L_RET + wid * 2048, v, s0); dma16(rr, L_RET + wid * 2048 + 1024, v, s0 + (kvp ? XROW : 8 * WROW));
            }
            issue(1);
            u64 ro_ptr = gw >= 448 ? args[6] : args[2];
            unsigned ro_at = (28 * ((gw & 31) >> 1) + 16 * (gw & 1)) * XROW + ((gw >= 448 ? gw - 448 : gw) >> 5) * 64;
            asm volatile("" : "+s"(ro_ptr), "+s"(ro_at));
            rsrc_t rro = make_rsrc((const void*)ro_ptr, WO_BYTES);
            asm volatile("" : "+s"(rro));
            unsigned rt_at = L_RET + wid * 2048 + (8 * hi + q4) * 64 + col, rt_v = voff | idle, rt_to = WS_WB + (uh * 56 + uj) * 2048;
            unsigned rt_word = gw < 896 ? WS_FW + (uh * 64 + uj) * 8 : WS_VOID + gw * 8;
            unsigned w0 = L_WKV + (32 * hi + q4) * 128 + (col ^ ((q4 >> 1) << 6)), w1 = L_WKV + (32 * hi + q4) * 128 + ((col + 56) ^ ((q4 >> 1) << 6));
            unsigned xo[4];
#pragma unroll
            for (int s = 0; s < 4; ++s) xo[s] = L_X + wid * 4096 + r32 * 128 + (((4 * hi + s) ^ ((r32 >> 1) & 7)) << 4);
            const unsigned pw_word = WS_FW + h * 512 + (lane < 56 ? lane : 55) * 8;
            asm volatile("" : "+s"(rt_word), "+s"(rt_to), "+v"(rt_at), "+v"(rt_v), "+v"(w0), "+v"(w1), "+v"(xo[0]), "+v"(xo[1]), "+v"(xo[2]), "+v"(xo[3]), "+v"(acc0), "+v"(acc1));
            asm volatile("s_waitcnt lgkmcnt(0)" : "+s"(epoch) :: "memory");
            nonce = epoch + 1;
            issue(2);
            STAMP(1);
            const unsigned wb_at = WS_WB + h * 114688;
            auto req_kvb = [&](int ch, int j) {
                const unsigned s = wb_at + chunk_b(ch) * 8192 + wid * 2048;
                if (j == 0) dma16_dev(rws, l_kvb(ch) + wid * 2048, voff, s); else dma_next_dev<1024>(rws, voff, s);
            };
            auto req_qb = [&](int ch) { dma16_dev(rws, L_QB + ch * 4096 + wid * 1024, voff, wb_at + 57344 + chunk_b(ch) * 4096 + wid * 1024); };
            auto req_wq = [&](int j) { dma16_dev(rws, L_QA + j * 4096 + wid * 1024, voff, wb_at + 86016 + chunk_at(j) * 4096 + wid * 1024); };
            auto xb_slot = [](int ch) { return ch == 0 || ch == 5 ? 3 : ch == 1 || ch == 6 ? X_SPARE : ch - 2; };
            unsigned xs[4];
#pragma unroll
            for (int s = 0; s < 4; ++s) xs[s] = xo[s] - 16384;
            auto pump = [&](int ch, int s) {
                const int slot = xb_slot(ch);
                if (slot == X_SPARE) lds128_acc<0>(xf[28 + 4 * ch + s], xs[s]); else if (slot == 0) lds128_acc<16384>(xf[28 + 4 * ch + s], xs[s]);
                else if (slot == 1) lds128_acc<32768>(xf[28 + 4 * ch + s], xs[s]); else if (slot == 2) lds128_acc<49152>(xf[28 + 4 * ch + s], xs[s]);
                else lds128_acc<49152>(xf[28 + 4 * ch + s], xo[s]);
            };
            u32x4 wf[2][8];
#pragma unroll
            for (int p = 0; p <= 7; ++p) {
                if (p < 7) {
                    if (p == 0 || p == 3) wait_vm<12>(); else if (p <= 2) wait_vm<14>(); else if (p == 4) wait_vm<13>(); else if (p == 5) wait_vm<15>(); else wait_vm<14>();
                    if (p == 3) publish(ws, rt_word, nonce);
                    barrier();
                    if (p == 0) asm volatile("" :: "v"(code_probe), "s"(code_pc));
                    if (p == 0) { STAMP(2); }
                    if (p == 6) { STAMP(3); }
                    if (p == 6) peek(pw, ws, pw_word);
                } else {
                    seen<4>(pw, ws, pw_word, nonce);
                    lds_bar();
                }
#pragma unroll
                for (int s = 0; s < 4; ++s) {
                    SBAR();
                    if (p > 0) mfma_pair(isv, xf[4 * p - 4 + s], wf[(p - 1) & 1][2 * s], wf[(p - 1) & 1][2 * s + 1], acc0, acc1);
                    SBAR();
                    if (p < 7) {
                        const unsigned o = p * 8192 + s * 1024;
                        if ((p & 3) == 0) lds128_acc<0>(xf[4 * p + s], xo[s]); else if ((p & 3) == 1) lds128_acc<16384>(xf[4 * p + s], xo[s]);
                        else if ((p & 3) == 2) lds128_acc<32768>(xf[4 * p + s], xo[s]); else lds128_acc<49152>(xf[4 * p + s], xo[s]);
                        wf[p & 1][2 * s] = tr_frag(w0 + o, w0 + o + 512); wf[p & 1][2 * s + 1] = tr_frag(w1 + o, w1 + o + 512);
                    }
                    if (p == 0 && s == 0) {
                        u32x4 g0 = tr_frag(rt_at, rt_at + 256), g1 = tr_frag(rt_at + 1024, rt_at + 1280);
                        if (r32 >= 28) { g0 = u32x4{}; g1 = u32x4{}; }
                        st128_async(g0, rws, rt_v, rt_to); st128_async<1024>(g1, rws, rt_v, rt_to);
                    }
                    if (p >= 1 && p <= 6 && s == 0) pin(wf[(p - 1) & 1][4], wf[(p - 1) & 1][5], wf[(p - 1) & 1][6], wf[(p - 1) & 1][7]);
                    if (p <= 3) {
                        if (s == 0) { req_w(p + 3, 0); req_w(p + 3, 1); }
                        if (s == 1) { req_x(rxa, (p + 3) & 3, chunk_at(p + 3), 0); req_x(rxa, (p + 3) & 3, chunk_at(p + 3), 1); }
                        if (s >= 2) req_x(rxa, (p + 3) & 3, chunk_at(p + 3), s);
                    }
                    if (p == 4) { req_x(rxb, xb_slot(s >> 1), chunk_b(s >> 1), 2 * (s & 1)); req_x(rxb, xb_slot(s >> 1), chunk_b(s >> 1), 2 * (s & 1) + 1); }
                    if (p == 5 || p == 6) req_x(rxb, xb_slot(p - 3), chunk_b(p - 3), s);
                    if (p == 5 && s == 3) {
                        const rsrc_t rp = make_rsrc((const char*)args[5] + h * 1568, 1568);
                        dma16(rp, L_WPHI, voff, 0); dma_next<1024>(rp, voff, 0);
                    }
                    if (p == 7) {
                        pump(0, s); pump(1, s);
                        if (s < 2) { req_kvb(s, 0); req_kvb(s, 1); }
                        if (s == 2) { req_kvb(2, 0); req_kvb(2, 1); req_x(rxb, xb_slot(4), chunk_b(4), 0); }
                        if (s == 3) { req_x(rxb, xb_slot(4), chunk_b(4), 1); req_x(rxb, xb_slot(4), chunk_b(4), 2); req_x(rxb, xb_slot(4), chunk_b(4), 3); }
                    }
                    SBAR();
                }
            }
            asm volatile("s_nop 3" : "+v"(acc0), "+v"(acc1));
            asm volatile("s_waitcnt lgkmcnt(0)" ::: "memory");
            u32x4 f[4];
#pragma unroll
            for (int i = 0; i < 4; ++i) {
                req_x(rxb, xb_slot(5 + (i >> 1)), chunk_b(5 + (i >> 1)), 2 * (i & 1)); req_x(rxb, xb_slot(5 + (i >> 1)), chunk_b(5 + (i >> 1)), 2 * (i & 1) + 1);
                f[i] = i == 0 ? pack_lo(acc0) : i == 1 ? pack_hi(acc0) : i == 2 ? pack_lo(acc1) : pack_hi(acc1);
                asm volatile("" : "+v"(f[i]));
                SBAR();
            }
            {
                const bool all = isv && r32 >= 28, tail = isv ? all : (bool)hi;
                const unsigned va = r32 == 28 ? 0x3F803F80u : 0u, v2 = isv ? va : 0x3F803F80u, v3 = isv ? va : 0x00003F80u;
#pragma unroll
                for (int i = 0; i < 4; ++i) {
                    f[i][0] = all ? va : f[i][0]; f[i][1] = all ? va : f[i][1];
                    if (i & 1) { f[i][2] = tail ? v2 : f[i][2]; f[i][3] = tail ? v3 : f[i][3]; }
                    else       { f[i][2] = all ? va : f[i][2]; f[i][3] = all ? va : f[i][3]; }
                }
                pin(f[0], f[1], f[2], f[3]);
            }
            const unsigned kv_to = WS_KVA + ((h & 14) * 64 + tile) * 4096 + isv * 2048;
            STAMP(4);
            if (wid == 0 && lane == 0) *(LAS unsigned*)(size_t)L_FLAG = 0;
            u32x2 pk_;
            const unsigned pk_word = WS_FK + ((h >> 1) * 64 + wid * 16) * 16 + r32 * 8;
            f32x16 ak, av;
            {
                u32x4 wg[28][2];
                static_for<0, 31>([&](auto nc) __attribute__((always_inline)) {
                    constexpr int n = decltype(nc)::value, i = n - 3, p = n >> 2, s = n & 3;
                    if constexpr (n < 28 && s == 0) {
                        if constexpr (p == 0 || p == 2) wait_vm<16>(); else if constexpr (p == 1) wait_vm<18>(); else if constexpr (p == 3) wait_vm<10>();
                        else if constexpr (p == 4) wait_vm<11>(); else if constexpr (p == 5) wait_vm<12>(); else wait_vm<14>();
                        barrier();
                        if constexpr (p == 5) { STAMP(5); }
                    }
                    auto request = [&](int r) __attribute__((always_inline)) {
                        if (p <= 1 && !(r & 1)) req_kvb(3 + 2 * p + (r >> 2), (r >> 1) & 1);
                        if (p == 2) {
                            if (r == 0) st128_async(f[0], rws, voff, kv_to);
                            if (r == 2) st128_async<1024>(f[1], rws, voff, kv_to);
                            if (r == 4) st128_async(f[2], rws, voff, kv_to + 64 * 4096);
                            if (r == 6) st128_async<1024>(f[3], rws, voff, kv_to + 64 * 4096);
                        }
                        if (p == 3 && r < 6 && !(r & 1)) req_qb(r >> 1);
                        if (p == 4 && r < 6 && !(r & 1)) req_wq(r >> 1);
                        if (p == 5 && !(r & 1)) { if (r & 2) req_wq(3 + (r >> 2)); else req_qb(3 + (r >> 2)); }
                    };
                    const unsigned at = l_kvb(p < 7 ? p : 0) + s * 2048 + voff;
                    SBAR();
                    if constexpr (i == 0) mfma_wx0(ak, wg[i][0], xf[28 + i]); else if constexpr (i > 0) mfma_wx(ak, wg[i][0], xf[28 + i]);
                    SBAR();
                    if constexpr (p <= 3) pump(p + 2, s);
                    if constexpr (p == 3) pump(6, s);
                    if constexpr (n < 28) wg[n][0] = lds128(at);
                    request(2 * s);
                    SBAR();
                    if constexpr (i == 0) mfma_xw0(av, xf[28 + i], wg[i][1]); else if constexpr (i > 0) mfma_xw(av, xf[28 + i], wg[i][1]);
                    SBAR();
                    if constexpr (n < 28) wg[n][1] = lds128(at + 1024);
                    request(2 * s + 1);
                    SBAR();
                });
            }
            STAMP(6);
            u32x4 wp[2];
            {
                const unsigned fc = 16 * ((lane >> 4) & 1) + 4 * (lane & 3);
                const unsigned a0 = L_WPHI + (4 * hi + q4) * 56 + (fc < 28 ? fc : 24) * 2;
                wp[0] = tr_frag(a0, a0 + 8 * 56); wp[1] = tr_frag(a0 + 16 * 56, a0 + (hi ? 20 : 24) * 56);
            }
            wait_vm<4>();
            publish(ws, WS_FK + (((h >> 1) * 64 + tile) * 2 + isv) * 8, nonce);
            barrier();
            u32x4 wq[28][2];
            unsigned qba = L_QB + voff;
            asm volatile("" : "+v"(qba));
#pragma unroll
            for (int i = 0; i < 4; ++i) { wq[i][0] = lds128(qba + (L_QA - L_QB) + i * 1024); wq[i][1] = lds128(qba + i * 1024); }
            asm volatile("s_nop 1" : "+v"(ak), "+v"(av));
            STAMP(7);
            f32x16 pkk, pqq, qa, qb;
            {
                u32x4 f0 = pack_lo(ak), f1 = pack_hi(ak);
                asm volatile("" : "+v"(f0), "+v"(f1));
                mfma_v0(pkk, wp[0], f0); mfma_v(pkk, wp[1], f1);
            }
#define QA(i) do { SBAR(); if (((i) & 1) == 0) pin(wq[i][0], wq[i][1], wq[(i) + 1][0], wq[(i) + 1][1]); \
                   if ((i) == 8) { wait_vm<6>(); barrier(); } \
                   if ((i) == 16) { wait_vm<0>(); barrier(); } \
                   if ((i) == 0) mfma_wx0(qa, wq[0][0], xf[0]); else mfma_wx(qa, wq[i][0], xf[i]); \
                   SBAR(); if ((i) + 4 < 28) wq[(i) + 4][0] = lds128(qba + (L_QA - L_QB) + ((i) + 4) * 1024); \
                   if ((i) == 0 || (i) == 2) req_qb(5 + (i) / 2); \
                   if ((i) == 1 || (i) == 3) req_wq(5 + (i) / 2); \
                   if ((i) == 4) { const int r_ = lane >> 2;     \
                       dma16(rro, L_PHI + wid * 4096 + 3072, (((gw & 1) && r_ >= 12 ? r_ - 4 : r_) * XROW + (lane & 3) * 16) | idle, ro_at); } \
                   SBAR(); } while (0)
#define QB(i) do { SBAR(); if ((i) == 0) mfma_wx0(qb, wq[0][1], xf[28]); else mfma_wx(qb, wq[i][1], xf[28 + (i)]); \
                   SBAR(); if ((i) + 4 < 28) wq[(i) + 4][1] = lds128(qba + ((i) + 4) * 1024); SBAR(); } while (0)
            QA(0);
            u32x4 v0 = pack_lo(av);
            QB(0);
            u32x4 v1 = pack_hi(av);
            QA(1);
            if (r32 == 28) { v0 = u32x4{0x3F803F80u, 0x3F803F80u, 0x3F803F80u, 0x3F803F80u}; v1 = v0; }
            asm volatile("" : "+v"(pkk), "+v"(v0), "+v"(v1));
            QB(1);
            const unsigned img = L_PHI + wid * 4096;
            {
                f32x16 ep, en;
                const float hx = max16(pkk);
                QA(2);
                const float hn = min16(pkk);
                QB(2);
                const float mx = both_max(hx) * LOG2E;
                QA(3);
                const float mn = both_min(hn) * LOG2E;
                QB(3);
                const float live = hi ? 0.f : 1.f;
                float sp = 0.f, sn = 0.f;
                float ap = fmaf(pkk[15], LOG2E, -mx), an = fmaf(pkk[15], -LOG2E, mn);
#pragma unroll
                for (int i = 0; i < 16; ++i) {
                    QA(4 + i);
                    ep[15 - i] = __builtin_amdgcn_exp2f(ap); en[15 - i] = __builtin_amdgcn_exp2f(an);
                    SBAR();
                    if (i < 15) ap = fmaf(pkk[14 - i], LOG2E, -mx);
                    QB(4 + i);
                    if (i < 15) an = fmaf(pkk[14 - i], -LOG2E, mn);
                    if (i == 1) { sp = ep[15]; sn = en[15]; }
                    if (i > 1) { sp += ep[16 - i]; sn += en[16 - i]; }
                    if (i == 4) { sp *= live; sn *= live; }
                    if (i == 13) {
                        const unsigned at = img + 3072 + (4 * hi + q4) * 64 + col;
                        st128_async(tr_frag(at, at + 512), rws, rt_v, WS_WO + gw * 1024);
                    }
                    }
                QA(20);
                const float tp = both_sum(sp + ep[0]);
                QB(20);
                const float rp = __builtin_amdgcn_rcpf(tp), rn = __builtin_amdgcn_rcpf(both_sum(sn + en[0]));
                const float rp3 = rp * live, rn3 = rn * live;
                const unsigned wr = img + (r32 & 15) * 136 + (r32 >> 4) * 16384 + hi * 8;
#pragma unroll
                for (int g = 0; g < 4; ++g) {
                    const float u = g == 3 ? rp3 : rp, v = g == 3 ? rn3 : rn;
                    QA(21 + 2 * g);
                    if (g == KP_PEEK) peek(pk_, ws, pk_word);
                    const unsigned e0 = pk(ep[4 * g] * u, ep[4 * g + 1] * u);
                    QB(21 + 2 * g);
                    sts64(wr + g * 16, u32x2{e0, pk(ep[4 * g + 2] * u, ep[4 * g + 3] * u)});
                    if (g < 3) QA(22 + 2 * g);
                    const unsigned e1 = pk(en[4 * g] * v, en[4 * g + 1] * v);
                    if (g < 3) QB(22 + 2 * g);
                    sts64(wr + 64 + g * 16, u32x2{e1, pk(en[4 * g + 2] * v, en[4 * g + 3] * v)});
                }
            }
#undef QA
#undef QB
            u32x4 pf[4];
            {
                const unsigned rd = img + (4 * hi + q4) * 136 + col;
#pragma unroll
                for (int i = 0; i < 4; ++i) pf[i] = tr_frag(rd + (i >> 1) * 64 + (i & 1) * 16384, rd + (i >> 1) * 64 + (i & 1) * 16384 + 8 * 136);
            }
            asm volatile("s_nop 1" : "+v"(qa), "+v"(qb));
            {
                u32x4 g0 = pack_lo(qb), g1 = pack_hi(qb);
                asm volatile("" : "+v"(g0), "+v"(g1));
                mfma_v0(pqq, wp[0], g0); mfma_v(pqq, wp[1], g1);
                SBAR();
                qa *= QSCALE;
                u32x4 q0 = pack_lo(qa), q1 = pack_hi(qa);
                sts128(L_QX + wid * 2048 + voff, q0); sts128(L_QX + wid * 2048 + 1024 + voff, q1);
            }
            f32x16 kv0, kv1, sc[4];
            SBAR(); mfma_v0(kv0, pf[0], v0); SBAR();
            asm volatile("" : "+v"(pqq));
            const float qhx = max16(pqq);
            SBAR(); mfma_v(kv0, pf[1], v1); SBAR();
            seen<0>(pk_, ws, pk_word, nonce);
            STAMP(8);
            LDKV(a, 0); LDKV(b, 1); LDKV(c, 2);
            lds_bar();
#pragma unroll
            for (int t = 0; t < 4; ++t) { const unsigned at = L_QX + ((wid + t) & 3) * 2048 + voff; q[t][0] = lds128(at); q[t][1] = lds128(at + 1024); }
            const float qhn = min16(pqq);
            SBAR(); mfma_v0(kv1, pf[2], v0); SBAR();
            const float qmx = both_max(qhx) * LOG2E, qmn = both_min(qhn) * LOG2E;
            SBAR(); mfma_v(kv1, pf[3], v1); SBAR();
            f32x16 eqp, eqn;
#pragma unroll
            for (int i = 0; i < 8; ++i) {
                const float a0 = fmaf(pqq[2 * i], LOG2E, -qmx), a1 = fmaf(pqq[2 * i + 1], LOG2E, -qmx), n0 = fmaf(pqq[2 * i], -LOG2E, qmn), n1 = fmaf(pqq[2 * i + 1], -LOG2E, qmn);
                SBAR();
                if (i == REF_AT) landed<8>(a.k0, a.k1, a.v0, a.v1);
                if (i >= REF_AT) { const int m = i - REF_AT; if (m & 1) mfma_v(sc[m >> 1], a.k1, q[m >> 1][1]); else mfma_v0(sc[m >> 1], a.k0, q[m >> 1][0]); }
                SBAR();
                eqp[2 * i] = __builtin_amdgcn_exp2f(a0); eqp[2 * i + 1] = __builtin_amdgcn_exp2f(a1); eqn[2 * i] = __builtin_amdgcn_exp2f(n0); eqn[2 * i + 1] = __builtin_amdgcn_exp2f(n1);
                if (i == 0) asm volatile("" : "+v"(kv0));
                if (i == 4) asm volatile("" : "+v"(kv1));
                sts128(L_RED + wid * 8192 + i * 1024 + voff, quad(i < 4 ? kv0 : kv1, i & 3));
                SBAR();
            }
#pragma unroll
            for (int i = 0; i < 4; ++i) {
#pragma unroll
                for (int m = 8 - REF_AT + i * REF_AT / 4; m < 8 - REF_AT + (i + 1) * REF_AT / 4; ++m) { if (m & 1) mfma_v(sc[m >> 1], a.k1, q[m >> 1][1]); else mfma_v0(sc[m >> 1], a.k0, q[m >> 1][0]); }
                SBAR();
                eq[i] = i == 0 ? pack_lo(eqp) : i == 1 ? pack_hi(eqp) : i == 2 ? pack_lo(eqn) : pack_hi(eqn);
                asm volatile("" : "+v"(eq[i]));
                SBAR();
            }
            asm volatile("s_nop 9" : "+v"(sc[0]), "+v"(sc[1]), "+v"(sc[2]), "+v"(sc[3]));
            {
                const float m[4] = {max16(sc[0]), max16(sc[1]), max16(sc[2]), max16(sc[3])};
                ref_post(wid, lane, m);
            }
            lds_bar();
            {
                ref_take(wid, lane, q, REF_OFFSET);
#pragma unroll
                for (int i = 0; i < 2; ++i) {
                    const unsigned off = (2 * wid + i) * 1024 + voff;
                    f32x4 u = __builtin_bit_cast(f32x4, lds128(L_RED + off));
#pragma unroll
                    for (int v = 1; v < 4; ++v) u += __builtin_bit_cast(f32x4, lds128(L_RED + v * 8192 + off));
                    st128_async(__builtin_bit_cast(u32x4, u), rws, off, WS_KVP + (h * 16 + rb) * 8192);
                }
                STAMP(9);
            }
        }
    }
    const int qt = rb * 4 + (h & 3), nt = (h & 12) + wid;
    const bool live = nt < 14;
    u32x4 wob_f[32], woa_f[32], f0, f1;
    const unsigned wo_at = WS_WO + (live ? nt : 0) * 32768;
    const unsigned o_mine = (tile * 16 + h) * 2048, o_need = (qt * 16 + wid * 4) * 2048;
    {
        const int lane = lane_id(), hi = lane >> 5, r32 = lane & 31;
        const unsigned voff = lane * 16;
        STAMP(10);
        f32x16 o0 = {}, o1 = {}, o2 = {}, o3 = {};
        f32x16 sa, sb; u32x4 pa, pb = {};
        float e12 = 0.f, e13 = 0.f, e14 = 0.f, e15 = 0.f;
        d.v1 = u32x4{};
#define QK0(S, K, Q) asm volatile("v_mfma_f32_32x32x16_bf16 %0, %1, %2, 0" : "=&v"(S) : "v"(K), "v"(Q))
#define QK1(S, K, Q) asm volatile("v_mfma_f32_32x32x16_bf16 %0, %1, %2, %0" : "+v"(S) : "v"(K), "v"(Q))
        asm volatile("s_nop 1" : "+v"(q[0][1]), "+v"(q[1][1]), "+v"(q[2][1]), "+v"(q[3][1]));
        QK0(sa, a.k0, q[0][0]); QK1(sa, a.k1, q[0][1]);
        asm volatile("s_nop 11" : "+v"(sa));
#define EX(S, i) S[i] = __builtin_amdgcn_exp2f(S[i])
#define STEP(NEXT, SC, SN, KN0, KN1, QN0, QN1, OP, VP1, OC, VC0) do { \
    if (NEXT) QK0(SN, KN0, QN0); SBAR(); \
    pb[2] = pk(e12, e13); SBAR(); EX(SC, 0); SBAR(); pb[3] = pk(e14, e15); SBAR(); EX(SC, 1); EX(SC, 2); EX(SC, 3); SBAR(); \
    OP = mfma(VP1, pb, OP); SBAR(); \
    EX(SC, 4); EX(SC, 5); SBAR(); pa[0] = pk(SC[0], SC[1]); SBAR(); EX(SC, 6); SBAR(); pa[1] = pk(SC[2], SC[3]); SBAR(); EX(SC, 7); SBAR(); \
    if (NEXT) QK1(SN, KN1, QN1); SBAR(); \
    EX(SC, 8); SBAR(); pa[2] = pk(SC[4], SC[5]); SBAR(); EX(SC, 9); SBAR(); pa[3] = pk(SC[6], SC[7]); SBAR(); EX(SC, 10); EX(SC, 11); SBAR(); \
    OC = mfma(VC0, pa, OC); SBAR(); \
    e12 = __builtin_amdgcn_exp2f(SC[12]); SBAR(); pb[0] = pk(SC[8], SC[9]); SBAR(); e13 = __builtin_amdgcn_exp2f(SC[13]); SBAR(); pb[1] = pk(SC[10], SC[11]); SBAR(); \
    e14 = __builtin_amdgcn_exp2f(SC[14]); e15 = __builtin_amdgcn_exp2f(SC[15]); SBAR(); } while (0)
#define TILE(J, CUR, PREV, NXT, NW, SEAM, MORE) do { \
    STEP(true, sa, sb, CUR.k0, CUR.k1, q[1][0], q[1][1], o3, PREV.v1, o0, CUR.v0); \
    if (J + 3 <= 15) LDKV(PREV, J + 3); \
    SEAM; SBAR(); \
    STEP(true, sb, sa, CUR.k0, CUR.k1, q[2][0], q[2][1], o0, CUR.v1, o1, CUR.v0); \
    MORE(0, 3); SBAR(); \
    STEP(true, sa, sb, CUR.k0, CUR.k1, q[3][0], q[3][1], o1, CUR.v1, o2, CUR.v0); \
    MORE(3, 6); \
    if (J < 15) landed<NW>(NXT.k0, NXT.k1, NXT.v0, NXT.v1); \
    SBAR(); \
    STEP(J < 15, sb, sa, NXT.k0, NXT.k1, q[0][0], q[0][1], o2, CUR.v1, o3, CUR.v0); \
    MORE(6, 8); SBAR(); } while (0)
        u32x4 pl[2], sf[4];
        u32x4 pp;
        u32x2 ps;
        const unsigned pp_word = lane < 8 ? WS_FP + (h * 4 + (rb >> 2)) * 128 + lane * 16 : WS_FWO + (((lane >> 4) & 1) * 14 + (live ? nt : 0)) * 256 + (lane & 15) * 16;
        const unsigned ps_word = WS_FS + h * 512 + lane * 8;
        auto slice_load = [&]() {
            const unsigned at = WS_KVP + h * 16 * 8192 + tile * 128, v = (lane >> 3) * 8192 + (lane & 7) * 16;
            ld128_async_dev(pl[0], rws, v, at); ld128_async_dev(pl[1], rws, v, at + 8 * 8192);
        };
        auto slice_sum = [&]() {
            asm volatile("s_waitcnt vmcnt(4)" : "+v"(pl[0]), "+v"(pl[1]) :: "memory");
            float u[4];
#pragma unroll
            for (int e = 0; e < 4; ++e) {
                u[e] = both_sum(__uint_as_float(pl[0][e]) + __uint_as_float(pl[1][e]));
                auto r = __builtin_amdgcn_permlane16_swap(__float_as_uint(u[e]), __float_as_uint(u[e]), false, false);
                u[e] = __uint_as_float(r[0]) + __uint_as_float(r[1]);
                u[e] += __uint_as_float(__builtin_amdgcn_update_dpp(0u, __float_as_uint(u[e]), 0x128, 0xf, 0xf, false));
            }
            const u32x2 v = {pk(u[0], u[1]), pk(u[2], u[3])};
            const unsigned at = WS_ST + h * 4096 + (rb >> 2) * 1024 + ((rb & 1) * 32 + wid * 8) * 16 + ((rb >> 1) & 1) * 8;
            asm volatile("buffer_store_dwordx2 %0, %1, %2, %3 offen sc1" ::"v"(v), "v"(lane < 8 ? voff : OOB), "s"(rws), "s"(at) : "memory");
        };
        auto state_load = [&]() {
#pragma unroll
            for (int i = 0; i < 4; ++i) ld_frag<false>(sf[i], rws, voff, WS_ST + h * 4096, i);
        };
        auto wo_load = [&](int n, int first, int last) {
#pragma unroll
            for (int i = 0; i < 8; ++i) if (i >= first && i < last) ld_frag<true>(wob_f[8 * n + i], rws, voff, wo_at + 14 * 32768, 8 * n + i);
        };
        auto wa_load = [&](int n, int first, int last) {
#pragma unroll
            for (int i = 0; i < 8; ++i) if (i >= first && i < last) {
                if (n == 3) ld_frag<true>(woa_f[8 * n + i], rws, voff, wo_at, 8 * n + i); else ld_frag<false>(woa_f[8 * n + i], rws, voff, wo_at, 8 * n + i);
            }
        };
        f32x16 np, nn;
        auto start_b = [&]() {
            landed<4>(sf[0], sf[1], sf[2], sf[3]);
            if (r32 == 29) {
                const unsigned tail = hi ? 0u : 0x3F803F80u;
                sf[0] = u32x4{0x3F803F80u, 0x3F803F80u, 0x3F803F80u, 0x3F803F80u}; sf[2] = sf[0];
                sf[1] = u32x4{0x3F803F80u, 0x3F803F80u, tail, tail}; sf[3] = sf[1];
            }
            asm volatile("" : "+v"(sf[0]), "+v"(sf[1]), "+v"(sf[2]), "+v"(sf[3]));
            mfma_v0(np, sf[0], eq[0]); mfma_v(np, sf[1], eq[1]); mfma_v0(nn, sf[2], eq[2]); mfma_v(nn, sf[3], eq[3]);
        };
        auto finish_b = [&](int first, int last) {
            if (first != 0) return;
            asm volatile("" : "+v"(np), "+v"(nn));
            const float rp_ = __builtin_amdgcn_rcpf(np[13]), rn_ = __builtin_amdgcn_rcpf(nn[13]);
            const float inv = __builtin_amdgcn_rcpf(fmaf(np[12], rp_, fmaf(nn[12], rn_, 1e-6f)));
            const float ap = upper_val(rp_ * inv), an = upper_val(rn_ * inv);
            f32x16 o;
#pragma unroll
            for (int i = 0; i < 16; ++i) o[i] = fmaf(np[i], ap, nn[i] * an);
            u32x4 g0 = pack_lo(o), g1 = pack_hi(o);
            if (hi) { g1[2] = 0; g1[3] = 0; }
            st128_async(g0, rws, voff, WS_OB + o_mine); st128_async<1024>(g1, rws, voff, WS_OB + o_mine);
        };
#define NONE(x, y) (void)0
#define WB0(x, y) wo_load(0, x, y)
#define WB1(x, y) wo_load(1, x, y)
#define WB2(x, y) wo_load(2, x, y)
#define WB3(x, y) wo_load(3, x, y)
#define WA0(x, y) wa_load(0, x, y)
#define WA1(x, y) wa_load(1, x, y)
#define WA2(x, y) wa_load(2, x, y)
#define WA3(x, y) wa_load(3, x, y)
        TILE(0, a, d, b, 10, (void)0, NONE);
        TILE(1, b, a, c, 12, (wait_vm<8>(), publish(ws, WS_FP + ((h * 4 + wid) * 16 + rb) * 8, nonce), publish(ws, (blk < 224 ? WS_FWO : WS_VOID) + (blk * 4 + wid) * 8, nonce)), NONE);
        TSTAMP(19); TILE(2, c, b, d, 10, (void)0, NONE);
        TILE(3, d, c, a, 10, (void)0, NONE);
        TILE(4, a, d, b, 9, peek(pp, ws, pp_word), NONE);
        TSTAMP(20); TILE(5, b, a, c, 11, (seen<4>(pp, ws, pp_word, nonce), slice_load()), NONE);
        TSTAMP(21); TILE(6, c, b, d, 18, slice_sum(), WB0);
        TSTAMP(22); TILE(7, d, c, a, 25, (void)0, WB1);
        TILE(8, a, d, b, 32, (wait_vm<24>(), publish(ws, WS_FS + (h * 64 + tile) * 8, nonce)), WB2);
        TILE(9, b, a, c, 31, (void)0, WB3);
        TILE(10, c, b, d, 32, peek(ps, ws, ps_word), WA3);
        TSTAMP(23); TILE(11, d, c, a, 29, (seen<12>(ps, ws, ps_word, nonce), state_load()), NONE);
        TSTAMP(24); TILE(12, a, d, b, 23, start_b(), finish_b);
        TSTAMP(25); TILE(13, b, a, c, 16, (void)0, WA0);
        TILE(14, c, b, d, 17, (wait_vm<8>(), publish(ws, WS_FOB + (tile * 16 + h) * 8, nonce)), WA1);
        TSTAMP(26); TILE(15, d, c, a, 0, (void)0, WA2);
        pb[2] = pk(e12, e13); pb[3] = pk(e14, e15);
        o3 = mfma(d.v1, pb, o3);
#undef TILE
#undef STEP
#undef EX
#undef QK0
#undef QK1
#undef NONE
#undef WB0
#undef WB1
#undef WB2
#undef WB3
#undef WA0
#undef WA1
#undef WA2
#undef WA3
        STAMP(11);
        combine_post(wid, lane, o1, o2, o3);
        u32x2 pob;
        const unsigned pob_word = WS_FOB + (qt * 16 + wid * 4 + (lane & 3)) * 8;
        peek(pob, ws, pob_word);
        lds_bar();
        const bool bad = combine_take(wid, lane, o0, f0, f1);
        st128_async(f0, rws, voff, WS_OA + o_mine); st128_async<1024>(f1, rws, voff, WS_OA + o_mine);
        STAMP(12);
        if (__any(bad) && lane == 0) *(LAS unsigned*)(size_t)L_FLAG = 1;
        seen<2>(pob, ws, pob_word, nonce);
#pragma unroll
        for (int i = 0; i < 8; ++i) dma_run_dev(rws, L_OIMG + wid * 8192, voff, WS_OB + o_need, i);
        wait_bar<8>();
        if (__builtin_amdgcn_readfirstlane(*(const LAS unsigned*)(size_t)L_FLAG)) {
            float ref[4];
#pragma unroll
            for (int t = 0; t < 4; ++t) { if (hi) { q[t][1][2] = 0; q[t][1][3] = 0; } ref[t] = -__builtin_inff(); }
#pragma unroll 1
            for (int j = 0; j < 16; ++j) {
                const u32x4 k0 = ld128_dev(rws, voff, kv_at + j * 4096), k1 = ld128_dev(rws, voff + 1024, kv_at + j * 4096);
#pragma unroll
                for (int t = 0; t < 4; ++t) { f32x16 s = mfma(k0, q[t][0], zero); s = mfma(k1, q[t][1], s); ref[t] = fmaxf(ref[t], max16(s)); }
            }
            ref_post(wid, lane, ref);
            lds_bar();
            ref_take(wid, lane, q, 0.f);
            lds_bar();
            f32x16 x[4] = {};
#pragma unroll 1
            for (int j = 0; j < 16; ++j) {
                const u32x4 k0 = ld128_dev(rws, voff, kv_at + j * 4096), k1 = ld128_dev(rws, voff + 1024, kv_at + j * 4096);
                const u32x4 v0 = ld128_dev(rws, voff + 2048, kv_at + j * 4096), v1 = ld128_dev(rws, voff + 3072, kv_at + j * 4096);
#pragma unroll
                for (int t = 0; t < 4; ++t) {
                    f32x16 s = mfma(k0, q[t][0], zero); s = mfma(k1, q[t][1], s);
#pragma unroll
                    for (int i = 0; i < 16; ++i) s[i] = __builtin_amdgcn_exp2f(s[i]);
                    x[t] = mfma(v0, pack_lo(s), x[t]); x[t] = mfma(v1, pack_hi(s), x[t]);
                }
            }
            combine_post(wid, lane, x[1], x[2], x[3]);
            lds_bar();
            combine_take(wid, lane, x[0], f0, f1);
            st128_async(f0, rws, voff, WS_OA + o_mine); st128_async<1024>(f1, rws, voff, WS_OA + o_mine);
            wait_vm<0>();
        }
        publish(ws, WS_FOA + (tile * 16 + h) * 8, nonce);
        STAMP(13);
    }
#undef LDKV
    {
        const int lane = lane_id();
        const unsigned voff = lane * 16;
#pragma unroll
        for (int i = 0; i < 32; i += 8)
            asm volatile("" : "+a"(wob_f[i]), "+a"(wob_f[i + 1]), "+a"(wob_f[i + 2]), "+a"(wob_f[i + 3]), "+a"(wob_f[i + 4]), "+a"(wob_f[i + 5]), "+a"(wob_f[i + 6]), "+a"(wob_f[i + 7]));
        f32x16 r = {};
        asm volatile("s_nop 1" : "+v"(r));
        u32x4 of[32];
        u32x2 poa;
        const unsigned poa_word = WS_FOA + (qt * 16 + wid * 4 + (lane & 3)) * 8;
        wait_vm<1>();
        barrier();
#pragma unroll
        for (int i = 0; i < 6; ++i) of[i] = lds128(L_OIMG + i * 1024 + voff);
#pragma unroll
        for (int i = 0; i < 32; ++i) {
            if (i + 6 < 32) of[i + 6] = lds128(L_OIMG + (i + 6) * 1024 + voff);
            SBAR();
            asm volatile("v_mfma_f32_32x32x16_bf16 %0, %1, %2, %0" : "+v"(r) : "a"(wob_f[i]), "v"(of[i]));
            if (i == PEEK_AT) peek(poa, ws, poa_word);
            if (i == SEEN_AT) { seen<0>(poa, ws, poa_word, nonce); STAMP(15); }
            if (i >= SEEN_AT && i < SEEN_AT + 8) dma_run_dev(rws, L_OIMG2 + wid * 8192, voff, WS_OA + o_need, i - SEEN_AT);
            SBAR();
        }
        settle(r);
        stage_out(L_OUT, wid, lane, r);
        STAMP(14);
        wait_bar<0>();
        if (blk == 0 && wid == 0) publish(ws, WS_EPOCH, nonce);
#pragma unroll
        for (int i = 0; i < 24; i += 8)
            asm volatile("" : "+v"(woa_f[i]), "+v"(woa_f[i + 1]), "+v"(woa_f[i + 2]), "+v"(woa_f[i + 3]), "+v"(woa_f[i + 4]), "+v"(woa_f[i + 5]), "+v"(woa_f[i + 6]), "+v"(woa_f[i + 7]));
        asm volatile("" : "+a"(woa_f[24]), "+a"(woa_f[25]), "+a"(woa_f[26]), "+a"(woa_f[27]), "+a"(woa_f[28]), "+a"(woa_f[29]), "+a"(woa_f[30]), "+a"(woa_f[31]));
        f32x16 s;
#pragma unroll
        for (int i = 0; i < 6; ++i) of[i] = lds128(L_OIMG2 + i * 1024 + voff);
#pragma unroll
        for (int i = 0; i < 32; ++i) {
            if (i + 6 < 32) of[i + 6] = lds128(L_OIMG2 + (i + 6) * 1024 + voff);
            SBAR();
            if (i == 0) mfma_v0(s, woa_f[0], of[0]); else if (i < 24) mfma_v(s, woa_f[i], of[i]);
            else asm volatile("v_mfma_f32_32x32x16_bf16 %0, %1, %2, %0" : "+v"(s) : "a"(woa_f[i]), "v"(of[i]));
            if (i == 4) flush_out(L_OUT, wid, lane, h, qt, 1, (u16*)args[7]);
            SBAR();
        }
        settle(s);
        STAMP(16);
        stage_out(L_OUT + 16384, wid, lane, s);
        lds_bar();
        asm volatile(".Lfused_code_end:");
        flush_out(L_OUT + 16384, wid, lane, h, qt, 0, (u16*)args[7]);
    }
}

}

extern "C" void kernel_launch(void* const* d_in, const int* in_sizes, int n_in, void* d_out, int out_size, void* d_ws, size_t ws_size, hipStream_t stream) {
    (void)hipFuncSetAttribute((const void*)fused, hipFuncAttributeMaxDynamicSharedMemorySize, LDS_BYTES);
    fused<<<256, 256, LDS_BYTES, stream>>>((const u16*)d_in[0], (const u16*)d_in[1], (const u16*)d_in[2], (const u16*)d_in[3], (const u16*)d_in[4], (const u16*)d_in[5],
                                           (const u16*)d_in[6], (u16*)d_out, (char*)d_ws);
}
```
